```python
import torch

def _softmax(x, axis=-1):
    return torch.softmax(x, dim=axis)
def _gelu(x):
    return torch.nn.functional.gelu(x, approximate="tanh")
def _silu(x):
    return torch.nn.functional.silu(x)
def _sigmoid(x):
    return torch.sigmoid(x)
def _layernorm(x, axis=-1, eps=1e-5):
    m = x.mean(dim=axis, keepdim=True)
    v = x.var(dim=axis, keepdim=True, unbiased=False)
    return (x - m) / torch.sqrt(v + eps)
def _scatter_add(tgt, idx, src, axis=0):
    return tgt.clone().index_add_(axis, idx, src)
def _segment_sum(src, seg, n):
    out = torch.zeros((n,) + src.shape[1:], dtype=src.dtype)
    return out.index_add_(0, seg, src)
def _pad(x, pw):
    flat = tuple(v for pair in reversed(pw) for v in pair)
    return torch.nn.functional.pad(x, flat)

kernel_name = 'sequential_preLN__spectral_gfnet__dense_activation_mish__prompt'

def setup_inputs():

    _w = (lambda a: (lambda t: t.to(torch.bfloat16) if t.is_floating_point() else t)(torch.as_tensor(a)))
    def _A_setup():
        B, S, D = 1, 2048, 1024
        Df = D // 2 + 1
        return {
            "x": _w((lambda *s: torch.randn(*s, dtype=torch.bfloat16) * 0.05)(B, S, D)),
            "w_r": _w((lambda *s: torch.randn(*s, dtype=torch.bfloat16) * 0.4)(S, Df) * 0.02),
            "w_i": _w((lambda *s: torch.randn(*s, dtype=torch.bfloat16) * 0.4)(S, Df) * 0.02),
        }
    _A_in = _A_setup()

    def _B_setup():
        B, S, D, F = 1, 2048, 1024, 3584
        return {
            "x": _w((lambda *s: torch.randn(*s, dtype=torch.bfloat16) * 0.05)(B, S, D)),
            "w1": _w((lambda *s: torch.randn(*s, dtype=torch.bfloat16) * 0.8)(D, F) * (D ** -0.5)),
            "b1": _w((lambda *s: torch.randn(*s, dtype=torch.bfloat16) * 0.2)(F)),
            "w2": _w((lambda *s: torch.randn(*s, dtype=torch.bfloat16) * 0.1)(F, D) * (F ** -0.5)),
        }
    _B_in = _B_setup()

    return {
        "A_x": _A_in["x"],
        "A_w_r": _A_in["w_r"],
        "A_w_i": _A_in["w_i"],
        "B_x": _B_in["x"],
        "B_w1": _B_in["w1"],
        "B_b1": _B_in["b1"],
        "B_w2": _B_in["w2"],
    }

def reference(A_x, A_w_r, A_w_i, B_x, B_w1, B_b1, B_w2):
    def _A_ref(x, w_r, w_i):
        B, S, D = x.shape
        X = (lambda x, axes=(-2,-1): torch.fft.rfft2(x.float(), dim=axes))(x, axes=(-2, -1))
        W = w_r + 1j * w_i
        X = X * W
        y = (lambda x, s, axes=(-2,-1): torch.fft.irfft2(x, s=s, dim=axes).to(torch.bfloat16))(X, (S, D), axes=(-2, -1))
        return y

    def _B_ref(x, w1, b1, w2):
        h = torch.einsum("bsd,df->bsf", x, w1) + b1
        sp = torch.log1p(torch.exp(-torch.abs(h))) + torch.where(h > 0, h, torch.zeros_like(h))
        act = h * torch.tanh(sp)
        return torch.einsum("bsf,fd->bsd", act, w2)

    x = A_x
    h = x + _A_ref(_layernorm(x, axis=-1), A_w_r, A_w_i)
    h2 = h + _B_ref(_layernorm(h, axis=-1), B_w1, B_b1, B_w2)
    return h2
```

```cpp
#include <hip/hip_runtime.h>
#include <hip/hip_bf16.h>
#include <stdint.h>

typedef unsigned short u16;
typedef __attribute__((ext_vector_type(8))) short bf16x8;
typedef __attribute__((ext_vector_type(4))) short bf16x4;
typedef __attribute__((ext_vector_type(4))) float f32x4;
typedef __attribute__((address_space(3))) bf16x4* lds_b4_ptr;

constexpr int S_ = 2048, D_ = 1024, F_ = 3584, DF_ = 513;

__device__ __forceinline__ float bf2f(u16 v) { return __uint_as_float(((uint32_t)v) << 16); }
__device__ __forceinline__ u16 f2bf(float f) { __bf16 b = (__bf16)f; return __builtin_bit_cast(u16, b); }
__device__ __forceinline__ float rbf(float f) { return bf2f(f2bf(f)); }

__device__ __forceinline__ float2 cmul(float2 a, float2 b) { return make_float2(a.x * b.x - a.y * b.y, a.x * b.y + a.y * b.x); }
__device__ __forceinline__ float2 cadd(float2 a, float2 b) { return make_float2(a.x + b.x, a.y + b.y); }
__device__ __forceinline__ float2 csub(float2 a, float2 b) { return make_float2(a.x - b.x, a.y - b.y); }
template <int DIR>
__device__ __forceinline__ float2 cexp2pi(float rev) {
  float c = __builtin_amdgcn_cosf(rev), s = __builtin_amdgcn_sinf(rev);
  return make_float2(c, DIR > 0 ? s : -s);
}
template <int DIR>
__device__ __forceinline__ float2 muli(float2 a) { return DIR > 0 ? make_float2(-a.y, a.x) : make_float2(a.y, -a.x); }

template <int DIR>
__device__ __forceinline__ void dft4(float2& x0, float2& x1, float2& x2, float2& x3) {
  float2 t0 = cadd(x0, x2), t1 = csub(x0, x2), t2 = cadd(x1, x3), t3 = muli<DIR>(csub(x1, x3));
  x0 = cadd(t0, t2); x2 = csub(t0, t2); x1 = cadd(t1, t3); x3 = csub(t1, t3);
}
template <int DIR>
__device__ __forceinline__ void dft8(float2 (&v)[8]) {
  float2 e0 = v[0], e1 = v[2], e2 = v[4], e3 = v[6];
  float2 o0 = v[1], o1 = v[3], o2 = v[5], o3 = v[7];
  dft4<DIR>(e0, e1, e2, e3);
  dft4<DIR>(o0, o1, o2, o3);
  const float r = 0.70710678118654752440f;
  float2 t1 = DIR > 0 ? make_float2((o1.x - o1.y) * r, (o1.x + o1.y) * r) : make_float2((o1.x + o1.y) * r, (o1.y - o1.x) * r);
  float2 t2 = muli<DIR>(o2);
  float2 t3 = DIR > 0 ? make_float2((-o3.x - o3.y) * r, (o3.x - o3.y) * r) : make_float2((o3.y - o3.x) * r, (-o3.x - o3.y) * r);
  v[0] = cadd(e0, o0); v[4] = csub(e0, o0);
  v[1] = cadd(e1, t1); v[5] = csub(e1, t1);
  v[2] = cadd(e2, t2); v[6] = csub(e2, t2);
  v[3] = cadd(e3, t3); v[7] = csub(e3, t3);
}
__device__ __forceinline__ void twiddle8(float2 (&v)[8], float2 w) {
  float2 w2 = cmul(w, w), w3 = cmul(w2, w), w4 = cmul(w2, w2), w5 = cmul(w4, w), w6 = cmul(w3, w3), w7 = cmul(w4, w3);
  v[1] = cmul(v[1], w); v[2] = cmul(v[2], w2); v[3] = cmul(v[3], w3); v[4] = cmul(v[4], w4);
  v[5] = cmul(v[5], w5); v[6] = cmul(v[6], w6); v[7] = cmul(v[7], w7);
}
__device__ __forceinline__ void twiddle8b(float2 (&v)[8], float2 b, float2 w) {
  float2 w2 = cmul(w, w), w4 = cmul(w2, w2);
  float2 b1 = cmul(b, w), b2 = cmul(b, w2), b3 = cmul(b1, w2), b4 = cmul(b, w4), b5 = cmul(b1, w4), b6 = cmul(b2, w4), b7 = cmul(b3, w4);
  v[0] = cmul(v[0], b); v[1] = cmul(v[1], b1); v[2] = cmul(v[2], b2); v[3] = cmul(v[3], b3);
  v[4] = cmul(v[4], b4); v[5] = cmul(v[5], b5); v[6] = cmul(v[6], b6); v[7] = cmul(v[7], b7);
}

__device__ __forceinline__ float wave_sum(float v) {
#pragma unroll
  for (int o = 32; o > 0; o >>= 1) v += __shfl_xor(v, o, 64);
  return v;
}

#define WAVE_SYNC() do { __builtin_amdgcn_fence(__ATOMIC_RELEASE, "wavefront"); __builtin_amdgcn_wave_barrier(); __builtin_amdgcn_fence(__ATOMIC_ACQUIRE, "wavefront"); } while (0)

template <int DIR>
__device__ __forceinline__ void fft512_wave(float2 (&v)[8], float2* ex, int lane) {
  const int hi = lane >> 3, lo = lane & 7;
  dft8<DIR>(v);
  twiddle8(v, cexp2pi<DIR>((float)hi * (1.0f / 64.0f)));
#pragma unroll
  for (int k0 = 0; k0 < 8; ++k0) ex[72 * k0 + lane] = v[k0];
  WAVE_SYNC();
#pragma unroll
  for (int b = 0; b < 8; ++b) v[b] = ex[72 * hi + 8 * b + lo];
  WAVE_SYNC();
  dft8<DIR>(v);
  twiddle8b(v, cexp2pi<DIR>((float)(lo * hi) * (1.0f / 512.0f)), cexp2pi<DIR>((float)lo * (1.0f / 64.0f)));
#pragma unroll
  for (int k1 = 0; k1 < 8; ++k1) ex[66 * lo + 8 * k1 + hi] = v[k1];
  WAVE_SYNC();
#pragma unroll
  for (int c = 0; c < 8; ++c) v[c] = ex[66 * c + lane];
  WAVE_SYNC();
  dft8<DIR>(v);
}

__device__ __forceinline__ void ln_bf16(float (&e)[16]) {
  float s = 0.f;
#pragma unroll
  for (int i = 0; i < 16; ++i) s += e[i];
  s = wave_sum(s);
  const float mean = s * (1.0f / 1024.0f);
  float q = 0.f;
#pragma unroll
  for (int i = 0; i < 16; ++i) { float d = e[i] - mean; q += d * d; }
  q = wave_sum(q);
  const float mb = rbf(mean);
  const float vb = rbf(q * (1.0f / 1024.0f));
  const float sd = rbf(sqrtf(rbf(vb + 1e-5f)));
#pragma unroll
  for (int i = 0; i < 16; ++i) e[i] = rbf(rbf(e[i] - mb) / sd);
}

__global__ __launch_bounds__(512) void k1_ln_rowfft(const u16* __restrict__ x, const u16* __restrict__ wr, const u16* __restrict__ wi,
                                                     float2* __restrict__ T, uint32_t* __restrict__ WT) {
  __shared__ float2 ex_all[8 * 576];
  __shared__ float2 st[512 * 9];
  const int tid = threadIdx.x, lane = tid & 63, w = tid >> 6;
  const int s0 = blockIdx.x * 8;
  const int row = s0 + w;
  {
    uint32_t p[8];
#pragma unroll
    for (int r = 0; r < 8; ++r) {
      const size_t o = (size_t)(s0 + r) * DF_ + tid;
      p[r] = (uint32_t)wr[o] | ((uint32_t)wi[o] << 16);
    }
    uint4* dst = reinterpret_cast<uint4*>(WT + (size_t)tid * S_ + s0);
    dst[0] = make_uint4(p[0], p[1], p[2], p[3]);
    dst[1] = make_uint4(p[4], p[5], p[6], p[7]);
    if (tid < 8) {
      const size_t o = (size_t)(s0 + tid) * DF_ + 512;
      WT[(size_t)512 * S_ + s0 + tid] = (uint32_t)wr[o] | ((uint32_t)wi[o] << 16);
    }
  }
  const uint32_t* xr = reinterpret_cast<const uint32_t*>(x + (size_t)row * D_);
  float e[16];
#pragma unroll
  for (int a = 0; a < 8; ++a) {
    uint32_t u = xr[lane + 64 * a];
    e[2 * a] = __uint_as_float(u << 16);
    e[2 * a + 1] = __uint_as_float(u & 0xffff0000u);
  }
  ln_bf16(e);
  float2 v[8];
#pragma unroll
  for (int a = 0; a < 8; ++a) v[a] = make_float2(e[2 * a], e[2 * a + 1]);
  float2* ex = ex_all + w * 576;
  fft512_wave<-1>(v, ex, lane);
#pragma unroll
  for (int j = 0; j < 8; ++j) ex[lane + 64 * j] = v[j];
  WAVE_SYNC();
#pragma unroll
  for (int j = 0; j < 8; ++j) {
    const int k = lane + 64 * j;
    float2 zp = ex[(512 - k) & 511];
    float2 z = v[j];
    float2 E = make_float2(0.5f * (z.x + zp.x), 0.5f * (z.y - zp.y));
    float2 O = make_float2(0.5f * (z.y + zp.y), 0.5f * (zp.x - z.x));
    float2 tw = cexp2pi<-1>((float)k * (1.0f / 1024.0f));
    float2 X = cadd(E, cmul(tw, O));
    if (k == 0) X = make_float2(z.x + z.y, z.x - z.y);
    st[k * 9 + w] = X;
  }
  __syncthreads();
#pragma unroll
  for (int it = 0; it < 4; ++it) {
    const int idx = it * 512 + tid;
    const int k = idx >> 2, part = idx & 3;
    float2 a = st[k * 9 + 2 * part], b = st[k * 9 + 2 * part + 1];
    reinterpret_cast<float4*>(T + (size_t)k * S_ + s0)[part] = make_float4(a.x, a.y, b.x, b.y);
  }
}

__device__ __forceinline__ float2 wunpack(uint32_t u) { return make_float2(__uint_as_float(u << 16), __uint_as_float(u & 0xffff0000u)); }

__global__ __launch_bounds__(256) void k2_colfft(float2* __restrict__ T, const uint32_t* __restrict__ WT) {
  __shared__ float2 ex[2304];
  const int t = threadIdx.x;
  const int col = blockIdx.x;
  float2* Tc = T + (size_t)col * S_;
  float2 v[8];
#pragma unroll
  for (int a = 0; a < 8; ++a) v[a] = Tc[t + 256 * a];
  uint32_t wv[8];
#pragma unroll
  for (int r = 0; r < 8; ++r) wv[r] = WT[(size_t)col * S_ + t + 256 * r];
  const int h5 = t >> 5, l5 = t & 31;
  const int m3 = (t >> 2) & 7, d2 = t & 3;
  dft8<-1>(v);
  twiddle8(v, cexp2pi<-1>((float)t * (1.0f / 2048.0f)));
#pragma unroll
  for (int k0 = 0; k0 < 8; ++k0) ex[256 * k0 + t] = v[k0];
  __syncthreads();
#pragma unroll
  for (int b = 0; b < 8; ++b) v[b] = ex[256 * h5 + 32 * b + l5];
  __syncthreads();
  dft8<-1>(v);
  twiddle8(v, cexp2pi<-1>((float)l5 * (1.0f / 256.0f)));
#pragma unroll
  for (int k1 = 0; k1 < 8; ++k1) ex[288 * h5 + 36 * k1 + l5] = v[k1];
  WAVE_SYNC();
#pragma unroll
  for (int c = 0; c < 8; ++c) v[c] = ex[288 * h5 + 36 * m3 + 4 * c + d2];
  __syncthreads();
  dft8<-1>(v);
  twiddle8(v, cexp2pi<-1>((float)d2 * (1.0f / 32.0f)));
#pragma unroll
  for (int k2 = 0; k2 < 8; ++k2) ex[(4 * k2 + d2) * 66 + 8 * m3 + h5] = v[k2];
  __syncthreads();
  const int dd = t >> 6, l6 = t & 63;
#pragma unroll
  for (int d = 0; d < 4; ++d) { v[d] = ex[(4 * dd + d) * 66 + l6]; v[4 + d] = ex[(4 * (dd + 4) + d) * 66 + l6]; }
  __syncthreads();
  dft4<-1>(v[0], v[1], v[2], v[3]);
  dft4<-1>(v[4], v[5], v[6], v[7]);
  if (col != 0) {
#pragma unroll
    for (int k3 = 0; k3 < 4; ++k3) {
      v[k3] = cmul(v[k3], wunpack(wv[2 * k3]));
      v[4 + k3] = cmul(v[4 + k3], wunpack(wv[2 * k3 + 1]));
    }
  } else {
#pragma unroll
    for (int k3 = 0; k3 < 4; ++k3) { ex[t + 512 * k3] = v[k3]; ex[t + 256 + 512 * k3] = v[4 + k3]; }
    __syncthreads();
#pragma unroll
    for (int r = 0; r < 8; ++r) {
      const int vi = (r & 1) * 4 + (r >> 1);
      const int k = t + 256 * r, kn = (2048 - k) & 2047;
      float2 p = v[vi], pn = ex[kn];
      float2 F0 = make_float2(0.5f * (p.x + pn.x), 0.5f * (p.y - pn.y));
      float2 F5 = make_float2(0.5f * (p.y + pn.y), 0.5f * (pn.x - p.x));
      float2 wa = wunpack(wv[r]), wan = wunpack(WT[kn]);
      float2 wb = wunpack(WT[(size_t)512 * S_ + k]), wbn = wunpack(WT[(size_t)512 * S_ + kn]);
      float2 W0 = make_float2(0.5f * (wa.x + wan.x), 0.5f * (wa.y - wan.y));
      float2 W5 = make_float2(0.5f * (wb.x + wbn.x), 0.5f * (wb.y - wbn.y));
      float2 H0 = cmul(F0, W0), H5 = cmul(F5, W5);
      v[vi] = make_float2(H0.x - H5.y, H0.y + H5.x);
    }
    __syncthreads();
  }
  dft4<1>(v[0], v[1], v[2], v[3]);
  dft4<1>(v[4], v[5], v[6], v[7]);
  {
    float2 wa = cexp2pi<1>((float)dd * (1.0f / 32.0f)), wb = cexp2pi<1>((float)(dd + 4) * (1.0f / 32.0f));
    float2 wa2 = cmul(wa, wa), wa3 = cmul(wa2, wa), wb2 = cmul(wb, wb), wb3 = cmul(wb2, wb);
    v[1] = cmul(v[1], wa); v[2] = cmul(v[2], wa2); v[3] = cmul(v[3], wa3);
    v[5] = cmul(v[5], wb); v[6] = cmul(v[6], wb2); v[7] = cmul(v[7], wb3);
  }
#pragma unroll
  for (int d = 0; d < 4; ++d) { ex[(4 * dd + d) * 66 + l6] = v[d]; ex[(4 * (dd + 4) + d) * 66 + l6] = v[4 + d]; }
  __syncthreads();
#pragma unroll
  for (int k2 = 0; k2 < 8; ++k2) v[k2] = ex[(4 * k2 + d2) * 66 + 8 * m3 + h5];
  __syncthreads();
  dft8<1>(v);
  twiddle8b(v, cexp2pi<1>((float)(m3 * d2) * (1.0f / 256.0f)), cexp2pi<1>((float)m3 * (1.0f / 64.0f)));
#pragma unroll
  for (int c = 0; c < 8; ++c) ex[288 * h5 + 36 * m3 + 4 * c + d2] = v[c];
  WAVE_SYNC();
#pragma unroll
  for (int k1 = 0; k1 < 8; ++k1) v[k1] = ex[288 * h5 + 36 * k1 + l5];
  __syncthreads();
  dft8<1>(v);
  twiddle8b(v, cexp2pi<1>((float)(h5 * l5) * (1.0f / 2048.0f)), cexp2pi<1>((float)h5 * (1.0f / 64.0f)));
#pragma unroll
  for (int b = 0; b < 8; ++b) ex[256 * h5 + 32 * b + l5] = v[b];
  __syncthreads();
#pragma unroll
  for (int k0 = 0; k0 < 8; ++k0) v[k0] = ex[256 * k0 + t];
  dft8<1>(v);
#pragma unroll
  for (int a = 0; a < 8; ++a) Tc[t + 256 * a] = v[a];
}

__global__ __launch_bounds__(512) void k3_irowfft_ln(const float2* __restrict__ T, const u16* __restrict__ x,
                                                      u16* __restrict__ h, u16* __restrict__ a_out) {
  __shared__ float2 ex_all[8 * 576];
  __shared__ float2 st[512 * 9];
  const int tid = threadIdx.x, lane = tid & 63, w = tid >> 6;
  const int s0 = blockIdx.x * 8;
  const int row = s0 + w;
#pragma unroll
  for (int it = 0; it < 4; ++it) {
    const int idx = it * 512 + tid;
    const int k = idx >> 2, part = idx & 3;
    float4 q = reinterpret_cast<const float4*>(T + (size_t)k * S_ + s0)[part];
    st[k * 9 + 2 * part] = make_float2(q.x, q.y);
    st[k * 9 + 2 * part + 1] = make_float2(q.z, q.w);
  }
  const uint32_t* xr = reinterpret_cast<const uint32_t*>(x + (size_t)row * D_);
  uint32_t xv[8];
#pragma unroll
  for (int a = 0; a < 8; ++a) xv[a] = xr[lane + 64 * a];
  __syncthreads();
  float2 v[8];
#pragma unroll
  for (int j = 0; j < 8; ++j) {
    const int k = lane + 64 * j;
    float2 g = st[k * 9 + w], gp = st[((512 - k) & 511) * 9 + w];
    float2 E = make_float2(g.x + gp.x, g.y - gp.y);
    float2 O = make_float2(g.x - gp.x, g.y + gp.y);
    float2 tw = cexp2pi<1>((float)k * (1.0f / 1024.0f));
    float2 P = cmul(tw, O);
    float2 Z = make_float2(E.x - P.y, E.y + P.x);
    if (k == 0) Z = make_float2(g.x + g.y, g.x - g.y);
    v[j] = Z;
  }
  float2* ex = ex_all + w * 576;
  fft512_wave<1>(v, ex, lane);
  const float scale = 1.0f / (2048.0f * 1024.0f);
  float e[16];
  uint32_t* hr = reinterpret_cast<uint32_t*>(h + (size_t)row * D_);
#pragma unroll
  for (int j = 0; j < 8; ++j) {
    float y0 = rbf(v[j].x * scale), y1 = rbf(v[j].y * scale);
    float h0 = rbf(__uint_as_float(xv[j] << 16) + y0), h1 = rbf(__uint_as_float(xv[j] & 0xffff0000u) + y1);
    e[2 * j] = h0; e[2 * j + 1] = h1;
    hr[lane + 64 * j] = (__float_as_uint(h0) >> 16) | (__float_as_uint(h1) & 0xffff0000u);
  }
  ln_bf16(e);
  uint32_t* ar = reinterpret_cast<uint32_t*>(a_out + (size_t)row * D_);
#pragma unroll
  for (int j = 0; j < 8; ++j) ar[lane + 64 * j] = (__float_as_uint(e[2 * j]) >> 16) | (__float_as_uint(e[2 * j + 1]) & 0xffff0000u);
}

#define WAIT_V(n) asm volatile("s_waitcnt vmcnt(" #n ")" ::: "memory")
#define WAIT_L0() asm volatile("s_waitcnt lgkmcnt(0)" ::: "memory")
#define RAW_BAR() __builtin_amdgcn_s_barrier()

__device__ __forceinline__ float mish_ref(float c, float b) {
  const float hh = rbf(rbf(c) + b);
  const float t = __expf(fminf(hh, 30.0f));
  const float n = t * (t + 2.0f);
  return hh * n * __builtin_amdgcn_rcpf(n + 2.0f);
}

constexpr int G1_A_BYTES = 128 * 128, G1_B_BYTES = 64 * 448, G1_STAGE = G1_A_BYTES + G1_B_BYTES;
__global__ __launch_bounds__(256, 1) void g1_gemm(const u16* __restrict__ A, const u16* __restrict__ W, const u16* __restrict__ b1,
                                                   u16* __restrict__ act) {
  __shared__ __attribute__((aligned(16))) char smem[3 * G1_STAGE];
  const int tid = threadIdx.x, lane = tid & 63, w = __builtin_amdgcn_readfirstlane(tid >> 6);
  const int wm = w >> 1, wn = w & 1;
  const int bid = blockIdx.x, xcd = bid & 7, idx = bid >> 3;
  const int tm = (xcd & 1) * 8 + (idx & 7), tn = (xcd >> 1) * 4 + (idx >> 3);
  const int m0 = tm * 128, n0 = tn * 224;
  uint32_t aoff[4], boff[7];
#pragma unroll
  for (int i = 0; i < 4; ++i) {
    const int r = w * 32 + i * 8 + (lane >> 3);
    aoff[i] = (uint32_t)(m0 + r) * 2048u + (uint32_t)(((lane & 7) ^ (r & 7)) << 4);
  }
#pragma unroll
  for (int i = 0; i < 7; ++i) {
    const int d = i * 64 + lane, r = d / 28, q = d - r * 28, k = w * 16 + r;
    const int c = (q >> 1) ^ ((k >> 3) & 1);
    boff[i] = (uint32_t)k * (uint32_t)(F_ * 2) + (uint32_t)(n0 * 2) + (uint32_t)(((c << 1) | (q & 1)) << 4);
  }
  const char* Ab = reinterpret_cast<const char*>(A);
  const char* Wb = reinterpret_cast<const char*>(W);
  auto issue = [&](int kt, int buf) {
    char* sa = smem + buf * G1_STAGE + w * 4096 + lane * 16;
    char* sb = smem + buf * G1_STAGE + G1_A_BYTES + w * 7168 + lane * 16;
    const char* ga = Ab + kt * 128;
    const char* gb = Wb + (size_t)kt * (64 * F_ * 2);
#pragma unroll
    for (int i = 0; i < 4; ++i)
      __builtin_amdgcn_global_load_lds((const unsigned*)(ga + aoff[i]), (unsigned*)(sa + i * 1024), 16, 0, 0);
#pragma unroll
    for (int i = 0; i < 7; ++i)
      __builtin_amdgcn_global_load_lds((const unsigned*)(gb + boff[i]), (unsigned*)(sb + i * 1024), 16, 0, 0);
  };
  f32x4 acc[4][7];
#pragma unroll
  for (int i = 0; i < 4; ++i)
#pragma unroll
    for (int j = 0; j < 7; ++j) acc[i][j] = (f32x4){0.f, 0.f, 0.f, 0.f};
  const int fr = lane & 15, fg = lane >> 4;
  uint32_t ard[4];
#pragma unroll
  for (int mi = 0; mi < 4; ++mi) { const int r = wm * 64 + mi * 16 + fr; ard[mi] = r * 128 + ((fg ^ (r & 7)) << 4); }
  const int q4 = (lane & 15) >> 2, p4 = lane & 3;
  const uint32_t brd = G1_A_BYTES + (8 * fg + q4) * 448 + 8 * p4;
  bf16x8 xa[4], xb[7], ya[4], yb[7];
  auto ldfrag = [&](bf16x8 (&fa)[4], bf16x8 (&fb)[7], int buf, int ks) {
    const char* base = smem + buf * G1_STAGE;
#pragma unroll
    for (int mi = 0; mi < 4; ++mi) fa[mi] = *reinterpret_cast<const bf16x8*>(base + (ard[mi] ^ (ks * 64)));
#pragma unroll
    for (int ni = 0; ni < 7; ++ni) {
      const int c = (wn * 7 + ni) ^ (fg & 1);
      const char* p = base + brd + ks * (32 * 448) + c * 32;
      bf16x4 lo = __builtin_amdgcn_ds_read_tr16_b64_v4i16((lds_b4_ptr)(p));
      bf16x4 hi = __builtin_amdgcn_ds_read_tr16_b64_v4i16((lds_b4_ptr)(p + 4 * 448));
      fb[ni] = __builtin_shufflevector(lo, hi, 0, 1, 2, 3, 4, 5, 6, 7);
    }
  };
  auto mma = [&](bf16x8 (&fa)[4], bf16x8 (&fb)[7]) {
#pragma unroll
    for (int mi = 0; mi < 4; ++mi)
#pragma unroll
      for (int ni = 0; ni < 7; ++ni)
        acc[mi][ni] = __builtin_amdgcn_mfma_f32_16x16x32_bf16(fb[ni], fa[mi], acc[mi][ni], 0, 0, 0);
  };
  constexpr int NK = 16;
  issue(0, 0); issue(1, 1); issue(2, 2);
  WAIT_V(22); RAW_BAR();
  ldfrag(xa, xb, 0, 0);
  int buf = 0;
  for (int kt = 0; kt < NK; ++kt) {
    ldfrag(ya, yb, buf, 1);
    mma(xa, xb);
    const int nbuf = buf == 2 ? 0 : buf + 1;
    if (kt + 1 < NK) {
      if (kt + 2 < NK) { WAIT_V(11); } else { WAIT_V(0); }
      WAIT_L0();
      RAW_BAR();
      if (kt + 3 < NK) issue(kt + 3, buf);
      ldfrag(xa, xb, nbuf, 0);
    }
    mma(ya, yb);
    buf = nbuf;
  }
#pragma unroll
  for (int ni = 0; ni < 7; ++ni) {
    const int n = n0 + wn * 112 + ni * 16 + 4 * fg;
    const uint2 bb = *reinterpret_cast<const uint2*>(b1 + n);
    const float b0 = __uint_as_float(bb.x << 16), b1f = __uint_as_float(bb.x & 0xffff0000u);
    const float b2 = __uint_as_float(bb.y << 16), b3 = __uint_as_float(bb.y & 0xffff0000u);
#pragma unroll
    for (int mi = 0; mi < 4; ++mi) {
      const int m = m0 + wm * 64 + mi * 16 + fr;
      const f32x4 c = acc[mi][ni];
      const uint32_t o0 = (uint32_t)f2bf(mish_ref(c[0], b0)) | ((uint32_t)f2bf(mish_ref(c[1], b1f)) << 16);
      const uint32_t o1 = (uint32_t)f2bf(mish_ref(c[2], b2)) | ((uint32_t)f2bf(mish_ref(c[3], b3)) << 16);
      *reinterpret_cast<uint2*>(act + (size_t)m * F_ + n) = make_uint2(o0, o1);
    }
  }
}

constexpr int G2_A_BYTES = 2 * 128 * 128, G2_B_BYTES = 128 * 128, G2_STAGE = G2_A_BYTES + G2_B_BYTES;
__global__ __launch_bounds__(256, 1) void g2_gemm(const u16* __restrict__ A, const u16* __restrict__ W, const u16* __restrict__ h,
                                                   u16* __restrict__ out) {
  __shared__ __attribute__((aligned(16))) char smem[3 * G2_STAGE];
  const int tid = threadIdx.x, lane = tid & 63, w = __builtin_amdgcn_readfirstlane(tid >> 6);
  const int wm = w >> 1, wk = w & 1;
  const int bid = blockIdx.x, xcd = bid & 7, idx = bid >> 3;
  const int tm = (xcd & 3) * 4 + (idx & 3), tn = (xcd >> 2) * 8 + (idx >> 2);
  const int m0 = tm * 128, n0 = tn * 64;
  uint32_t aoff[4], boff[4];
#pragma unroll
  for (int i = 0; i < 4; ++i) {
    const int r = w * 32 + i * 8 + (lane >> 3);
    aoff[i] = (uint32_t)(m0 + r) * (uint32_t)(F_ * 2) + (uint32_t)(((lane & 7) ^ (r & 7)) << 4);
  }
#pragma unroll
  for (int i = 0; i < 4; ++i) {
    const int k = w * 32 + i * 8 + (lane >> 3), q = lane & 7;
    const int c = (q >> 1) ^ ((k >> 1) & 1) ^ (((k >> 3) & 1) << 1);
    boff[i] = (uint32_t)k * (uint32_t)(D_ * 2) + (uint32_t)(n0 * 2) + (uint32_t)(((c << 1) | (q & 1)) << 4);
  }
  const char* Ab = reinterpret_cast<const char*>(A);
  const char* Wb = reinterpret_cast<const char*>(W);
  auto issue = [&](int kt, int buf) {
    char* sa = smem + buf * G2_STAGE + w * 4096 + lane * 16;
    char* sb = smem + buf * G2_STAGE + G2_A_BYTES + w * 4096 + lane * 16;
    const char* ga = Ab + kt * 256;
    const char* gb = Wb + (size_t)kt * (128 * D_ * 2);
#pragma unroll
    for (int i = 0; i < 4; ++i)
      __builtin_amdgcn_global_load_lds((const unsigned*)(ga + aoff[i]), (unsigned*)(sa + i * 1024), 16, 0, 0);
#pragma unroll
    for (int i = 0; i < 4; ++i)
      __builtin_amdgcn_global_load_lds((const unsigned*)(ga + 128 + aoff[i]), (unsigned*)(sa + 16384 + i * 1024), 16, 0, 0);
#pragma unroll
    for (int i = 0; i < 4; ++i)
      __builtin_amdgcn_global_load_lds((const unsigned*)(gb + boff[i]), (unsigned*)(sb + i * 1024), 16, 0, 0);
  };
  f32x4 acc[4][4];
#pragma unroll
  for (int i = 0; i < 4; ++i)
#pragma unroll
    for (int j = 0; j < 4; ++j) acc[i][j] = (f32x4){0.f, 0.f, 0.f, 0.f};
  const int fr = lane & 15, fg = lane >> 4;
  uint32_t ard[4];
#pragma unroll
  for (int mi = 0; mi < 4; ++mi) { const int r = wm * 64 + mi * 16 + fr; ard[mi] = wk * 16384 + r * 128 + ((fg ^ (r & 7)) << 4); }
  const int q4 = (lane & 15) >> 2, p4 = lane & 3;
  const uint32_t brd = G2_A_BYTES + (wk * 64 + 8 * fg + q4) * 128 + 8 * p4;
  const int bsw = ((q4 >> 1) & 1) ^ ((fg & 1) << 1);
  bf16x8 xa[4], xb[4], ya[4], yb[4];
  auto ldfrag = [&](bf16x8 (&fa)[4], bf16x8 (&fb)[4], int buf, int ks) {
    const char* base = smem + buf * G2_STAGE;
#pragma unroll
    for (int mi = 0; mi < 4; ++mi) fa[mi] = *reinterpret_cast<const bf16x8*>(base + (ard[mi] ^ (ks * 64)));
#pragma unroll
    for (int ni = 0; ni < 4; ++ni) {
      const char* p = base + brd + ks * (32 * 128) + ((ni ^ bsw) * 32);
      bf16x4 lo = __builtin_amdgcn_ds_read_tr16_b64_v4i16((lds_b4_ptr)(p));
      bf16x4 hi = __builtin_amdgcn_ds_read_tr16_b64_v4i16((lds_b4_ptr)(p + 4 * 128));
      fb[ni] = __builtin_shufflevector(lo, hi, 0, 1, 2, 3, 4, 5, 6, 7);
    }
  };
  auto mma = [&](bf16x8 (&fa)[4], bf16x8 (&fb)[4]) {
#pragma unroll
    for (int mi = 0; mi < 4; ++mi)
#pragma unroll
      for (int ni = 0; ni < 4; ++ni)
        acc[mi][ni] = __builtin_amdgcn_mfma_f32_16x16x32_bf16(fb[ni], fa[mi], acc[mi][ni], 0, 0, 0);
  };
  constexpr int NK = 28;
  issue(0, 0); issue(1, 1); issue(2, 2);
  WAIT_V(24); RAW_BAR();
  ldfrag(xa, xb, 0, 0);
  int buf = 0;
  for (int kt = 0; kt < NK; ++kt) {
    ldfrag(ya, yb, buf, 1);
    mma(xa, xb);
    const int nbuf = buf == 2 ? 0 : buf + 1;
    if (kt + 1 < NK) {
      if (kt + 2 < NK) { WAIT_V(12); } else { WAIT_V(0); }
      WAIT_L0();
      RAW_BAR();
      if (kt + 3 < NK) issue(kt + 3, buf);
      ldfrag(xa, xb, nbuf, 0);
    }
    mma(ya, yb);
    buf = nbuf;
  }
  WAIT_L0();
  RAW_BAR();
  f32x4* red = reinterpret_cast<f32x4*>(smem) + wm * 1024;
  if (wk == 1) {
#pragma unroll
    for (int mi = 0; mi < 4; ++mi)
#pragma unroll
      for (int ni = 0; ni < 4; ++ni) red[(mi * 4 + ni) * 64 + lane] = acc[mi][ni];
  }
  __syncthreads();
  if (wk == 0) {
#pragma unroll
    for (int mi = 0; mi < 4; ++mi) {
      const int m = m0 + wm * 64 + mi * 16 + fr;
#pragma unroll
      for (int ni = 0; ni < 4; ++ni) {
        const int n = n0 + ni * 16 + 4 * fg;
        const f32x4 o = red[(mi * 4 + ni) * 64 + lane];
        const f32x4 c = acc[mi][ni] + o;
        const uint2 hv = *reinterpret_cast<const uint2*>(h + (size_t)m * D_ + n);
        const float r0 = __uint_as_float(hv.x << 16) + rbf(c[0]), r1 = __uint_as_float(hv.x & 0xffff0000u) + rbf(c[1]);
        const float r2 = __uint_as_float(hv.y << 16) + rbf(c[2]), r3 = __uint_as_float(hv.y & 0xffff0000u) + rbf(c[3]);
        const uint32_t o0 = (uint32_t)f2bf(r0) | ((uint32_t)f2bf(r1) << 16);
        const uint32_t o1 = (uint32_t)f2bf(r2) | ((uint32_t)f2bf(r3) << 16);
        *reinterpret_cast<uint2*>(out + (size_t)m * D_ + n) = make_uint2(o0, o1);
      }
    }
  }
}

extern "C" void kernel_launch(void* const* d_in, const int* in_sizes, int n_in,
                              void* d_out, int out_size, void* d_ws, size_t ws_size,
                              hipStream_t stream) {
  const u16* x = (const u16*)d_in[0];
  const u16* wr = (const u16*)d_in[1];
  const u16* wi = (const u16*)d_in[2];
  const u16* w1 = (const u16*)d_in[4];
  const u16* b1 = (const u16*)d_in[5];
  const u16* w2 = (const u16*)d_in[6];
  u16* out = (u16*)d_out;
  char* ws = (char*)d_ws;
  float2* T = (float2*)ws;
  uint32_t* WT = (uint32_t*)(ws + (8u << 20));
  u16* h = (u16*)(ws + (16u << 20));
  u16* a = (u16*)(ws + (20u << 20));
  u16* act = (u16*)(ws + (24u << 20));
  k1_ln_rowfft<<<256, 512, 0, stream>>>(x, wr, wi, T, WT);
  k2_colfft<<<512, 256, 0, stream>>>(T, WT);
  k3_irowfft_ln<<<256, 512, 0, stream>>>(T, x, h, a);
  g1_gemm<<<256, 256, 0, stream>>>(a, w1, b1, act);
  g2_gemm<<<256, 256, 0, stream>>>(act, w2, h, out);
}
```

```cpp
#include <hip/hip_runtime.h>
#include <hip/hip_bf16.h>
#include <stdint.h>

typedef unsigned short u16;
typedef __attribute__((ext_vector_type(8))) short bf16x8;
typedef __attribute__((ext_vector_type(4))) short bf16x4;
typedef __attribute__((ext_vector_type(4))) float f32x4;
typedef __attribute__((address_space(3))) bf16x4* lds_b4_ptr;

constexpr int S_ = 2048, D_ = 1024, F_ = 3584, DF_ = 513;

__device__ __forceinline__ float bf2f(u16 v) { return __uint_as_float(((uint32_t)v) << 16); }
__device__ __forceinline__ u16 f2bf(float f) { __bf16 b = (__bf16)f; return __builtin_bit_cast(u16, b); }
__device__ __forceinline__ float rbf(float f) { return bf2f(f2bf(f)); }

__device__ __forceinline__ float2 cmul(float2 a, float2 b) { return make_float2(a.x * b.x - a.y * b.y, a.x * b.y + a.y * b.x); }
__device__ __forceinline__ float2 cadd(float2 a, float2 b) { return make_float2(a.x + b.x, a.y + b.y); }
__device__ __forceinline__ float2 csub(float2 a, float2 b) { return make_float2(a.x - b.x, a.y - b.y); }
template <int DIR>
__device__ __forceinline__ float2 cexp2pi(float rev) {
  float c = __builtin_amdgcn_cosf(rev), s = __builtin_amdgcn_sinf(rev);
  return make_float2(c, DIR > 0 ? s : -s);
}
template <int DIR>
__device__ __forceinline__ float2 muli(float2 a) { return DIR > 0 ? make_float2(-a.y, a.x) : make_float2(a.y, -a.x); }

template <int DIR>
__device__ __forceinline__ void dft4(float2& x0, float2& x1, float2& x2, float2& x3) {
  float2 t0 = cadd(x0, x2), t1 = csub(x0, x2), t2 = cadd(x1, x3), t3 = muli<DIR>(csub(x1, x3));
  x0 = cadd(t0, t2); x2 = csub(t0, t2); x1 = cadd(t1, t3); x3 = csub(t1, t3);
}
template <int DIR>
__device__ __forceinline__ void dft8(float2 (&v)[8]) {
  float2 e0 = v[0], e1 = v[2], e2 = v[4], e3 = v[6];
  float2 o0 = v[1], o1 = v[3], o2 = v[5], o3 = v[7];
  dft4<DIR>(e0, e1, e2, e3);
  dft4<DIR>(o0, o1, o2, o3);
  const float r = 0.70710678118654752440f;
  float2 t1 = DIR > 0 ? make_float2((o1.x - o1.y) * r, (o1.x + o1.y) * r) : make_float2((o1.x + o1.y) * r, (o1.y - o1.x) * r);
  float2 t2 = muli<DIR>(o2);
  float2 t3 = DIR > 0 ? make_float2((-o3.x - o3.y) * r, (o3.x - o3.y) * r) : make_float2((o3.y - o3.x) * r, (-o3.x - o3.y) * r);
  v[0] = cadd(e0, o0); v[4] = csub(e0, o0);
  v[1] = cadd(e1, t1); v[5] = csub(e1, t1);
  v[2] = cadd(e2, t2); v[6] = csub(e2, t2);
  v[3] = cadd(e3, t3); v[7] = csub(e3, t3);
}
__device__ __forceinline__ void twiddle8(float2 (&v)[8], float2 w) {
  float2 w2 = cmul(w, w), w3 = cmul(w2, w), w4 = cmul(w2, w2), w5 = cmul(w4, w), w6 = cmul(w3, w3), w7 = cmul(w4, w3);
  v[1] = cmul(v[1], w); v[2] = cmul(v[2], w2); v[3] = cmul(v[3], w3); v[4] = cmul(v[4], w4);
  v[5] = cmul(v[5], w5); v[6] = cmul(v[6], w6); v[7] = cmul(v[7], w7);
}
__device__ __forceinline__ void twiddle8b(float2 (&v)[8], float2 b, float2 w) {
  float2 w2 = cmul(w, w), w4 = cmul(w2, w2);
  float2 b1 = cmul(b, w), b2 = cmul(b, w2), b3 = cmul(b1, w2), b4 = cmul(b, w4), b5 = cmul(b1, w4), b6 = cmul(b2, w4), b7 = cmul(b3, w4);
  v[0] = cmul(v[0], b); v[1] = cmul(v[1], b1); v[2] = cmul(v[2], b2); v[3] = cmul(v[3], b3);
  v[4] = cmul(v[4], b4); v[5] = cmul(v[5], b5); v[6] = cmul(v[6], b6); v[7] = cmul(v[7], b7);
}

__device__ __forceinline__ float wave_sum(float v) {
#pragma unroll
  for (int o = 32; o > 0; o >>= 1) v += __shfl_xor(v, o, 64);
  return v;
}

#define WAVE_SYNC() do { __builtin_amdgcn_fence(__ATOMIC_RELEASE, "wavefront"); __builtin_amdgcn_wave_barrier(); __builtin_amdgcn_fence(__ATOMIC_ACQUIRE, "wavefront"); } while (0)

template <int DIR>
__device__ __forceinline__ void fft512_wave(float2 (&v)[8], float2* ex, int lane) {
  const int hi = lane >> 3, lo = lane & 7;
  dft8<DIR>(v);
  twiddle8(v, cexp2pi<DIR>((float)hi * (1.0f / 64.0f)));
#pragma unroll
  for (int k0 = 0; k0 < 8; ++k0) ex[72 * k0 + lane] = v[k0];
  WAVE_SYNC();
#pragma unroll
  for (int b = 0; b < 8; ++b) v[b] = ex[72 * hi + 8 * b + lo];
  WAVE_SYNC();
  dft8<DIR>(v);
  twiddle8b(v, cexp2pi<DIR>((float)(lo * hi) * (1.0f / 512.0f)), cexp2pi<DIR>((float)lo * (1.0f / 64.0f)));
#pragma unroll
  for (int k1 = 0; k1 < 8; ++k1) ex[66 * lo + 8 * k1 + hi] = v[k1];
  WAVE_SYNC();
#pragma unroll
  for (int c = 0; c < 8; ++c) v[c] = ex[66 * c + lane];
  WAVE_SYNC();
  dft8<DIR>(v);
}

__device__ __forceinline__ void ln_bf16(float (&e)[16]) {
  float s = 0.f;
#pragma unroll
  for (int i = 0; i < 16; ++i) s += e[i];
  s = wave_sum(s);
  const float mean = s * (1.0f / 1024.0f);
  float q = 0.f;
#pragma unroll
  for (int i = 0; i < 16; ++i) { float d = e[i] - mean; q += d * d; }
  q = wave_sum(q);
  const float mb = rbf(mean);
  const float vb = rbf(q * (1.0f / 1024.0f));
  const float sd = rbf(sqrtf(rbf(vb + 1e-5f)));
#pragma unroll
  for (int i = 0; i < 16; ++i) e[i] = rbf(rbf(e[i] - mb) / sd);
}

__global__ __launch_bounds__(512) void k1_ln_rowfft(const u16* __restrict__ x, const u16* __restrict__ wr, const u16* __restrict__ wi,
                                                     float2* __restrict__ T, uint32_t* __restrict__ WT) {
  __shared__ float2 ex_all[8 * 576];
  __shared__ float2 st[512 * 9];
  const int tid = threadIdx.x, lane = tid & 63, w = tid >> 6;
  const int s0 = blockIdx.x * 8;
  const int row = s0 + w;
  {
    uint32_t p[8];
#pragma unroll
    for (int r = 0; r < 8; ++r) {
      const size_t o = (size_t)(s0 + r) * DF_ + tid;
      p[r] = (uint32_t)wr[o] | ((uint32_t)wi[o] << 16);
    }
    uint4* dst = reinterpret_cast<uint4*>(WT + (size_t)tid * S_ + s0);
    dst[0] = make_uint4(p[0], p[1], p[2], p[3]);
    dst[1] = make_uint4(p[4], p[5], p[6], p[7]);
    if (tid < 8) {
      const size_t o = (size_t)(s0 + tid) * DF_ + 512;
      WT[(size_t)512 * S_ + s0 + tid] = (uint32_t)wr[o] | ((uint32_t)wi[o] << 16);
    }
  }
  const uint32_t* xr = reinterpret_cast<const uint32_t*>(x + (size_t)row * D_);
  float e[16];
#pragma unroll
  for (int a = 0; a < 8; ++a) {
    uint32_t u = xr[lane + 64 * a];
    e[2 * a] = __uint_as_float(u << 16);
    e[2 * a + 1] = __uint_as_float(u & 0xffff0000u);
  }
  ln_bf16(e);
  float2 v[8];
#pragma unroll
  for (int a = 0; a < 8; ++a) v[a] = make_float2(e[2 * a], e[2 * a + 1]);
  float2* ex = ex_all + w * 576;
  fft512_wave<-1>(v, ex, lane);
#pragma unroll
  for (int j = 0; j < 8; ++j) ex[lane + 64 * j] = v[j];
  WAVE_SYNC();
#pragma unroll
  for (int j = 0; j < 8; ++j) {
    const int k = lane + 64 * j;
    float2 zp = ex[(512 - k) & 511];
    float2 z = v[j];
    float2 E = make_float2(0.5f * (z.x + zp.x), 0.5f * (z.y - zp.y));
    float2 O = make_float2(0.5f * (z.y + zp.y), 0.5f * (zp.x - z.x));
    float2 tw = cexp2pi<-1>((float)k * (1.0f / 1024.0f));
    float2 X = cadd(E, cmul(tw, O));
    if (k == 0) X = make_float2(z.x + z.y, z.x - z.y);
    st[k * 9 + w] = X;
  }
  __syncthreads();
#pragma unroll
  for (int it = 0; it < 4; ++it) {
    const int idx = it * 512 + tid;
    const int k = idx >> 2, part = idx & 3;
    float2 a = st[k * 9 + 2 * part], b = st[k * 9 + 2 * part + 1];
    reinterpret_cast<float4*>(T + (size_t)k * S_ + s0)[part] = make_float4(a.x, a.y, b.x, b.y);
  }
}

__device__ __forceinline__ float2 wunpack(uint32_t u) { return make_float2(__uint_as_float(u << 16), __uint_as_float(u & 0xffff0000u)); }

#define KEEP(v) asm volatile("" :: "v"(v))
__global__ __launch_bounds__(256) void k2_colfft(float2* __restrict__ T, const uint32_t* __restrict__ WT, const char* __restrict__ pf) {
  __shared__ float2 ex[2304];
  const int t = threadIdx.x;
  const int col = blockIdx.x;
  float2* Tc = T + (size_t)col * S_;
  uint32_t pfv = 0;
  if (t < 112) pfv = *reinterpret_cast<const uint32_t*>(pf + ((size_t)col * 112 + t) * 128);
  float2 v[8];
#pragma unroll
  for (int a = 0; a < 8; ++a) v[a] = Tc[t + 256 * a];
  uint32_t wv[8];
#pragma unroll
  for (int r = 0; r < 8; ++r) wv[r] = WT[(size_t)col * S_ + t + 256 * r];
  const int h5 = t >> 5, l5 = t & 31;
  const int m3 = (t >> 2) & 7, d2 = t & 3;
  dft8<-1>(v);
  twiddle8(v, cexp2pi<-1>((float)t * (1.0f / 2048.0f)));
#pragma unroll
  for (int k0 = 0; k0 < 8; ++k0) ex[256 * k0 + t] = v[k0];
  __syncthreads();
#pragma unroll
  for (int b = 0; b < 8; ++b) v[b] = ex[256 * h5 + 32 * b + l5];
  __syncthreads();
  dft8<-1>(v);
  twiddle8(v, cexp2pi<-1>((float)l5 * (1.0f / 256.0f)));
#pragma unroll
  for (int k1 = 0; k1 < 8; ++k1) ex[288 * h5 + 36 * k1 + l5] = v[k1];
  WAVE_SYNC();
#pragma unroll
  for (int c = 0; c < 8; ++c) v[c] = ex[288 * h5 + 36 * m3 + 4 * c + d2];
  __syncthreads();
  dft8<-1>(v);
  twiddle8(v, cexp2pi<-1>((float)d2 * (1.0f / 32.0f)));
#pragma unroll
  for (int k2 = 0; k2 < 8; ++k2) ex[(4 * k2 + d2) * 66 + 8 * m3 + h5] = v[k2];
  __syncthreads();
  const int dd = t >> 6, l6 = t & 63;
#pragma unroll
  for (int d = 0; d < 4; ++d) { v[d] = ex[(4 * dd + d) * 66 + l6]; v[4 + d] = ex[(4 * (dd + 4) + d) * 66 + l6]; }
  __syncthreads();
  dft4<-1>(v[0], v[1], v[2], v[3]);
  dft4<-1>(v[4], v[5], v[6], v[7]);
  if (col != 0) {
#pragma unroll
    for (int k3 = 0; k3 < 4; ++k3) {
      v[k3] = cmul(v[k3], wunpack(wv[2 * k3]));
      v[4 + k3] = cmul(v[4 + k3], wunpack(wv[2 * k3 + 1]));
    }
  } else {
#pragma unroll
    for (int k3 = 0; k3 < 4; ++k3) { ex[t + 512 * k3] = v[k3]; ex[t + 256 + 512 * k3] = v[4 + k3]; }
    __syncthreads();
#pragma unroll
    for (int r = 0; r < 8; ++r) {
      const int vi = (r & 1) * 4 + (r >> 1);
      const int k = t + 256 * r, kn = (2048 - k) & 2047;
      float2 p = v[vi], pn = ex[kn];
      float2 F0 = make_float2(0.5f * (p.x + pn.x), 0.5f * (p.y - pn.y));
      float2 F5 = make_float2(0.5f * (p.y + pn.y), 0.5f * (pn.x - p.x));
      float2 wa = wunpack(wv[r]), wan = wunpack(WT[kn]);
      float2 wb = wunpack(WT[(size_t)512 * S_ + k]), wbn = wunpack(WT[(size_t)512 * S_ + kn]);
      float2 W0 = make_float2(0.5f * (wa.x + wan.x), 0.5f * (wa.y - wan.y));
      float2 W5 = make_float2(0.5f * (wb.x + wbn.x), 0.5f * (wb.y - wbn.y));
      float2 H0 = cmul(F0, W0), H5 = cmul(F5, W5);
      v[vi] = make_float2(H0.x - H5.y, H0.y + H5.x);
    }
    __syncthreads();
  }
  dft4<1>(v[0], v[1], v[2], v[3]);
  dft4<1>(v[4], v[5], v[6], v[7]);
  {
    float2 wa = cexp2pi<1>((float)dd * (1.0f / 32.0f)), wb = cexp2pi<1>((float)(dd + 4) * (1.0f / 32.0f));
    float2 wa2 = cmul(wa, wa), wa3 = cmul(wa2, wa), wb2 = cmul(wb, wb), wb3 = cmul(wb2, wb);
    v[1] = cmul(v[1], wa); v[2] = cmul(v[2], wa2); v[3] = cmul(v[3], wa3);
    v[5] = cmul(v[5], wb); v[6] = cmul(v[6], wb2); v[7] = cmul(v[7], wb3);
  }
#pragma unroll
  for (int d = 0; d < 4; ++d) { ex[(4 * dd + d) * 66 + l6] = v[d]; ex[(4 * (dd + 4) + d) * 66 + l6] = v[4 + d]; }
  __syncthreads();
#pragma unroll
  for (int k2 = 0; k2 < 8; ++k2) v[k2] = ex[(4 * k2 + d2) * 66 + 8 * m3 + h5];
  __syncthreads();
  dft8<1>(v);
  twiddle8b(v, cexp2pi<1>((float)(m3 * d2) * (1.0f / 256.0f)), cexp2pi<1>((float)m3 * (1.0f / 64.0f)));
#pragma unroll
  for (int c = 0; c < 8; ++c) ex[288 * h5 + 36 * m3 + 4 * c + d2] = v[c];
  WAVE_SYNC();
#pragma unroll
  for (int k1 = 0; k1 < 8; ++k1) v[k1] = ex[288 * h5 + 36 * k1 + l5];
  __syncthreads();
  dft8<1>(v);
  twiddle8b(v, cexp2pi<1>((float)(h5 * l5) * (1.0f / 2048.0f)), cexp2pi<1>((float)h5 * (1.0f / 64.0f)));
#pragma unroll
  for (int b = 0; b < 8; ++b) ex[256 * h5 + 32 * b + l5] = v[b];
  __syncthreads();
#pragma unroll
  for (int k0 = 0; k0 < 8; ++k0) v[k0] = ex[256 * k0 + t];
  dft8<1>(v);
#pragma unroll
  for (int a = 0; a < 8; ++a) Tc[t + 256 * a] = v[a];
  KEEP(pfv);
}

__global__ __launch_bounds__(512) void k3_irowfft_ln(const float2* __restrict__ T, const u16* __restrict__ x,
                                                      u16* __restrict__ h, u16* __restrict__ a_out, const char* __restrict__ pf) {
  __shared__ float2 ex_all[8 * 576];
  __shared__ float2 st[512 * 9];
  const int tid = threadIdx.x, lane = tid & 63, w = tid >> 6;
  uint32_t pfv = 0;
  if (tid < 224) pfv = *reinterpret_cast<const uint32_t*>(pf + ((size_t)blockIdx.x * 224 + tid) * 128);
  const int s0 = blockIdx.x * 8;
  const int row = s0 + w;
#pragma unroll
  for (int it = 0; it < 4; ++it) {
    const int idx = it * 512 + tid;
    const int k = idx >> 2, part = idx & 3;
    float4 q = reinterpret_cast<const float4*>(T + (size_t)k * S_ + s0)[part];
    st[k * 9 + 2 * part] = make_float2(q.x, q.y);
    st[k * 9 + 2 * part + 1] = make_float2(q.z, q.w);
  }
  const uint32_t* xr = reinterpret_cast<const uint32_t*>(x + (size_t)row * D_);
  uint32_t xv[8];
#pragma unroll
  for (int a = 0; a < 8; ++a) xv[a] = xr[lane + 64 * a];
  __syncthreads();
  float2 v[8];
#pragma unroll
  for (int j = 0; j < 8; ++j) {
    const int k = lane + 64 * j;
    float2 g = st[k * 9 + w], gp = st[((512 - k) & 511) * 9 + w];
    float2 E = make_float2(g.x + gp.x, g.y - gp.y);
    float2 O = make_float2(g.x - gp.x, g.y + gp.y);
    float2 tw = cexp2pi<1>((float)k * (1.0f / 1024.0f));
    float2 P = cmul(tw, O);
    float2 Z = make_float2(E.x - P.y, E.y + P.x);
    if (k == 0) Z = make_float2(g.x + g.y, g.x - g.y);
    v[j] = Z;
  }
  float2* ex = ex_all + w * 576;
  fft512_wave<1>(v, ex, lane);
  const float scale = 1.0f / (2048.0f * 1024.0f);
  float e[16];
  uint32_t* hr = reinterpret_cast<uint32_t*>(h + (size_t)row * D_);
#pragma unroll
  for (int j = 0; j < 8; ++j) {
    float y0 = rbf(v[j].x * scale), y1 = rbf(v[j].y * scale);
    float h0 = rbf(__uint_as_float(xv[j] << 16) + y0), h1 = rbf(__uint_as_float(xv[j] & 0xffff0000u) + y1);
    e[2 * j] = h0; e[2 * j + 1] = h1;
    hr[lane + 64 * j] = (__float_as_uint(h0) >> 16) | (__float_as_uint(h1) & 0xffff0000u);
  }
  ln_bf16(e);
  uint32_t* ar = reinterpret_cast<uint32_t*>(a_out + (size_t)row * D_);
#pragma unroll
  for (int j = 0; j < 8; ++j) ar[lane + 64 * j] = (__float_as_uint(e[2 * j]) >> 16) | (__float_as_uint(e[2 * j + 1]) & 0xffff0000u);
  KEEP(pfv);
}

#define WAIT_V(n) asm volatile("s_waitcnt vmcnt(" #n ")" ::: "memory")
#define WAIT_L0() asm volatile("s_waitcnt lgkmcnt(0)" ::: "memory")
#define RAW_BAR() __builtin_amdgcn_s_barrier()

typedef __attribute__((ext_vector_type(2))) float f32x2;
__device__ __forceinline__ uint32_t cvt_pk(float lo, float hi) { uint32_t r; asm("v_cvt_pk_bf16_f32 %0, %1, %2" : "=v"(r) : "v"(lo), "v"(hi)); return r; }
__device__ __forceinline__ f32x2 unpk(uint32_t u) { return (f32x2){__uint_as_float(u << 16), __uint_as_float(u & 0xffff0000u)}; }
__device__ __forceinline__ uint32_t mish_pair(float c0, float c1, f32x2 b) {
  f32x2 x = unpk(cvt_pk(c0, c1)) + b;
  const f32x2 hh = unpk(cvt_pk(x[0], x[1]));
  f32x2 z = hh * 1.44269504088896340736f;
  f32x2 t = {__builtin_amdgcn_exp2f(__builtin_amdgcn_fmed3f(z[0], -200.0f, 43.0f)), __builtin_amdgcn_exp2f(__builtin_amdgcn_fmed3f(z[1], -200.0f, 43.0f))};
  const f32x2 n = t * (t + 2.0f);
  const f32x2 d = n + 2.0f;
  const f32x2 r = {__builtin_amdgcn_rcpf(d[0]), __builtin_amdgcn_rcpf(d[1])};
  const f32x2 o = (hh * n) * r;
  return cvt_pk(o[0], o[1]);
}

typedef __attribute__((ext_vector_type(4))) unsigned int u32x4;
typedef __attribute__((ext_vector_type(2))) unsigned int u32x2;
template <int OFF> __device__ __forceinline__ void lds_rd128(u32x4& d, uint32_t a) { asm volatile("ds_read_b128 %0, %1 offset:%2" : "=v"(d) : "v"(a), "i"(OFF)); }
template <int OFF> __device__ __forceinline__ void lds_rdtr(u32x2& d, uint32_t a) { asm volatile("ds_read_b64_tr_b16 %0, %1 offset:%2" : "=v"(d) : "v"(a), "i"(OFF)); }
__device__ __forceinline__ bf16x8 mk8(u32x2 lo, u32x2 hi) { u32x4 v = {lo[0], lo[1], hi[0], hi[1]}; return __builtin_bit_cast(bf16x8, v); }
#define SCHED0() __builtin_amdgcn_sched_barrier(0)
__device__ __forceinline__ void mfma16(f32x4& c, const u32x2& alo, const u32x2& ahi, const u32x4& b) {
  u32x4 a = {alo[0], alo[1], ahi[0], ahi[1]};
  asm volatile("v_mfma_f32_16x16x32_bf16 %0, %1, %2, %0" : "+v"(c) : "v"(a), "v"(b));
}
template <int N> struct IC { static constexpr int v = N; };

constexpr int G1_A_BYTES = 128 * 128, G1_B_BYTES = 64 * 448, G1_STAGE = G1_A_BYTES + G1_B_BYTES;
constexpr int G1_OT = 57344, G1_OTS = 464;
struct G1Frag { u32x4 a[4]; u32x2 b[7][2]; };
__global__ __launch_bounds__(512, 2) void g1_gemm(const u16* __restrict__ A, const u16* __restrict__ W, const u16* __restrict__ b1,
                                                   u16* __restrict__ act) {
  __shared__ __attribute__((aligned(16))) char smem[3 * G1_STAGE];
  const int tid = threadIdx.x, lane = tid & 63, w8 = __builtin_amdgcn_readfirstlane(tid >> 6), w = w8 & 3;
  const int wm = w >> 1, wn = w & 1;
  const int bid = blockIdx.x, xcd = bid & 7, idx = bid >> 3;
  const int tm = (xcd & 1) * 8 + (idx & 7), tn = (xcd >> 1) * 4 + (idx >> 3);
  const int m0 = tm * 128, n0 = tn * 224;
  uint32_t aoff[4], boff[7];
#pragma unroll
  for (int i = 0; i < 4; ++i) {
    const int r = w * 32 + i * 8 + (lane >> 3);
    aoff[i] = (uint32_t)(m0 + r) * 2048u + (uint32_t)(((lane & 7) ^ (r & 7)) << 4);
  }
#pragma unroll
  for (int i = 0; i < 7; ++i) {
    const int d = i * 64 + lane, r = d / 28, q = d - r * 28, k = w * 16 + r;
    const int c = (q >> 1) ^ ((k >> 3) & 1);
    boff[i] = (uint32_t)k * (uint32_t)(F_ * 2) + (uint32_t)(n0 * 2) + (uint32_t)(((c << 1) | (q & 1)) << 4);
  }
  const char* Ab = reinterpret_cast<const char*>(A);
  const char* Wb = reinterpret_cast<const char*>(W);
  auto issue1 = [&](int i, int kt, int buf) {
    if (i < 4) {
      __builtin_amdgcn_global_load_lds((const unsigned*)(Ab + kt * 128 + aoff[i]),
                                       (unsigned*)(smem + buf * G1_STAGE + w * 4096 + lane * 16 + i * 1024), 16, 0, 0);
    } else {
      __builtin_amdgcn_global_load_lds((const unsigned*)(Wb + (size_t)kt * (64 * F_ * 2) + boff[i - 4]),
                                       (unsigned*)(smem + buf * G1_STAGE + G1_A_BYTES + w * 7168 + lane * 16 + (i - 4) * 1024), 16, 0, 0);
    }
  };
  auto g1_store = [&]() {
    WAIT_L0();
    RAW_BAR();
#pragma unroll
    for (int i = 0; i < 7; ++i) {
      const int c = tid + 512 * i, r = c / 28, q = c - r * 28;
      const uint4 v = *reinterpret_cast<const uint4*>(smem + G1_OT + r * G1_OTS + q * 16);
      *reinterpret_cast<uint4*>(reinterpret_cast<char*>(act) + (size_t)(m0 + r) * (F_ * 2) + n0 * 2 + q * 16) = v;
    }
  };
  constexpr int NK = 16;
  if (w8 >= 4) {
#pragma unroll
    for (int i = 0; i < 11; ++i) issue1(i, 0, 0);
#pragma unroll
    for (int i = 0; i < 11; ++i) issue1(i, 1, 1);
#pragma unroll
    for (int i = 0; i < 11; ++i) issue1(i, 2, 2);
    WAIT_V(22); RAW_BAR();
    int buf = 0;
    for (int kt = 0; kt < NK - 3; ++kt) {
      WAIT_V(11); RAW_BAR();
#pragma unroll
      for (int i = 0; i < 11; ++i) issue1(i, kt + 3, buf);
      buf = buf == 2 ? 0 : buf + 1;
    }
    WAIT_V(11); RAW_BAR();
    WAIT_V(0); RAW_BAR();
    uint2 bb[7];
#pragma unroll
    for (int ni = 0; ni < 7; ++ni) bb[ni] = *reinterpret_cast<const uint2*>(b1 + n0 + wn * 112 + ni * 16 + 4 * (lane >> 4));
    RAW_BAR(); RAW_BAR();
    const f32x4* xfer = reinterpret_cast<const f32x4*>(smem) + w * (14 * 64) + lane;
    char* ot = smem + G1_OT + (wm * 64 + (lane & 15)) * G1_OTS + (wn * 112 + 4 * (lane >> 4)) * 2;
#pragma unroll
    for (int ni = 0; ni < 7; ++ni) {
      const f32x2 b01 = unpk(bb[ni].x), b23 = unpk(bb[ni].y);
#pragma unroll
      for (int mi = 2; mi < 4; ++mi) {
        const f32x4 c = xfer[((mi - 2) * 7 + ni) * 64];
        *reinterpret_cast<uint2*>(ot + mi * 16 * G1_OTS + ni * 32) = make_uint2(mish_pair(c[0], c[1], b01), mish_pair(c[2], c[3], b23));
      }
    }
    g1_store();
    return;
  }
  uint2 bb[7];
#pragma unroll
  for (int ni = 0; ni < 7; ++ni) bb[ni] = *reinterpret_cast<const uint2*>(b1 + n0 + wn * 112 + ni * 16 + 4 * (lane >> 4));
  f32x4 acc[4][7];
#pragma unroll
  for (int i = 0; i < 4; ++i)
#pragma unroll
    for (int j = 0; j < 7; ++j) acc[i][j] = (f32x4){0.f, 0.f, 0.f, 0.f};
  const int fr = lane & 15, fg = lane >> 4;
  const uint32_t sbase = (uint32_t)(size_t)(smem);
  const uint32_t ard0 = sbase + (wm * 64 + fr) * 128 + ((fg ^ (fr & 7)) << 4);
  const int q4 = (lane & 15) >> 2, p4 = lane & 3;
  const uint32_t brd = sbase + G1_A_BYTES + (8 * fg + q4) * 448 + 8 * p4 + wn * 224;
  const int f32_ = (fg & 1) * 32;
  const uint32_t brdE = brd + ((wn & 1) ? -f32_ : f32_), brdO = brd + ((wn & 1) ? f32_ : -f32_);
  G1Frag X, Y;
  auto rd = [&](G1Frag& Fr, auto I, auto KS, uint32_t so) {
    constexpr int i = decltype(I)::v, ks = decltype(KS)::v;
    if constexpr (i < 4) {
      lds_rd128<i * 2048>(Fr.a[i], (ard0 ^ (ks * 64)) + so);
    } else {
      constexpr int ni = (i - 4) >> 1, t = (i - 4) & 1;
      lds_rdtr<ks * 32 * 448 + ni * 32 + t * 4 * 448>(Fr.b[ni][t], ((ni & 1) ? brdO : brdE) + so);
    }
  };
  auto mma1 = [&](G1Frag& Fr, auto J) {
    constexpr int j = decltype(J)::v, mi = j / 7, ni = j % 7;
    mfma16(acc[mi][ni], Fr.b[ni][0], Fr.b[ni][1], Fr.a[mi]);
  };
#define G1_TIE(Fr) asm volatile("s_waitcnt lgkmcnt(0)" : "+v"(Fr.a[0]), "+v"(Fr.a[1]), "+v"(Fr.a[2]), "+v"(Fr.a[3]), \
    "+v"(Fr.b[0][0]), "+v"(Fr.b[0][1]), "+v"(Fr.b[1][0]), "+v"(Fr.b[1][1]), "+v"(Fr.b[2][0]), "+v"(Fr.b[2][1]), "+v"(Fr.b[3][0]), "+v"(Fr.b[3][1]), \
    "+v"(Fr.b[4][0]), "+v"(Fr.b[4][1]), "+v"(Fr.b[5][0]), "+v"(Fr.b[5][1]), "+v"(Fr.b[6][0]), "+v"(Fr.b[6][1]) :: "memory")
  auto block = [&](G1Frag& C, G1Frag& R, auto KS, uint32_t so, auto DO_RD, auto DO_IS, int kt_is, int buf_is) {
    auto one = [&](auto J) {
      constexpr int j = decltype(J)::v;
      mma1(C, J);
      if constexpr (decltype(DO_RD)::v && j < 18) rd(R, J, KS, so);
      if constexpr (decltype(DO_IS)::v && j >= 6 && j < 28 && ((j - 6) & 1) == 0) issue1((j - 6) >> 1, kt_is, buf_is);
      SCHED0();
    };
    one(IC<0>{}); one(IC<1>{}); one(IC<2>{}); one(IC<3>{}); one(IC<4>{}); one(IC<5>{}); one(IC<6>{});
    one(IC<7>{}); one(IC<8>{}); one(IC<9>{}); one(IC<10>{}); one(IC<11>{}); one(IC<12>{}); one(IC<13>{});
    one(IC<14>{}); one(IC<15>{}); one(IC<16>{}); one(IC<17>{}); one(IC<18>{}); one(IC<19>{}); one(IC<20>{});
    one(IC<21>{}); one(IC<22>{}); one(IC<23>{}); one(IC<24>{}); one(IC<25>{}); one(IC<26>{}); one(IC<27>{});
  };
  auto rdall = [&](G1Frag& R, uint32_t so) {
    rd(R, IC<0>{}, IC<0>{}, so); rd(R, IC<1>{}, IC<0>{}, so); rd(R, IC<2>{}, IC<0>{}, so); rd(R, IC<3>{}, IC<0>{}, so);
    rd(R, IC<4>{}, IC<0>{}, so); rd(R, IC<5>{}, IC<0>{}, so); rd(R, IC<6>{}, IC<0>{}, so); rd(R, IC<7>{}, IC<0>{}, so);
    rd(R, IC<8>{}, IC<0>{}, so); rd(R, IC<9>{}, IC<0>{}, so); rd(R, IC<10>{}, IC<0>{}, so); rd(R, IC<11>{}, IC<0>{}, so);
    rd(R, IC<12>{}, IC<0>{}, so); rd(R, IC<13>{}, IC<0>{}, so); rd(R, IC<14>{}, IC<0>{}, so); rd(R, IC<15>{}, IC<0>{}, so);
    rd(R, IC<16>{}, IC<0>{}, so); rd(R, IC<17>{}, IC<0>{}, so);
  };
  RAW_BAR();
  rdall(X, 0);
  int buf = 0;
  for (int kt = 0; kt < NK - 3; ++kt) {
    const int nbuf = buf == 2 ? 0 : buf + 1;
    G1_TIE(X); SCHED0();
    block(X, Y, IC<1>{}, buf * G1_STAGE, IC<1>{}, IC<0>{}, 0, 0);
    G1_TIE(Y); RAW_BAR(); SCHED0();
    block(Y, X, IC<0>{}, nbuf * G1_STAGE, IC<1>{}, IC<0>{}, 0, 0);
    buf = nbuf;
  }
  G1_TIE(X); SCHED0();
  block(X, Y, IC<1>{}, 1 * G1_STAGE, IC<1>{}, IC<0>{}, 0, 0);
  G1_TIE(Y); RAW_BAR(); SCHED0();
  block(Y, X, IC<0>{}, 2 * G1_STAGE, IC<1>{}, IC<0>{}, 0, 0);
  G1_TIE(X); SCHED0();
  block(X, Y, IC<1>{}, 2 * G1_STAGE, IC<1>{}, IC<0>{}, 0, 0);
  G1_TIE(Y); RAW_BAR(); SCHED0();
  block(Y, X, IC<0>{}, 0 * G1_STAGE, IC<1>{}, IC<0>{}, 0, 0);
  G1_TIE(X); SCHED0();
  block(X, Y, IC<1>{}, 0 * G1_STAGE, IC<1>{}, IC<0>{}, 0, 0);
  G1_TIE(Y); SCHED0();
  block(Y, X, IC<0>{}, 0, IC<0>{}, IC<0>{}, 0, 0);
  asm volatile("s_nop 15\n\ts_nop 15" : "+v"(acc[0][0]), "+v"(acc[0][1]), "+v"(acc[0][2]), "+v"(acc[0][3]), "+v"(acc[0][4]), "+v"(acc[0][5]), "+v"(acc[0][6]), "+v"(acc[1][0]), "+v"(acc[1][1]), "+v"(acc[1][2]), "+v"(acc[1][3]), "+v"(acc[1][4]), "+v"(acc[1][5]), "+v"(acc[1][6]), "+v"(acc[2][0]), "+v"(acc[2][1]), "+v"(acc[2][2]), "+v"(acc[2][3]), "+v"(acc[2][4]), "+v"(acc[2][5]), "+v"(acc[2][6]), "+v"(acc[3][0]), "+v"(acc[3][1]), "+v"(acc[3][2]), "+v"(acc[3][3]), "+v"(acc[3][4]), "+v"(acc[3][5]), "+v"(acc[3][6]) :: "memory");
  SCHED0();
  RAW_BAR();
  {
    f32x4* xfer = reinterpret_cast<f32x4*>(smem) + w * (14 * 64) + lane;
#pragma unroll
    for (int mi = 2; mi < 4; ++mi)
#pragma unroll
      for (int ni = 0; ni < 7; ++ni) xfer[((mi - 2) * 7 + ni) * 64] = acc[mi][ni];
  }
  WAIT_L0();
  RAW_BAR();
  char* ot = smem + G1_OT + (wm * 64 + fr) * G1_OTS + (wn * 112 + 4 * fg) * 2;
#pragma unroll
  for (int ni = 0; ni < 7; ++ni) {
    const f32x2 b01 = unpk(bb[ni].x), b23 = unpk(bb[ni].y);
#pragma unroll
    for (int mi = 0; mi < 2; ++mi) {
      const f32x4 c = acc[mi][ni];
      *reinterpret_cast<uint2*>(ot + mi * 16 * G1_OTS + ni * 32) = make_uint2(mish_pair(c[0], c[1], b01), mish_pair(c[2], c[3], b23));
    }
  }
  g1_store();
}

constexpr int G2_A_BYTES = 2 * 128 * 128, G2_B_BYTES = 128 * 128, G2_STAGE = G2_A_BYTES + G2_B_BYTES;
struct G2Frag { u32x4 a[4]; u32x2 b[4][2]; };
__global__ __launch_bounds__(512, 2) void g2_gemm(const u16* __restrict__ A, const u16* __restrict__ W, const u16* __restrict__ h,
                                                   u16* __restrict__ out) {
  __shared__ __attribute__((aligned(16))) char smem[3 * G2_STAGE];
  const int tid = threadIdx.x, lane = tid & 63, w8 = __builtin_amdgcn_readfirstlane(tid >> 6), w = w8 & 3;
  const int wm = w >> 1, wk = w & 1;
  const int bid = blockIdx.x, xcd = bid & 7, idx = bid >> 3;
  const int tm = (xcd & 3) * 4 + (idx & 3), tn = (xcd >> 2) * 8 + (idx >> 2);
  const int m0 = tm * 128, n0 = tn * 64;
  uint32_t aoff[4], boff[4];
#pragma unroll
  for (int i = 0; i < 4; ++i) {
    const int r = w * 32 + i * 8 + (lane >> 3);
    aoff[i] = (uint32_t)(m0 + r) * (uint32_t)(F_ * 2) + (uint32_t)(((lane & 7) ^ (r & 7)) << 4);
  }
#pragma unroll
  for (int i = 0; i < 4; ++i) {
    const int k = w * 32 + i * 8 + (lane >> 3), q = lane & 7;
    const int c = (q >> 1) ^ ((k >> 1) & 1) ^ (((k >> 3) & 1) << 1);
    boff[i] = (uint32_t)k * (uint32_t)(D_ * 2) + (uint32_t)(n0 * 2) + (uint32_t)(((c << 1) | (q & 1)) << 4);
  }
  const char* Ab = reinterpret_cast<const char*>(A);
  const char* Wb = reinterpret_cast<const char*>(W);
  auto issue1 = [&](int i, int kt, int buf) {
    if (i < 8) {
      __builtin_amdgcn_global_load_lds((const unsigned*)(Ab + kt * 256 + (i >> 2) * 128 + aoff[i & 3]),
                                       (unsigned*)(smem + buf * G2_STAGE + w * 4096 + lane * 16 + (i >> 2) * 16384 + (i & 3) * 1024), 16, 0, 0);
    } else {
      __builtin_amdgcn_global_load_lds((const unsigned*)(Wb + (size_t)kt * (128 * D_ * 2) + boff[i - 8]),
                                       (unsigned*)(smem + buf * G2_STAGE + G2_A_BYTES + w * 4096 + lane * 16 + (i - 8) * 1024), 16, 0, 0);
    }
  };
  auto g2_store = [&]() {
    WAIT_L0();
    RAW_BAR();
#pragma unroll
    for (int i = 0; i < 2; ++i) {
      const int c = tid + 512 * i, r = c >> 3, q = c & 7;
      const uint4 v = *reinterpret_cast<const uint4*>(smem + 32768 + r * 144 + q * 16);
      *reinterpret_cast<uint4*>(reinterpret_cast<char*>(out) + (size_t)(m0 + r) * (D_ * 2) + n0 * 2 + q * 16) = v;
    }
  };
  constexpr int NK = 28;
  if (w8 >= 4) {
#pragma unroll
    for (int i = 0; i < 12; ++i) issue1(i, 0, 0);
#pragma unroll
    for (int i = 0; i < 12; ++i) issue1(i, 1, 1);
#pragma unroll
    for (int i = 0; i < 12; ++i) issue1(i, 2, 2);
    WAIT_V(24); RAW_BAR();
    int buf = 0;
    for (int kt = 0; kt < NK - 3; ++kt) {
      WAIT_V(12); RAW_BAR();
#pragma unroll
      for (int i = 0; i < 12; ++i) issue1(i, kt + 3, buf);
      buf = buf == 2 ? 0 : buf + 1;
    }
    WAIT_V(12); RAW_BAR();
    WAIT_V(0); RAW_BAR();
    RAW_BAR(); RAW_BAR();
    g2_store();
    return;
  }
  uint2 hv[2][4];
#pragma unroll
  for (int i = 0; i < 2; ++i)
#pragma unroll
    for (int ni = 0; ni < 4; ++ni)
      hv[i][ni] = *reinterpret_cast<const uint2*>(h + (size_t)(m0 + wm * 64 + (2 * wk + i) * 16 + (lane & 15)) * D_ + n0 + ni * 16 + 4 * (lane >> 4));
  f32x4 acc[4][4];
#pragma unroll
  for (int i = 0; i < 4; ++i)
#pragma unroll
    for (int j = 0; j < 4; ++j) acc[i][j] = (f32x4){0.f, 0.f, 0.f, 0.f};
  const int fr = lane & 15, fg = lane >> 4;
  const uint32_t sbase = (uint32_t)(size_t)(smem);
  const uint32_t ard0 = sbase + wk * 16384 + (wm * 64 + fr) * 128 + ((fg ^ (fr & 7)) << 4);
  const int q4 = (lane & 15) >> 2, p4 = lane & 3;
  const int bsw = ((q4 >> 1) & 1) ^ ((fg & 1) << 1);
  const uint32_t brd = sbase + G2_A_BYTES + (wk * 64 + 8 * fg + q4) * 128 + 8 * p4;
  uint32_t brdn[4];
#pragma unroll
  for (int ni = 0; ni < 4; ++ni) brdn[ni] = brd + ((ni ^ bsw) * 32);
  G2Frag X, Y;
  auto rd = [&](G2Frag& Fr, auto I, auto KS, uint32_t so) {
    constexpr int i = decltype(I)::v, ks = decltype(KS)::v;
    if constexpr (i < 4) {
      lds_rd128<i * 2048>(Fr.a[i], (ard0 ^ (ks * 64)) + so);
    } else {
      constexpr int ni = (i - 4) >> 1, t = (i - 4) & 1;
      lds_rdtr<ks * 32 * 128 + t * 4 * 128>(Fr.b[ni][t], brdn[ni] + so);
    }
  };
  auto mma1 = [&](G2Frag& Fr, auto J) {
    constexpr int j = decltype(J)::v, mi = j / 4, ni = j % 4;
    mfma16(acc[mi][ni], Fr.b[ni][0], Fr.b[ni][1], Fr.a[mi]);
  };
#define G2_TIE(Fr) asm volatile("s_waitcnt lgkmcnt(0)" : "+v"(Fr.a[0]), "+v"(Fr.a[1]), "+v"(Fr.a[2]), "+v"(Fr.a[3]), \
    "+v"(Fr.b[0][0]), "+v"(Fr.b[0][1]), "+v"(Fr.b[1][0]), "+v"(Fr.b[1][1]), "+v"(Fr.b[2][0]), "+v"(Fr.b[2][1]), "+v"(Fr.b[3][0]), "+v"(Fr.b[3][1]) :: "memory")
  auto block = [&](G2Frag& C, G2Frag& R, auto KS, uint32_t so, auto DO_RD, auto DO_IS, int kt_is, int buf_is) {
    auto one = [&](auto J) {
      constexpr int j = decltype(J)::v;
      mma1(C, J);
      if constexpr (decltype(DO_RD)::v && j < 12) rd(R, J, KS, so);
      if constexpr (decltype(DO_IS)::v && j >= 4) issue1(j - 4, kt_is, buf_is);
      SCHED0();
    };
    one(IC<0>{}); one(IC<1>{}); one(IC<2>{}); one(IC<3>{}); one(IC<4>{}); one(IC<5>{}); one(IC<6>{}); one(IC<7>{});
    one(IC<8>{}); one(IC<9>{}); one(IC<10>{}); one(IC<11>{}); one(IC<12>{}); one(IC<13>{}); one(IC<14>{}); one(IC<15>{});
  };
  RAW_BAR();
  rd(X, IC<0>{}, IC<0>{}, 0); rd(X, IC<1>{}, IC<0>{}, 0); rd(X, IC<2>{}, IC<0>{}, 0); rd(X, IC<3>{}, IC<0>{}, 0);
  rd(X, IC<4>{}, IC<0>{}, 0); rd(X, IC<5>{}, IC<0>{}, 0); rd(X, IC<6>{}, IC<0>{}, 0); rd(X, IC<7>{}, IC<0>{}, 0);
  rd(X, IC<8>{}, IC<0>{}, 0); rd(X, IC<9>{}, IC<0>{}, 0); rd(X, IC<10>{}, IC<0>{}, 0); rd(X, IC<11>{}, IC<0>{}, 0);
  int buf = 0;
  for (int kt = 0; kt < NK - 3; ++kt) {
    const int nbuf = buf == 2 ? 0 : buf + 1;
    G2_TIE(X); SCHED0();
    block(X, Y, IC<1>{}, buf * G2_STAGE, IC<1>{}, IC<0>{}, 0, 0);
    G2_TIE(Y); RAW_BAR(); SCHED0();
    block(Y, X, IC<0>{}, nbuf * G2_STAGE, IC<1>{}, IC<0>{}, 0, 0);
    buf = nbuf;
  }
  G2_TIE(X); SCHED0();
  block(X, Y, IC<1>{}, 1 * G2_STAGE, IC<1>{}, IC<0>{}, 0, 0);
  G2_TIE(Y); RAW_BAR(); SCHED0();
  block(Y, X, IC<0>{}, 2 * G2_STAGE, IC<1>{}, IC<0>{}, 0, 0);
  G2_TIE(X); SCHED0();
  block(X, Y, IC<1>{}, 2 * G2_STAGE, IC<1>{}, IC<0>{}, 0, 0);
  G2_TIE(Y); RAW_BAR(); SCHED0();
  block(Y, X, IC<0>{}, 0 * G2_STAGE, IC<1>{}, IC<0>{}, 0, 0);
  G2_TIE(X); SCHED0();
  block(X, Y, IC<1>{}, 0 * G2_STAGE, IC<1>{}, IC<0>{}, 0, 0);
  G2_TIE(Y); SCHED0();
  block(Y, X, IC<0>{}, 0, IC<0>{}, IC<0>{}, 0, 0);
  asm volatile("s_nop 15\n\ts_nop 15" : "+v"(acc[0][0]), "+v"(acc[0][1]), "+v"(acc[0][2]), "+v"(acc[0][3]), "+v"(acc[1][0]), "+v"(acc[1][1]), "+v"(acc[1][2]), "+v"(acc[1][3]), "+v"(acc[2][0]), "+v"(acc[2][1]), "+v"(acc[2][2]), "+v"(acc[2][3]), "+v"(acc[3][0]), "+v"(acc[3][1]), "+v"(acc[3][2]), "+v"(acc[3][3]) :: "memory");
  SCHED0();
  RAW_BAR();
  f32x4* red = reinterpret_cast<f32x4*>(smem) + w * 512 + lane;
  const f32x4* rin = reinterpret_cast<const f32x4*>(smem) + (w ^ 1) * 512 + lane;
  if (wk == 0) {
#pragma unroll
    for (int i = 0; i < 2; ++i)
#pragma unroll
      for (int ni = 0; ni < 4; ++ni) red[(i * 4 + ni) * 64] = acc[2 + i][ni];
  } else {
#pragma unroll
    for (int i = 0; i < 2; ++i)
#pragma unroll
      for (int ni = 0; ni < 4; ++ni) red[(i * 4 + ni) * 64] = acc[i][ni];
  }
  WAIT_L0();
  RAW_BAR();
  auto fin = [&](auto I, const f32x4 (&own)[4]) {
    constexpr int i = decltype(I)::v;
    char* ot = smem + 32768 + (wm * 64 + (2 * wk + i) * 16 + fr) * 144 + 8 * fg;
#pragma unroll
    for (int ni = 0; ni < 4; ++ni) {
      const f32x4 c = own[ni] + rin[(i * 4 + ni) * 64];
      const f32x2 r01 = unpk(hv[i][ni].x) + unpk(cvt_pk(c[0], c[1])), r23 = unpk(hv[i][ni].y) + unpk(cvt_pk(c[2], c[3]));
      *reinterpret_cast<uint2*>(ot + ni * 32) = make_uint2(cvt_pk(r01[0], r01[1]), cvt_pk(r23[0], r23[1]));
    }
  };
  if (wk == 0) { fin(IC<0>{}, acc[0]); fin(IC<1>{}, acc[1]); }
  else { fin(IC<0>{}, acc[2]); fin(IC<1>{}, acc[3]); }
  g2_store();
}

#define G1_LAUNCH(st) g1_gemm<<<256, 512, 0, st>>>(da, dw1, db1, dact)
#define G2_LAUNCH(st) g2_gemm<<<256, 512, 0, st>>>(dact, dw2, dh, dout)
extern "C" void kernel_launch(void* const* d_in, const int* in_sizes, int n_in,
                              void* d_out, int out_size, void* d_ws, size_t ws_size,
                              hipStream_t stream) {
  const u16* x = (const u16*)d_in[0];
  const u16* wr = (const u16*)d_in[1];
  const u16* wi = (const u16*)d_in[2];
  const u16* w1 = (const u16*)d_in[4];
  const u16* b1 = (const u16*)d_in[5];
  const u16* w2 = (const u16*)d_in[6];
  u16* out = (u16*)d_out;
  char* ws = (char*)d_ws;
  float2* T = (float2*)ws;
  uint32_t* WT = (uint32_t*)(ws + (8u << 20));
  u16* h = (u16*)(ws + (16u << 20));
  u16* a = (u16*)(ws + (20u << 20));
  u16* act = (u16*)(ws + (24u << 20));
  k1_ln_rowfft<<<256, 512, 0, stream>>>(x, wr, wi, T, WT);
  k2_colfft<<<512, 256, 0, stream>>>(T, WT, (const char*)w1);
  k3_irowfft_ln<<<256, 512, 0, stream>>>(T, x, h, a, (const char*)w2);
  g1_gemm<<<256, 512, 0, stream>>>(a, w1, b1, act);
  g2_gemm<<<256, 512, 0, stream>>>(act, w2, h, out);
}
```

```cpp
#include <hip/hip_runtime.h>
#include <hip/hip_bf16.h>
#include <stdint.h>

typedef unsigned short u16;
typedef __attribute__((ext_vector_type(8))) short bf16x8;
typedef __attribute__((ext_vector_type(4))) short bf16x4;
typedef __attribute__((ext_vector_type(4))) float f32x4;
typedef __attribute__((address_space(3))) bf16x4* lds_b4_ptr;

constexpr int TBS = 4096 + 16;
constexpr int S_ = 2048, D_ = 1024, F_ = 3584, DF_ = 513;

__device__ __forceinline__ float bf2f(u16 v) { return __uint_as_float(((uint32_t)v) << 16); }
__device__ __forceinline__ u16 f2bf(float f) { __bf16 b = (__bf16)f; return __builtin_bit_cast(u16, b); }
__device__ __forceinline__ float rbf(float f) { return bf2f(f2bf(f)); }

typedef __attribute__((ext_vector_type(4))) unsigned int u32v4;
typedef __attribute__((ext_vector_type(4))) float f32v4;
template <typename T> __device__ __forceinline__ void st_nt(T* p, T v) { __builtin_nontemporal_store(v, p); }
typedef __attribute__((ext_vector_type(2))) float c2;
typedef c2 f32x2;
__device__ __forceinline__ uint32_t cvt_pk(float lo, float hi) { uint32_t r; asm("v_cvt_pk_bf16_f32 %0, %1, %2" : "=v"(r) : "v"(lo), "v"(hi)); return r; }
__device__ __forceinline__ c2 unpk(uint32_t u) { return (c2){__uint_as_float(u << 16), __uint_as_float(u & 0xffff0000u)}; }
__device__ __forceinline__ c2 rbf2(c2 v) { return unpk(cvt_pk(v.x, v.y)); }
__device__ __forceinline__ c2 cmul(c2 a, c2 b) {
  c2 t, r;
  asm("v_pk_mul_f32 %0, %1, %2 op_sel:[0,0] op_sel_hi:[0,1]" : "=v"(t) : "v"(a), "v"(b));
  asm("v_pk_fma_f32 %0, %1, %2, %3 op_sel:[1,1,0] op_sel_hi:[1,0,1] neg_lo:[0,1,0]" : "=v"(r) : "v"(a), "v"(b), "v"(t));
  return r;
}
__device__ __forceinline__ c2 sub_i(c2 t, c2 d) { c2 r; asm("v_pk_add_f32 %0, %1, %2 op_sel:[0,1] op_sel_hi:[1,0] neg_hi:[0,1]" : "=v"(r) : "v"(t), "v"(d)); return r; }
__device__ __forceinline__ c2 add_i(c2 t, c2 d) { c2 r; asm("v_pk_add_f32 %0, %1, %2 op_sel:[0,1] op_sel_hi:[1,0] neg_lo:[0,1]" : "=v"(r) : "v"(t), "v"(d)); return r; }
__device__ __forceinline__ c2 add_c(c2 a, c2 b) { c2 r; asm("v_pk_add_f32 %0, %1, %2 neg_hi:[0,1]" : "=v"(r) : "v"(a), "v"(b)); return r; }
__device__ __forceinline__ c2 sub_c(c2 a, c2 b) { c2 r; asm("v_pk_add_f32 %0, %1, %2 neg_lo:[0,1]" : "=v"(r) : "v"(a), "v"(b)); return r; }
template <int DIR> __device__ __forceinline__ c2 addD(c2 t, c2 d) { return DIR > 0 ? add_i(t, d) : sub_i(t, d); }
template <int DIR> __device__ __forceinline__ c2 subD(c2 t, c2 d) { return DIR > 0 ? sub_i(t, d) : add_i(t, d); }
template <int DIR>
__device__ __forceinline__ c2 cexp2pi(float rev) {
  const float r = DIR > 0 ? rev : -rev;
  c2 w = {__builtin_amdgcn_cosf(r), __builtin_amdgcn_sinf(r)};
  asm("s_nop 0" : "+v"(w));
  return w;
}
template <int DIR>
__device__ __forceinline__ void dft4(c2& x0, c2& x1, c2& x2, c2& x3) {
  const c2 t0 = x0 + x2, t1 = x0 - x2, t2 = x1 + x3, d = x1 - x3;
  x0 = t0 + t2; x2 = t0 - t2; x1 = addD<DIR>(t1, d); x3 = subD<DIR>(t1, d);
}
template <int DIR>
__device__ __forceinline__ void dft8(c2 (&v)[8]) {
  c2 e0 = v[0], e1 = v[2], e2 = v[4], e3 = v[6];
  c2 o0 = v[1], o1 = v[3], o2 = v[5], o3 = v[7];
  dft4<DIR>(e0, e1, e2, e3);
  dft4<DIR>(o0, o1, o2, o3);
  const c2 r = {0.70710678118654752440f, 0.70710678118654752440f};
  const c2 u1 = addD<DIR>(o1, o1), u3 = subD<DIR>(o3, o3);
  v[0] = e0 + o0; v[4] = e0 - o0;
  v[1] = __builtin_elementwise_fma(u1, r, e1); v[5] = __builtin_elementwise_fma(u1, -r, e1);
  v[2] = addD<DIR>(e2, o2); v[6] = subD<DIR>(e2, o2);
  v[3] = __builtin_elementwise_fma(u3, -r, e3); v[7] = __builtin_elementwise_fma(u3, r, e3);
}
__device__ __forceinline__ void twiddle8(c2 (&v)[8], c2 w) {
  const c2 w2 = cmul(w, w), w3 = cmul(w2, w), w4 = cmul(w2, w2), w5 = cmul(w4, w), w6 = cmul(w3, w3), w7 = cmul(w4, w3);
  v[1] = cmul(v[1], w); v[2] = cmul(v[2], w2); v[3] = cmul(v[3], w3); v[4] = cmul(v[4], w4);
  v[5] = cmul(v[5], w5); v[6] = cmul(v[6], w6); v[7] = cmul(v[7], w7);
}
__device__ __forceinline__ void twiddle8b(c2 (&v)[8], c2 b, c2 w) {
  const c2 w2 = cmul(w, w), w4 = cmul(w2, w2);
  const c2 b1 = cmul(b, w), b2 = cmul(b, w2), b3 = cmul(b1, w2), b4 = cmul(b, w4), b5 = cmul(b1, w4), b6 = cmul(b2, w4), b7 = cmul(b3, w4);
  v[0] = cmul(v[0], b); v[1] = cmul(v[1], b1); v[2] = cmul(v[2], b2); v[3] = cmul(v[3], b3);
  v[4] = cmul(v[4], b4); v[5] = cmul(v[5], b5); v[6] = cmul(v[6], b6); v[7] = cmul(v[7], b7);
}

__device__ __forceinline__ float wave_sum(float v) {
#define DPP_ADD(ctrl, rmask) v += __uint_as_float(__builtin_amdgcn_update_dpp(0u, __float_as_uint(v), ctrl, rmask, 0xf, true))
  DPP_ADD(0x111, 0xf); DPP_ADD(0x112, 0xf); DPP_ADD(0x114, 0xf); DPP_ADD(0x118, 0xf);
  DPP_ADD(0x142, 0xa); DPP_ADD(0x143, 0xc);
#undef DPP_ADD
  return __uint_as_float(__builtin_amdgcn_readlane(__float_as_uint(v), 63));
}

#define WAVE_SYNC() do { __builtin_amdgcn_fence(__ATOMIC_RELEASE, "wavefront"); __builtin_amdgcn_wave_barrier(); __builtin_amdgcn_fence(__ATOMIC_ACQUIRE, "wavefront"); } while (0)

template <int DIR>
__device__ __forceinline__ void fft512_wave(c2 (&v)[8], c2* ex, int lane) {
  const int hi = lane >> 3, lo = lane & 7;
  dft8<DIR>(v);
  twiddle8(v, cexp2pi<DIR>((float)hi * (1.0f / 64.0f)));
#pragma unroll
  for (int k0 = 0; k0 < 8; ++k0) ex[72 * k0 + lane] = v[k0];
  WAVE_SYNC();
#pragma unroll
  for (int b = 0; b < 8; ++b) v[b] = ex[72 * hi + 8 * b + lo];
  WAVE_SYNC();
  dft8<DIR>(v);
  twiddle8b(v, cexp2pi<DIR>((float)(lo * hi) * (1.0f / 512.0f)), cexp2pi<DIR>((float)lo * (1.0f / 64.0f)));
#pragma unroll
  for (int k1 = 0; k1 < 8; ++k1) ex[66 * lo + 8 * k1 + hi] = v[k1];
  WAVE_SYNC();
#pragma unroll
  for (int c = 0; c < 8; ++c) v[c] = ex[66 * c + lane];
  WAVE_SYNC();
  dft8<DIR>(v);
}

__device__ __forceinline__ void ln_bf16(c2 (&e)[8], uint32_t (&o)[8]) {
  c2 s2 = ((e[0] + e[1]) + (e[2] + e[3])) + ((e[4] + e[5]) + (e[6] + e[7]));
  const float mean = wave_sum(s2.x + s2.y) * (1.0f / 1024.0f);
  c2 q2 = {0.f, 0.f};
#pragma unroll
  for (int i = 0; i < 8; ++i) { const c2 d = e[i] - mean; q2 = __builtin_elementwise_fma(d, d, q2); }
  const float var = wave_sum(q2.x + q2.y) * (1.0f / 1024.0f);
  const float mb = rbf(mean);
  const float sd = rbf(sqrtf(rbf(rbf(var) + 1e-5f)));
  const float ri = 1.0f / sd;
#pragma unroll
  for (int i = 0; i < 8; ++i) {
    const c2 d = rbf2(e[i] - mb);
    const c2 q = d * ri;
    const c2 rem = __builtin_elementwise_fma(-q, (c2){sd, sd}, d);
    const c2 qq = __builtin_elementwise_fma(rem, (c2){ri, ri}, q);
    o[i] = cvt_pk(qq.x, qq.y);
  }
}

__global__ __launch_bounds__(512) void k1_ln_rowfft(const u16* __restrict__ x, const u16* __restrict__ wr, const u16* __restrict__ wi,
                                                     c2* __restrict__ T, uint32_t* __restrict__ WT, unsigned* __restrict__ flags) {
  __shared__ c2 ex_all[8 * 576];
  __shared__ c2 st[512 * 9];
  const int tid = threadIdx.x, lane = tid & 63, w = tid >> 6;
  const int s0 = blockIdx.x * 8;
  const int row = s0 + w;
  const uint32_t* xr = reinterpret_cast<const uint32_t*>(x + (size_t)row * D_);
  uint32_t xu[8];
#pragma unroll
  for (int a = 0; a < 8; ++a) xu[a] = xr[lane + 64 * a];
  if (tid == 0) flags[blockIdx.x * 16] = 0;
  {
    uint32_t p[8];
#pragma unroll
    for (int r = 0; r < 8; ++r) {
      const size_t o = (size_t)(s0 + r) * DF_ + tid;
      p[r] = (uint32_t)wr[o] | ((uint32_t)wi[o] << 16);
    }
    u32v4* dst = reinterpret_cast<u32v4*>(WT + (size_t)tid * S_ + s0);
    st_nt(dst, (u32v4){p[0], p[1], p[2], p[3]});
    st_nt(dst + 1, (u32v4){p[4], p[5], p[6], p[7]});
    if (tid < 8) {
      const size_t o = (size_t)(s0 + tid) * DF_ + 512;
      WT[(size_t)512 * S_ + s0 + tid] = (uint32_t)wr[o] | ((uint32_t)wi[o] << 16);
    }
  }
  c2 v[8];
#pragma unroll
  for (int a = 0; a < 8; ++a) v[a] = unpk(xu[a]);
  ln_bf16(v, xu);
#pragma unroll
  for (int a = 0; a < 8; ++a) v[a] = unpk(xu[a]);
  c2* ex = ex_all + w * 576;
  fft512_wave<-1>(v, ex, lane);
#pragma unroll
  for (int j = 4; j < 8; ++j) ex[lane + 64 * j] = v[j];
  ex[512 + lane] = v[0];
  WAVE_SYNC();
  const c2 tw0 = cexp2pi<-1>((float)lane * (1.0f / 1024.0f));
  const c2 half = {0.5f, 0.5f};
#pragma unroll
  for (int j = 0; j < 4; ++j) {
    const int k = lane + 64 * j;
    const c2 z = v[j], zp = ex[512 - k];
    const c2 cj = j == 0 ? (c2){1.f, 0.f} : j == 1 ? (c2){0.92387953251128674f, -0.38268343236508977f}
                : j == 2 ? (c2){0.70710678118654752f, -0.70710678118654752f} : (c2){0.38268343236508977f, -0.92387953251128674f};
    const c2 tw = j == 0 ? tw0 : cmul(tw0, cj);
    const c2 E = add_c(z, zp) * half, Dz = sub_c(z, zp) * half;
    const c2 P = cmul(tw, Dz);
    c2 Xa = sub_i(E, P), Xb = add_i(E, P);
    Xb.y = -Xb.y;
    if (k == 0) { Xa = (c2){z.x + z.y, z.x - z.y}; Xb = (c2){v[4].x, -v[4].y}; }
    st[k * 9 + w] = Xa;
    st[(k == 0 ? 256 : 512 - k) * 9 + w] = Xb;
  }
  __syncthreads();
#pragma unroll
  for (int it = 0; it < 4; ++it) {
    const int idx = it * 512 + tid;
    const int k = idx >> 2, part = idx & 3;
    const c2 a = st[k * 9 + 2 * part], b = st[k * 9 + 2 * part + 1];
    st_nt(reinterpret_cast<f32v4*>(T + (size_t)k * S_ + s0) + part, (f32v4){a.x, a.y, b.x, b.y});
  }
}

#define KEEP(v) asm volatile("" :: "v"(v))
__device__ __forceinline__ uint32_t wt_at(const uint32_t* WT, int k1, int c) { return WT[(size_t)c * S_ + k1]; }
__global__ __launch_bounds__(256) void k2_colfft(const c2* __restrict__ T, c2* __restrict__ U, const uint32_t* __restrict__ WT, const char* __restrict__ pf) {
  __shared__ c2 ex[2304];
  const int t = threadIdx.x, col = blockIdx.x;
  c2 v[8];
#pragma unroll
  for (int a = 0; a < 8; ++a) v[a] = T[(size_t)col * S_ + t + 256 * a];
  uint32_t wv[8];
#pragma unroll
  for (int r = 0; r < 8; ++r) wv[r] = wt_at(WT, t + 256 * r, col);
  uint32_t pfv = 0;
  if (t < 112) pfv = *reinterpret_cast<const uint32_t*>(pf + ((size_t)col * 112 + t) * 128);
  c2* Tg = U + (size_t)(t >> 3) * TBS + col * 8 + (t & 7);
  const int h5 = t >> 5, l5 = t & 31;
  const int m3 = (t >> 2) & 7, d2 = t & 3;
  dft8<-1>(v);
  twiddle8(v, cexp2pi<-1>((float)t * (1.0f / 2048.0f)));
#pragma unroll
  for (int k0 = 0; k0 < 8; ++k0) ex[256 * k0 + t] = v[k0];
  __syncthreads();
#pragma unroll
  for (int b = 0; b < 8; ++b) v[b] = ex[256 * h5 + 32 * b + l5];
  __syncthreads();
  dft8<-1>(v);
  twiddle8(v, cexp2pi<-1>((float)l5 * (1.0f / 256.0f)));
#pragma unroll
  for (int k1 = 0; k1 < 8; ++k1) ex[288 * h5 + 36 * k1 + l5] = v[k1];
  WAVE_SYNC();
#pragma unroll
  for (int c = 0; c < 8; ++c) v[c] = ex[288 * h5 + 36 * m3 + 4 * c + d2];
  __syncthreads();
  dft8<-1>(v);
  twiddle8(v, cexp2pi<-1>((float)d2 * (1.0f / 32.0f)));
#pragma unroll
  for (int k2 = 0; k2 < 8; ++k2) ex[(4 * k2 + d2) * 66 + 8 * m3 + h5] = v[k2];
  __syncthreads();
  const int dd = t >> 6, l6 = t & 63;
#pragma unroll
  for (int d = 0; d < 4; ++d) { v[d] = ex[(4 * dd + d) * 66 + l6]; v[4 + d] = ex[(4 * (dd + 4) + d) * 66 + l6]; }
  __syncthreads();
  dft4<-1>(v[0], v[1], v[2], v[3]);
  dft4<-1>(v[4], v[5], v[6], v[7]);
  if (col == 0) {
#pragma unroll
    for (int k3 = 0; k3 < 4; ++k3) { ex[t + 512 * k3] = v[k3]; ex[t + 256 + 512 * k3] = v[4 + k3]; }
    __syncthreads();
  }
  if (col != 0) {
#pragma unroll
    for (int k3 = 0; k3 < 4; ++k3) {
      v[k3] = cmul(v[k3], unpk(wv[2 * k3]));
      v[4 + k3] = cmul(v[4 + k3], unpk(wv[2 * k3 + 1]));
    }
  } else {
#pragma unroll
    for (int r = 0; r < 8; ++r) {
      const int vi = (r & 1) * 4 + (r >> 1);
      const int k = t + 256 * r, kn = (2048 - k) & 2047;
      const c2 p = v[vi], pn = ex[kn];
      const c2 F0 = {0.5f * (p.x + pn.x), 0.5f * (p.y - pn.y)};
      const c2 F5 = {0.5f * (p.y + pn.y), 0.5f * (pn.x - p.x)};
      const c2 wa = unpk(wv[r]), wan = unpk(wt_at(WT, kn, 0));
      const c2 wb = unpk(wt_at(WT, k, 512)), wbn = unpk(wt_at(WT, kn, 512));
      const c2 W0 = {0.5f * (wa.x + wan.x), 0.5f * (wa.y - wan.y)};
      const c2 W5 = {0.5f * (wb.x + wbn.x), 0.5f * (wb.y - wbn.y)};
      const c2 H0 = cmul(F0, W0), H5 = cmul(F5, W5);
      v[vi] = add_i(H0, H5);
    }
  }
  if (col == 0) __syncthreads();
  dft4<1>(v[0], v[1], v[2], v[3]);
  dft4<1>(v[4], v[5], v[6], v[7]);
  {
    const c2 wa = cexp2pi<1>((float)dd * (1.0f / 32.0f)), wb = cexp2pi<1>((float)(dd + 4) * (1.0f / 32.0f));
    const c2 wa2 = cmul(wa, wa), wa3 = cmul(wa2, wa), wb2 = cmul(wb, wb), wb3 = cmul(wb2, wb);
    v[1] = cmul(v[1], wa); v[2] = cmul(v[2], wa2); v[3] = cmul(v[3], wa3);
    v[5] = cmul(v[5], wb); v[6] = cmul(v[6], wb2); v[7] = cmul(v[7], wb3);
  }
#pragma unroll
  for (int d = 0; d < 4; ++d) { ex[(4 * dd + d) * 66 + l6] = v[d]; ex[(4 * (dd + 4) + d) * 66 + l6] = v[4 + d]; }
  __syncthreads();
#pragma unroll
  for (int k2 = 0; k2 < 8; ++k2) v[k2] = ex[(4 * k2 + d2) * 66 + 8 * m3 + h5];
  __syncthreads();
  dft8<1>(v);
  twiddle8b(v, cexp2pi<1>((float)(m3 * d2) * (1.0f / 256.0f)), cexp2pi<1>((float)m3 * (1.0f / 64.0f)));
#pragma unroll
  for (int c = 0; c < 8; ++c) ex[288 * h5 + 36 * m3 + 4 * c + d2] = v[c];
  WAVE_SYNC();
#pragma unroll
  for (int k1 = 0; k1 < 8; ++k1) v[k1] = ex[288 * h5 + 36 * k1 + l5];
  __syncthreads();
  dft8<1>(v);
  twiddle8b(v, cexp2pi<1>((float)(h5 * l5) * (1.0f / 2048.0f)), cexp2pi<1>((float)h5 * (1.0f / 64.0f)));
#pragma unroll
  for (int b = 0; b < 8; ++b) ex[256 * h5 + 32 * b + l5] = v[b];
  __syncthreads();
#pragma unroll
  for (int k0 = 0; k0 < 8; ++k0) v[k0] = ex[256 * k0 + t];
  dft8<1>(v);
#pragma unroll
  for (int a = 0; a < 8; ++a) st_nt(Tg + (size_t)a * (32 * TBS), v[a]);
  KEEP(pfv);
}

__global__ __launch_bounds__(512) void k3_irowfft_ln(const c2* __restrict__ T, const u16* __restrict__ x,
                                                      u16* __restrict__ h, u16* __restrict__ a_out, const char* __restrict__ pf) {
  __shared__ c2 ex_all[8 * 576];
  __shared__ c2 st[512 * 9];
  const int tid = threadIdx.x, lane = tid & 63, w = tid >> 6;
  const int s0 = blockIdx.x * 8;
  const int row = s0 + w;
  float4 q4[4];
#pragma unroll
  for (int it = 0; it < 4; ++it) {
    const int idx = it * 512 + tid;
    q4[it] = reinterpret_cast<const float4*>(T + (size_t)blockIdx.x * TBS)[idx];
  }
  const uint32_t* xr = reinterpret_cast<const uint32_t*>(x + (size_t)row * D_);
  uint32_t xv[8];
#pragma unroll
  for (int a = 0; a < 8; ++a) xv[a] = xr[lane + 64 * a];
  uint32_t pfv = 0;
  if (tid < 224) pfv = *reinterpret_cast<const uint32_t*>(pf + ((size_t)blockIdx.x * 224 + tid) * 128);
#pragma unroll
  for (int it = 0; it < 4; ++it) {
    const int idx = it * 512 + tid;
    const int k = idx >> 2, part = idx & 3;
    st[k * 9 + 2 * part] = (c2){q4[it].x, q4[it].y};
    st[k * 9 + 2 * part + 1] = (c2){q4[it].z, q4[it].w};
  }
  __syncthreads();
  c2 v[8];
  const c2 tw0 = cexp2pi<1>((float)lane * (1.0f / 1024.0f));
#pragma unroll
  for (int j = 0; j < 8; ++j) {
    const int k = lane + 64 * j;
    const c2 g = st[k * 9 + w], gp = st[((512 - k) & 511) * 9 + w];
    const float cs[8] = {1.f, 0.92387953251128674f, 0.70710678118654752f, 0.38268343236508977f, 0.f, -0.38268343236508977f, -0.70710678118654752f, -0.92387953251128674f};
    const float sn[8] = {0.f, 0.38268343236508977f, 0.70710678118654752f, 0.92387953251128674f, 1.f, 0.92387953251128674f, 0.70710678118654752f, 0.38268343236508977f};
    const c2 tw = j == 0 ? tw0 : cmul(tw0, (c2){cs[j], sn[j]});
    c2 Z = add_i(add_c(g, gp), cmul(tw, sub_c(g, gp)));
    if (k == 0) Z = (c2){g.x + g.y, g.x - g.y};
    v[j] = Z;
  }
  c2* ex = ex_all + w * 576;
  fft512_wave<1>(v, ex, lane);
  const float scale = 1.0f / (2048.0f * 1024.0f);
  uint32_t* hr = reinterpret_cast<uint32_t*>(h + (size_t)row * D_);
#pragma unroll
  for (int j = 0; j < 8; ++j) {
    const c2 hh = unpk(xv[j]) + rbf2(v[j] * scale);
    const uint32_t hu = cvt_pk(hh.x, hh.y);
    st_nt(hr + lane + 64 * j, hu);
    v[j] = unpk(hu);
  }
  ln_bf16(v, xv);
  uint32_t* ar = reinterpret_cast<uint32_t*>(a_out + (size_t)row * D_);
#pragma unroll
  for (int j = 0; j < 8; ++j) st_nt(ar + lane + 64 * j, xv[j]);
  KEEP(pfv);
}

#define WAIT_V(n) asm volatile("s_waitcnt vmcnt(" #n ")" ::: "memory")
#define WAIT_L0() asm volatile("s_waitcnt lgkmcnt(0)" ::: "memory")
#define RAW_BAR() __builtin_amdgcn_s_barrier()

__device__ __forceinline__ uint32_t mish_pair(float c0, float c1, f32x2 b) {
  f32x2 x = unpk(cvt_pk(c0, c1)) + b;
  const f32x2 hh = unpk(cvt_pk(x[0], x[1]));
  f32x2 z = hh * 1.44269504088896340736f;
  f32x2 t = {__builtin_amdgcn_exp2f(__builtin_amdgcn_fmed3f(z[0], -200.0f, 43.0f)), __builtin_amdgcn_exp2f(__builtin_amdgcn_fmed3f(z[1], -200.0f, 43.0f))};
  const f32x2 n = t * (t + 2.0f);
  const f32x2 d = n + 2.0f;
  const f32x2 r = {__builtin_amdgcn_rcpf(d[0]), __builtin_amdgcn_rcpf(d[1])};
  const f32x2 o = (hh * n) * r;
  return cvt_pk(o[0], o[1]);
}

typedef __attribute__((ext_vector_type(4))) unsigned int u32x4;
typedef __attribute__((ext_vector_type(2))) unsigned int u32x2;
template <int OFF> __device__ __forceinline__ void lds_rd128(u32x4& d, uint32_t a) { asm volatile("ds_read_b128 %0, %1 offset:%2" : "=v"(d) : "v"(a), "i"(OFF)); }
template <int OFF> __device__ __forceinline__ void lds_rdtr(u32x2& d, uint32_t a) { asm volatile("ds_read_b64_tr_b16 %0, %1 offset:%2" : "=v"(d) : "v"(a), "i"(OFF)); }
__device__ __forceinline__ bf16x8 mk8(u32x2 lo, u32x2 hi) { u32x4 v = {lo[0], lo[1], hi[0], hi[1]}; return __builtin_bit_cast(bf16x8, v); }
#define SCHED0() __builtin_amdgcn_sched_barrier(0)
__device__ __forceinline__ void mfma16(f32x4& c, const u32x2& alo, const u32x2& ahi, const u32x4& b) {
  u32x4 a = {alo[0], alo[1], ahi[0], ahi[1]};
  asm volatile("v_mfma_f32_16x16x32_bf16 %0, %1, %2, %0" : "+v"(c) : "v"(a), "v"(b));
}
template <int N> struct IC { static constexpr int v = N; };

constexpr int G1_A_BYTES = 128 * 128, G1_B_BYTES = 64 * 448, G1_STAGE = G1_A_BYTES + G1_B_BYTES;
constexpr int G1_OT = 57344, G1_OTS = 464;
struct G1Frag { u32x4 a[4]; u32x2 b[7][2]; };
__global__ __launch_bounds__(512, 2) void g1_gemm(const u16* __restrict__ A, const u16* __restrict__ W, const u16* __restrict__ b1,
                                                   u16* __restrict__ act) {
  __shared__ __attribute__((aligned(16))) char smem[3 * G1_STAGE];
  const int tid = threadIdx.x, lane = tid & 63, w8 = __builtin_amdgcn_readfirstlane(tid >> 6), w = w8 & 3;
  const int wm = w >> 1, wn = w & 1;
  const int bid = blockIdx.x, xcd = bid & 7, idx = bid >> 3;
  const int tm = (xcd & 1) * 8 + (idx & 7), tn = (xcd >> 1) * 4 + (idx >> 3);
  const int m0 = tm * 128, n0 = tn * 224;
  uint32_t aoff[4], boff[7];
#pragma unroll
  for (int i = 0; i < 4; ++i) {
    const int r = w * 32 + i * 8 + (lane >> 3);
    aoff[i] = (uint32_t)(m0 + r) * 2048u + (uint32_t)(((lane & 7) ^ (r & 7)) << 4);
  }
#pragma unroll
  for (int i = 0; i < 7; ++i) {
    const int d = i * 64 + lane, r = d / 28, q = d - r * 28, k = w * 16 + r;
    const int c = (q >> 1) ^ ((k >> 3) & 1);
    boff[i] = (uint32_t)k * (uint32_t)(F_ * 2) + (uint32_t)(n0 * 2) + (uint32_t)(((c << 1) | (q & 1)) << 4);
  }
  const char* Ab = reinterpret_cast<const char*>(A);
  const char* Wb = reinterpret_cast<const char*>(W);
  auto issue1 = [&](int i, int kt, int buf) {
    if (i < 4) {
      __builtin_amdgcn_global_load_lds((const unsigned*)(Ab + kt * 128 + aoff[i]),
                                       (unsigned*)(smem + buf * G1_STAGE + w * 4096 + lane * 16 + i * 1024), 16, 0, 0);
    } else {
      __builtin_amdgcn_global_load_lds((const unsigned*)(Wb + (size_t)kt * (64 * F_ * 2) + boff[i - 4]),
                                       (unsigned*)(smem + buf * G1_STAGE + G1_A_BYTES + w * 7168 + lane * 16 + (i - 4) * 1024), 16, 0, 0);
    }
  };
  auto g1_store = [&]() {
    WAIT_L0();
    RAW_BAR();
#pragma unroll
    for (int i = 0; i < 7; ++i) {
      const int c = tid + 512 * i, r = c / 28, q = c - r * 28;
      const u32v4 v = *reinterpret_cast<const u32v4*>(smem + G1_OT + r * G1_OTS + q * 16);
      *reinterpret_cast<u32v4*>(reinterpret_cast<char*>(act) + (size_t)(m0 + r) * (F_ * 2) + n0 * 2 + q * 16) = v;
    }
  };
  constexpr int NK = 16;
  if (w8 >= 4) {
#pragma unroll
    for (int i = 0; i < 11; ++i) issue1(i, 0, 0);
#pragma unroll
    for (int i = 0; i < 11; ++i) issue1(i, 1, 1);
#pragma unroll
    for (int i = 0; i < 11; ++i) issue1(i, 2, 2);
    WAIT_V(22); RAW_BAR();
    int buf = 0;
    for (int kt = 0; kt < NK - 3; ++kt) {
      WAIT_V(11); RAW_BAR();
#pragma unroll
      for (int i = 0; i < 11; ++i) issue1(i, kt + 3, buf);
      buf = buf == 2 ? 0 : buf + 1;
    }
    WAIT_V(11); RAW_BAR();
    WAIT_V(0); RAW_BAR();
    uint2 bb[7];
#pragma unroll
    for (int ni = 0; ni < 7; ++ni) bb[ni] = *reinterpret_cast<const uint2*>(b1 + n0 + wn * 112 + ni * 16 + 4 * (lane >> 4));
    RAW_BAR(); RAW_BAR();
    const f32x4* xfer = reinterpret_cast<const f32x4*>(smem) + w * (14 * 64) + lane;
    char* ot = smem + G1_OT + (wm * 64 + (lane & 15)) * G1_OTS + (wn * 112 + 4 * (lane >> 4)) * 2;
#pragma unroll
    for (int ni = 0; ni < 7; ++ni) {
      const f32x2 b01 = unpk(bb[ni].x), b23 = unpk(bb[ni].y);
#pragma unroll
      for (int mi = 2; mi < 4; ++mi) {
        const f32x4 c = xfer[((mi - 2) * 7 + ni) * 64];
        *reinterpret_cast<uint2*>(ot + mi * 16 * G1_OTS + ni * 32) = make_uint2(mish_pair(c[0], c[1], b01), mish_pair(c[2], c[3], b23));
      }
    }
    g1_store();
    return;
  }
  uint2 bb[7];
#pragma unroll
  for (int ni = 0; ni < 7; ++ni) bb[ni] = *reinterpret_cast<const uint2*>(b1 + n0 + wn * 112 + ni * 16 + 4 * (lane >> 4));
  f32x4 acc[4][7];
#pragma unroll
  for (int i = 0; i < 4; ++i)
#pragma unroll
    for (int j = 0; j < 7; ++j) acc[i][j] = (f32x4){0.f, 0.f, 0.f, 0.f};
  const int fr = lane & 15, fg = lane >> 4;
  const uint32_t sbase = (uint32_t)(size_t)(smem);
  const uint32_t ard0 = sbase + (wm * 64 + fr) * 128 + ((fg ^ (fr & 7)) << 4);
  const int q4 = (lane & 15) >> 2, p4 = lane & 3;
  const uint32_t brd = sbase + G1_A_BYTES + (8 * fg + q4) * 448 + 8 * p4 + wn * 224;
  const int f32_ = (fg & 1) * 32;
  const uint32_t brdE = brd + ((wn & 1) ? -f32_ : f32_), brdO = brd + ((wn & 1) ? f32_ : -f32_);
  G1Frag X, Y;
  auto rd = [&](G1Frag& Fr, auto I, auto KS, uint32_t so) {
    constexpr int i = decltype(I)::v, ks = decltype(KS)::v;
    if constexpr (i < 4) {
      lds_rd128<i * 2048>(Fr.a[i], (ard0 ^ (ks * 64)) + so);
    } else {
      constexpr int ni = (i - 4) >> 1, t = (i - 4) & 1;
      lds_rdtr<ks * 32 * 448 + ni * 32 + t * 4 * 448>(Fr.b[ni][t], ((ni & 1) ? brdO : brdE) + so);
    }
  };
  auto mma1 = [&](G1Frag& Fr, auto J) {
    constexpr int j = decltype(J)::v, mi = j / 7, ni = j % 7;
    mfma16(acc[mi][ni], Fr.b[ni][0], Fr.b[ni][1], Fr.a[mi]);
  };
#define G1_TIE(Fr) asm volatile("s_waitcnt lgkmcnt(0)" : "+v"(Fr.a[0]), "+v"(Fr.a[1]), "+v"(Fr.a[2]), "+v"(Fr.a[3]), \
    "+v"(Fr.b[0][0]), "+v"(Fr.b[0][1]), "+v"(Fr.b[1][0]), "+v"(Fr.b[1][1]), "+v"(Fr.b[2][0]), "+v"(Fr.b[2][1]), "+v"(Fr.b[3][0]), "+v"(Fr.b[3][1]), \
    "+v"(Fr.b[4][0]), "+v"(Fr.b[4][1]), "+v"(Fr.b[5][0]), "+v"(Fr.b[5][1]), "+v"(Fr.b[6][0]), "+v"(Fr.b[6][1]) :: "memory")
  auto block = [&](G1Frag& C, G1Frag& R, auto KS, uint32_t so, auto DO_RD, auto DO_IS, int kt_is, int buf_is) {
    auto one = [&](auto J) {
      constexpr int j = decltype(J)::v;
      mma1(C, J);
      if constexpr (decltype(DO_RD)::v && j < 18) rd(R, J, KS, so);
      if constexpr (decltype(DO_IS)::v && j >= 6 && j < 28 && ((j - 6) & 1) == 0) issue1((j - 6) >> 1, kt_is, buf_is);
      SCHED0();
    };
    one(IC<0>{}); one(IC<1>{}); one(IC<2>{}); one(IC<3>{}); one(IC<4>{}); one(IC<5>{}); one(IC<6>{});
    one(IC<7>{}); one(IC<8>{}); one(IC<9>{}); one(IC<10>{}); one(IC<11>{}); one(IC<12>{}); one(IC<13>{});
    one(IC<14>{}); one(IC<15>{}); one(IC<16>{}); one(IC<17>{}); one(IC<18>{}); one(IC<19>{}); one(IC<20>{});
    one(IC<21>{}); one(IC<22>{}); one(IC<23>{}); one(IC<24>{}); one(IC<25>{}); one(IC<26>{}); one(IC<27>{});
  };
  auto rdall = [&](G1Frag& R, uint32_t so) {
    rd(R, IC<0>{}, IC<0>{}, so); rd(R, IC<1>{}, IC<0>{}, so); rd(R, IC<2>{}, IC<0>{}, so); rd(R, IC<3>{}, IC<0>{}, so);
    rd(R, IC<4>{}, IC<0>{}, so); rd(R, IC<5>{}, IC<0>{}, so); rd(R, IC<6>{}, IC<0>{}, so); rd(R, IC<7>{}, IC<0>{}, so);
    rd(R, IC<8>{}, IC<0>{}, so); rd(R, IC<9>{}, IC<0>{}, so); rd(R, IC<10>{}, IC<0>{}, so); rd(R, IC<11>{}, IC<0>{}, so);
    rd(R, IC<12>{}, IC<0>{}, so); rd(R, IC<13>{}, IC<0>{}, so); rd(R, IC<14>{}, IC<0>{}, so); rd(R, IC<15>{}, IC<0>{}, so);
    rd(R, IC<16>{}, IC<0>{}, so); rd(R, IC<17>{}, IC<0>{}, so);
  };
  RAW_BAR();
  rdall(X, 0);
  int buf = 0;
  for (int kt = 0; kt < NK - 3; ++kt) {
    const int nbuf = buf == 2 ? 0 : buf + 1;
    G1_TIE(X); SCHED0();
    block(X, Y, IC<1>{}, buf * G1_STAGE, IC<1>{}, IC<0>{}, 0, 0);
    G1_TIE(Y); RAW_BAR(); SCHED0();
    block(Y, X, IC<0>{}, nbuf * G1_STAGE, IC<1>{}, IC<0>{}, 0, 0);
    buf = nbuf;
  }
  G1_TIE(X); SCHED0();
  block(X, Y, IC<1>{}, 1 * G1_STAGE, IC<1>{}, IC<0>{}, 0, 0);
  G1_TIE(Y); RAW_BAR(); SCHED0();
  block(Y, X, IC<0>{}, 2 * G1_STAGE, IC<1>{}, IC<0>{}, 0, 0);
  G1_TIE(X); SCHED0();
  block(X, Y, IC<1>{}, 2 * G1_STAGE, IC<1>{}, IC<0>{}, 0, 0);
  G1_TIE(Y); RAW_BAR(); SCHED0();
  block(Y, X, IC<0>{}, 0 * G1_STAGE, IC<1>{}, IC<0>{}, 0, 0);
  G1_TIE(X); SCHED0();
  block(X, Y, IC<1>{}, 0 * G1_STAGE, IC<1>{}, IC<0>{}, 0, 0);
  G1_TIE(Y); SCHED0();
  block(Y, X, IC<0>{}, 0, IC<0>{}, IC<0>{}, 0, 0);
  asm volatile("s_nop 15\n\ts_nop 15" : "+v"(acc[0][0]), "+v"(acc[0][1]), "+v"(acc[0][2]), "+v"(acc[0][3]), "+v"(acc[0][4]), "+v"(acc[0][5]), "+v"(acc[0][6]), "+v"(acc[1][0]), "+v"(acc[1][1]), "+v"(acc[1][2]), "+v"(acc[1][3]), "+v"(acc[1][4]), "+v"(acc[1][5]), "+v"(acc[1][6]), "+v"(acc[2][0]), "+v"(acc[2][1]), "+v"(acc[2][2]), "+v"(acc[2][3]), "+v"(acc[2][4]), "+v"(acc[2][5]), "+v"(acc[2][6]), "+v"(acc[3][0]), "+v"(acc[3][1]), "+v"(acc[3][2]), "+v"(acc[3][3]), "+v"(acc[3][4]), "+v"(acc[3][5]), "+v"(acc[3][6]) :: "memory");
  SCHED0();
  RAW_BAR();
  {
    f32x4* xfer = reinterpret_cast<f32x4*>(smem) + w * (14 * 64) + lane;
#pragma unroll
    for (int mi = 2; mi < 4; ++mi)
#pragma unroll
      for (int ni = 0; ni < 7; ++ni) xfer[((mi - 2) * 7 + ni) * 64] = acc[mi][ni];
  }
  WAIT_L0();
  RAW_BAR();
  char* ot = smem + G1_OT + (wm * 64 + fr) * G1_OTS + (wn * 112 + 4 * fg) * 2;
#pragma unroll
  for (int ni = 0; ni < 7; ++ni) {
    const f32x2 b01 = unpk(bb[ni].x), b23 = unpk(bb[ni].y);
#pragma unroll
    for (int mi = 0; mi < 2; ++mi) {
      const f32x4 c = acc[mi][ni];
      *reinterpret_cast<uint2*>(ot + mi * 16 * G1_OTS + ni * 32) = make_uint2(mish_pair(c[0], c[1], b01), mish_pair(c[2], c[3], b23));
    }
  }
  g1_store();
}

constexpr int G2_A_BYTES = 128 * 128, G2_B_BYTES = 64 * 256, G2_STAGE = G2_A_BYTES + G2_B_BYTES;
struct G2Frag { u32x4 a[4]; u32x2 b[4][2]; };
typedef __attribute__((address_space(1))) unsigned int gu32;
__global__ __launch_bounds__(512, 2) void g2_gemm(const u16* __restrict__ A, const u16* __restrict__ W, const u16* __restrict__ h,
                                                   u16* __restrict__ out, float* __restrict__ part, unsigned* __restrict__ flags) {
  __shared__ __attribute__((aligned(16))) char smem[4 * G2_STAGE];
  const int tid = threadIdx.x, lane = tid & 63, w8 = __builtin_amdgcn_readfirstlane(tid >> 6), w = w8 & 3;
  const int wm = w >> 1, wn = w & 1;
  const int bid = blockIdx.x, xcd = bid & 7, idx = bid >> 3;
  const int kh = xcd & 1, tm = ((xcd >> 1) & 1) * 8 + (idx & 7), tn = (xcd >> 2) * 4 + (idx >> 3);
  const int m0 = tm * 128, n0 = tn * 128, tile = tm * 8 + tn;
  auto g2_store = [&]() {
    WAIT_L0();
    RAW_BAR();
#pragma unroll
    for (int i = 0; i < 2; ++i) {
      const int c = tid + 512 * i, r = c >> 4, q = c & 15;
      const int row = (r >> 5) * 64 + kh * 32 + (r & 31);
      const uint4 v = *reinterpret_cast<const uint4*>(smem + r * 272 + q * 16);
      *reinterpret_cast<uint4*>(reinterpret_cast<char*>(out) + (size_t)(m0 + row) * (D_ * 2) + n0 * 2 + q * 16) = v;
    }
  };
  constexpr int NK = 28;
  if (w8 >= 4) {
    uint32_t aoff[4], boff[4];
#pragma unroll
    for (int i = 0; i < 4; ++i) {
      const int r = w * 32 + i * 8 + (lane >> 3);
      aoff[i] = (uint32_t)(m0 + r) * (uint32_t)(F_ * 2) + (uint32_t)(((lane & 7) ^ (r & 7)) << 4);
    }
#pragma unroll
    for (int i = 0; i < 4; ++i) {
      const int k = w * 16 + i * 4 + (lane >> 4), q = lane & 15;
      const int c = (q >> 1) ^ ((k & 3) | (((k >> 3) & 1) << 2));
      boff[i] = (uint32_t)k * (uint32_t)(D_ * 2) + (uint32_t)(n0 * 2) + (uint32_t)(((c << 1) | (q & 1)) << 4);
    }
    const char* Ab = reinterpret_cast<const char*>(A) + kh * (1792 * 2);
    const char* Wb = reinterpret_cast<const char*>(W) + (size_t)kh * (1792 * D_ * 2);
    auto issue = [&](int kt, int buf) {
#pragma unroll
      for (int i = 0; i < 4; ++i)
        __builtin_amdgcn_global_load_lds((const unsigned*)(Ab + kt * 128 + aoff[i]),
                                         (unsigned*)(smem + buf * G2_STAGE + w * 4096 + lane * 16 + i * 1024), 16, 0, 0);
#pragma unroll
      for (int i = 0; i < 4; ++i)
        __builtin_amdgcn_global_load_lds((const unsigned*)(Wb + (size_t)kt * (64 * D_ * 2) + boff[i]),
                                         (unsigned*)(smem + buf * G2_STAGE + G2_A_BYTES + w * 4096 + lane * 16 + i * 1024), 16, 0, 0);
    };
    issue(0, 0); issue(1, 1); issue(2, 2); issue(3, 3);
    WAIT_V(24); RAW_BAR();
    for (int kt = 0; kt < NK - 4; ++kt) {
      WAIT_V(16); RAW_BAR();
      issue(kt + 4, kt & 3);
    }
    WAIT_V(16); RAW_BAR();
    WAIT_V(8); RAW_BAR();
    WAIT_V(0); RAW_BAR();
    RAW_BAR();
    RAW_BAR();
    g2_store();
    return;
  }
  const int fr = lane & 15, fg = lane >> 4;
  uint2 hv[2][4];
#pragma unroll
  for (int i = 0; i < 2; ++i)
#pragma unroll
    for (int ni = 0; ni < 4; ++ni)
      hv[i][ni] = *reinterpret_cast<const uint2*>(h + (size_t)(m0 + wm * 64 + (2 * kh + i) * 16 + fr) * D_ + n0 + wn * 64 + ni * 16 + 4 * fg);
  f32x4 acc[4][4];
#pragma unroll
  for (int i = 0; i < 4; ++i)
#pragma unroll
    for (int j = 0; j < 4; ++j) acc[i][j] = (f32x4){0.f, 0.f, 0.f, 0.f};
  const uint32_t sbase = (uint32_t)(size_t)(smem);
  const uint32_t ard0 = sbase + (wm * 64 + fr) * 128 + ((fg ^ (fr & 7)) << 4);
  const int q4 = (lane & 15) >> 2, p4 = lane & 3;
  const int bsw = q4 | ((fg & 1) << 2);
  const uint32_t brd = sbase + G2_A_BYTES + (8 * fg + q4) * 256 + 8 * p4;
  uint32_t brdn[4];
#pragma unroll
  for (int ni = 0; ni < 4; ++ni) brdn[ni] = brd + (((wn * 4 + ni) ^ bsw) * 32);
  G2Frag X, Y;
  auto rd = [&](G2Frag& Fr, auto I, auto KS, uint32_t so) {
    constexpr int i = decltype(I)::v, ks = decltype(KS)::v;
    if constexpr (i < 4) {
      lds_rd128<i * 2048>(Fr.a[i], (ard0 ^ (ks * 64)) + so);
    } else {
      constexpr int ni = (i - 4) >> 1, t = (i - 4) & 1;
      lds_rdtr<ks * 32 * 256 + t * 4 * 256>(Fr.b[ni][t], brdn[ni] + so);
    }
  };
  auto mma1 = [&](G2Frag& Fr, auto J) {
    constexpr int j = decltype(J)::v, mi = j / 4, ni = j % 4;
    mfma16(acc[mi][ni], Fr.b[ni][0], Fr.b[ni][1], Fr.a[mi]);
  };
#define G2_TIE(Fr) asm volatile("s_waitcnt lgkmcnt(0)" : "+v"(Fr.a[0]), "+v"(Fr.a[1]), "+v"(Fr.a[2]), "+v"(Fr.a[3]), \
    "+v"(Fr.b[0][0]), "+v"(Fr.b[0][1]), "+v"(Fr.b[1][0]), "+v"(Fr.b[1][1]), "+v"(Fr.b[2][0]), "+v"(Fr.b[2][1]), "+v"(Fr.b[3][0]), "+v"(Fr.b[3][1]) :: "memory")
  auto block = [&](G2Frag& C, G2Frag& R, auto KS, uint32_t so, auto DO_RD) {
    auto one = [&](auto J) {
      constexpr int j = decltype(J)::v;
      mma1(C, J);
      if constexpr (decltype(DO_RD)::v && j < 12) rd(R, J, KS, so);
      SCHED0();
    };
    one(IC<0>{}); one(IC<1>{}); one(IC<2>{}); one(IC<3>{}); one(IC<4>{}); one(IC<5>{}); one(IC<6>{}); one(IC<7>{});
    one(IC<8>{}); one(IC<9>{}); one(IC<10>{}); one(IC<11>{}); one(IC<12>{}); one(IC<13>{}); one(IC<14>{}); one(IC<15>{});
  };
  RAW_BAR();
  rd(X, IC<0>{}, IC<0>{}, 0); rd(X, IC<1>{}, IC<0>{}, 0); rd(X, IC<2>{}, IC<0>{}, 0); rd(X, IC<3>{}, IC<0>{}, 0);
  rd(X, IC<4>{}, IC<0>{}, 0); rd(X, IC<5>{}, IC<0>{}, 0); rd(X, IC<6>{}, IC<0>{}, 0); rd(X, IC<7>{}, IC<0>{}, 0);
  rd(X, IC<8>{}, IC<0>{}, 0); rd(X, IC<9>{}, IC<0>{}, 0); rd(X, IC<10>{}, IC<0>{}, 0); rd(X, IC<11>{}, IC<0>{}, 0);
  for (int kt = 0; kt < NK - 1; ++kt) {
    G2_TIE(X); SCHED0();
    block(X, Y, IC<1>{}, (kt & 3) * G2_STAGE, IC<1>{});
    G2_TIE(Y); RAW_BAR(); SCHED0();
    block(Y, X, IC<0>{}, ((kt + 1) & 3) * G2_STAGE, IC<1>{});
  }
  G2_TIE(X); SCHED0();
  block(X, Y, IC<1>{}, ((NK - 1) & 3) * G2_STAGE, IC<1>{});
  G2_TIE(Y); SCHED0();
  block(Y, X, IC<0>{}, 0, IC<0>{});
  asm volatile("s_nop 15\n\ts_nop 15" : "+v"(acc[0][0]), "+v"(acc[0][1]), "+v"(acc[0][2]), "+v"(acc[0][3]), "+v"(acc[1][0]), "+v"(acc[1][1]), "+v"(acc[1][2]), "+v"(acc[1][3]), "+v"(acc[2][0]), "+v"(acc[2][1]), "+v"(acc[2][2]), "+v"(acc[2][3]), "+v"(acc[3][0]), "+v"(acc[3][1]), "+v"(acc[3][2]), "+v"(acc[3][3]) :: "memory");
  SCHED0();
  const auto rs = __builtin_amdgcn_make_buffer_rsrc(part, 0, 256 * 32768, 0x00020000);
  const uint32_t myoff = (uint32_t)(tile * 2 + kh) * 32768u + (uint32_t)w * 8192u + (uint32_t)lane * 16u;
  const uint32_t proff = (uint32_t)(tile * 2 + (kh ^ 1)) * 32768u + (uint32_t)w * 8192u + (uint32_t)lane * 16u;
  auto send = [&](const f32x4 (&r0)[4], const f32x4 (&r1)[4]) {
#pragma unroll
    for (int ni = 0; ni < 4; ++ni) {
      __builtin_amdgcn_raw_buffer_store_b128(__builtin_bit_cast(u32x4, r0[ni]), rs, myoff + ni * 1024, 0, 16);
      __builtin_amdgcn_raw_buffer_store_b128(__builtin_bit_cast(u32x4, r1[ni]), rs, myoff + 4096 + ni * 1024, 0, 16);
    }
  };
  if (kh == 0) send(acc[2], acc[3]); else send(acc[0], acc[1]);
  WAIT_V(0);
  RAW_BAR();
  if (w == 0) {
    if (lane == 0) {
      __hip_atomic_store((gu32*)(flags + (tile * 2 + kh) * 16), 1u, __ATOMIC_RELAXED, __HIP_MEMORY_SCOPE_AGENT);
      for (int spin = 0; spin < (1 << 22); ++spin) {
        if (__hip_atomic_load((gu32*)(flags + (tile * 2 + (kh ^ 1)) * 16), __ATOMIC_RELAXED, __HIP_MEMORY_SCOPE_AGENT) == 1u) break;
        __builtin_amdgcn_s_sleep(1);
      }
    }
  }
  asm volatile("" ::: "memory");
  RAW_BAR();
  asm volatile("" ::: "memory");
  auto fin = [&](auto I, const f32x4 (&own)[4]) {
    constexpr int i = decltype(I)::v;
    char* ot = smem + (wm * 32 + i * 16 + fr) * 272 + wn * 128 + 8 * fg;
    f32x4 pr[4];
#pragma unroll
    for (int ni = 0; ni < 4; ++ni) pr[ni] = __builtin_bit_cast(f32x4, __builtin_amdgcn_raw_buffer_load_b128(rs, proff + i * 4096 + ni * 1024, 0, 16));
#pragma unroll
    for (int ni = 0; ni < 4; ++ni) {
      const f32x4 c = kh == 0 ? own[ni] + pr[ni] : pr[ni] + own[ni];
      const f32x2 r01 = unpk(hv[i][ni].x) + unpk(cvt_pk(c[0], c[1])), r23 = unpk(hv[i][ni].y) + unpk(cvt_pk(c[2], c[3]));
      *reinterpret_cast<uint2*>(ot + ni * 32) = make_uint2(cvt_pk(r01[0], r01[1]), cvt_pk(r23[0], r23[1]));
    }
  };
  if (kh == 0) { fin(IC<0>{}, acc[0]); fin(IC<1>{}, acc[1]); }
  else { fin(IC<0>{}, acc[2]); fin(IC<1>{}, acc[3]); }
  g2_store();
}

#define G1_LAUNCH(st) g1_gemm<<<256, 512, 0, st>>>(da, dw1, db1, dact)
#define G2_LAUNCH(st) do { (void)hipMemsetAsync(dflags, 0, 16384, st); g2_gemm<<<256, 512, 0, st>>>(dact, dw2, dh, dout, dpart, dflags); } while (0)
extern "C" void kernel_launch(void* const* d_in, const int* in_sizes, int n_in,
                              void* d_out, int out_size, void* d_ws, size_t ws_size,
                              hipStream_t stream) {
  const u16* x = (const u16*)d_in[0];
  const u16* wr = (const u16*)d_in[1];
  const u16* wi = (const u16*)d_in[2];
  const u16* w1 = (const u16*)d_in[4];
  const u16* b1 = (const u16*)d_in[5];
  const u16* w2 = (const u16*)d_in[6];
  u16* out = (u16*)d_out;
  char* ws = (char*)d_ws;
  c2* T = (c2*)ws;
  uint32_t* WT = (uint32_t*)(ws + (9u << 20));
  u16* h = (u16*)(ws + (16u << 20));
  u16* a = (u16*)(ws + (20u << 20));
  u16* act = (u16*)(ws + (24u << 20));
  float* part = (float*)(ws + (40u << 20));
  unsigned* flags = (unsigned*)(ws + (48u << 20));
  k1_ln_rowfft<<<256, 512, 0, stream>>>(x, wr, wi, T, WT, flags);
  c2* U = (c2*)(ws + (50u << 20));
  k2_colfft<<<512, 256, 0, stream>>>(T, U, WT, (const char*)w1);
  k3_irowfft_ln<<<256, 512, 0, stream>>>(U, x, h, a, (const char*)w2);
  g1_gemm<<<256, 512, 0, stream>>>(a, w1, b1, act);
  g2_gemm<<<256, 512, 0, stream>>>(act, w2, h, out, part, flags);
}
```

```cpp
#include <hip/hip_runtime.h>
#include <hip/hip_bf16.h>
#include <stdint.h>

typedef unsigned short u16;
typedef __attribute__((ext_vector_type(8))) short bf16x8;
typedef __attribute__((ext_vector_type(4))) short bf16x4;
typedef __attribute__((ext_vector_type(4))) float f32x4;
typedef __attribute__((address_space(3))) bf16x4* lds_b4_ptr;

constexpr int TBS = 4096 + 16;
constexpr int S_ = 2048, D_ = 1024, F_ = 3584, DF_ = 513;

__device__ __forceinline__ float bf2f(u16 v) { return __uint_as_float(((uint32_t)v) << 16); }
__device__ __forceinline__ u16 f2bf(float f) { __bf16 b = (__bf16)f; return __builtin_bit_cast(u16, b); }
__device__ __forceinline__ float rbf(float f) { return bf2f(f2bf(f)); }

typedef __attribute__((ext_vector_type(4))) unsigned int u32v4;
typedef __attribute__((ext_vector_type(4))) float f32v4;
template <typename T> __device__ __forceinline__ void st_nt(T* p, T v) { __builtin_nontemporal_store(v, p); }
typedef __attribute__((ext_vector_type(2))) float c2;
typedef c2 f32x2;
__device__ __forceinline__ uint32_t cvt_pk(float lo, float hi) { uint32_t r; asm("v_cvt_pk_bf16_f32 %0, %1, %2" : "=v"(r) : "v"(lo), "v"(hi)); return r; }
__device__ __forceinline__ c2 unpk(uint32_t u) { return (c2){__uint_as_float(u << 16), __uint_as_float(u & 0xffff0000u)}; }
__device__ __forceinline__ c2 rbf2(c2 v) { return unpk(cvt_pk(v.x, v.y)); }
__device__ __forceinline__ c2 cmul(c2 a, c2 b) {
  c2 t, r;
  asm("v_pk_mul_f32 %0, %1, %2 op_sel:[0,0] op_sel_hi:[0,1]" : "=v"(t) : "v"(a), "v"(b));
  asm("v_pk_fma_f32 %0, %1, %2, %3 op_sel:[1,1,0] op_sel_hi:[1,0,1] neg_lo:[0,1,0]" : "=v"(r) : "v"(a), "v"(b), "v"(t));
  return r;
}
__device__ __forceinline__ c2 sub_i(c2 t, c2 d) { c2 r; asm("v_pk_add_f32 %0, %1, %2 op_sel:[0,1] op_sel_hi:[1,0] neg_hi:[0,1]" : "=v"(r) : "v"(t), "v"(d)); return r; }
__device__ __forceinline__ c2 add_i(c2 t, c2 d) { c2 r; asm("v_pk_add_f32 %0, %1, %2 op_sel:[0,1] op_sel_hi:[1,0] neg_lo:[0,1]" : "=v"(r) : "v"(t), "v"(d)); return r; }
__device__ __forceinline__ c2 add_c(c2 a, c2 b) { c2 r; asm("v_pk_add_f32 %0, %1, %2 neg_hi:[0,1]" : "=v"(r) : "v"(a), "v"(b)); return r; }
__device__ __forceinline__ c2 sub_c(c2 a, c2 b) { c2 r; asm("v_pk_add_f32 %0, %1, %2 neg_lo:[0,1]" : "=v"(r) : "v"(a), "v"(b)); return r; }
template <int DIR> __device__ __forceinline__ c2 addD(c2 t, c2 d) { return DIR > 0 ? add_i(t, d) : sub_i(t, d); }
template <int DIR> __device__ __forceinline__ c2 subD(c2 t, c2 d) { return DIR > 0 ? sub_i(t, d) : add_i(t, d); }
template <int DIR>
__device__ __forceinline__ c2 cexp2pi(float rev) {
  const float r = DIR > 0 ? rev : -rev;
  c2 w = {__builtin_amdgcn_cosf(r), __builtin_amdgcn_sinf(r)};
  asm("s_nop 0" : "+v"(w));
  return w;
}
template <int DIR>
__device__ __forceinline__ void dft4(c2& x0, c2& x1, c2& x2, c2& x3) {
  const c2 t0 = x0 + x2, t1 = x0 - x2, t2 = x1 + x3, d = x1 - x3;
  x0 = t0 + t2; x2 = t0 - t2; x1 = addD<DIR>(t1, d); x3 = subD<DIR>(t1, d);
}
template <int DIR>
__device__ __forceinline__ void dft8(c2 (&v)[8]) {
  c2 e0 = v[0], e1 = v[2], e2 = v[4], e3 = v[6];
  c2 o0 = v[1], o1 = v[3], o2 = v[5], o3 = v[7];
  dft4<DIR>(e0, e1, e2, e3);
  dft4<DIR>(o0, o1, o2, o3);
  const c2 r = {0.70710678118654752440f, 0.70710678118654752440f};
  const c2 u1 = addD<DIR>(o1, o1), u3 = subD<DIR>(o3, o3);
  v[0] = e0 + o0; v[4] = e0 - o0;
  v[1] = __builtin_elementwise_fma(u1, r, e1); v[5] = __builtin_elementwise_fma(u1, -r, e1);
  v[2] = addD<DIR>(e2, o2); v[6] = subD<DIR>(e2, o2);
  v[3] = __builtin_elementwise_fma(u3, -r, e3); v[7] = __builtin_elementwise_fma(u3, r, e3);
}
__device__ __forceinline__ void twiddle8(c2 (&v)[8], c2 w) {
  const c2 w2 = cmul(w, w), w3 = cmul(w2, w), w4 = cmul(w2, w2), w5 = cmul(w4, w), w6 = cmul(w3, w3), w7 = cmul(w4, w3);
  v[1] = cmul(v[1], w); v[2] = cmul(v[2], w2); v[3] = cmul(v[3], w3); v[4] = cmul(v[4], w4);
  v[5] = cmul(v[5], w5); v[6] = cmul(v[6], w6); v[7] = cmul(v[7], w7);
}
__device__ __forceinline__ void twiddle8b(c2 (&v)[8], c2 b, c2 w) {
  const c2 w2 = cmul(w, w), w4 = cmul(w2, w2);
  const c2 b1 = cmul(b, w), b2 = cmul(b, w2), b3 = cmul(b1, w2), b4 = cmul(b, w4), b5 = cmul(b1, w4), b6 = cmul(b2, w4), b7 = cmul(b3, w4);
  v[0] = cmul(v[0], b); v[1] = cmul(v[1], b1); v[2] = cmul(v[2], b2); v[3] = cmul(v[3], b3);
  v[4] = cmul(v[4], b4); v[5] = cmul(v[5], b5); v[6] = cmul(v[6], b6); v[7] = cmul(v[7], b7);
}

__device__ __forceinline__ float wave_sum(float v) {
#define DPP_ADD(ctrl, rmask) v += __uint_as_float(__builtin_amdgcn_update_dpp(0u, __float_as_uint(v), ctrl, rmask, 0xf, true))
  DPP_ADD(0x111, 0xf); DPP_ADD(0x112, 0xf); DPP_ADD(0x114, 0xf); DPP_ADD(0x118, 0xf);
  DPP_ADD(0x142, 0xa); DPP_ADD(0x143, 0xc);
#undef DPP_ADD
  return __uint_as_float(__builtin_amdgcn_readlane(__float_as_uint(v), 63));
}

#define WAVE_SYNC() do { __builtin_amdgcn_fence(__ATOMIC_RELEASE, "wavefront"); __builtin_amdgcn_wave_barrier(); __builtin_amdgcn_fence(__ATOMIC_ACQUIRE, "wavefront"); } while (0)

template <int DIR>
__device__ __forceinline__ void fft512_wave(c2 (&v)[8], c2* ex, int lane) {
  const int hi = lane >> 3, lo = lane & 7;
  dft8<DIR>(v);
  twiddle8(v, cexp2pi<DIR>((float)hi * (1.0f / 64.0f)));
#pragma unroll
  for (int k0 = 0; k0 < 8; ++k0) ex[72 * k0 + lane] = v[k0];
  WAVE_SYNC();
#pragma unroll
  for (int b = 0; b < 8; ++b) v[b] = ex[72 * hi + 8 * b + lo];
  WAVE_SYNC();
  dft8<DIR>(v);
  twiddle8b(v, cexp2pi<DIR>((float)(lo * hi) * (1.0f / 512.0f)), cexp2pi<DIR>((float)lo * (1.0f / 64.0f)));
#pragma unroll
  for (int k1 = 0; k1 < 8; ++k1) ex[66 * lo + 8 * k1 + hi] = v[k1];
  WAVE_SYNC();
#pragma unroll
  for (int c = 0; c < 8; ++c) v[c] = ex[66 * c + lane];
  WAVE_SYNC();
  dft8<DIR>(v);
}

__device__ __forceinline__ void ln_bf16(c2 (&e)[8], uint32_t (&o)[8]) {
  c2 s2 = ((e[0] + e[1]) + (e[2] + e[3])) + ((e[4] + e[5]) + (e[6] + e[7]));
  const float mean = wave_sum(s2.x + s2.y) * (1.0f / 1024.0f);
  c2 q2 = {0.f, 0.f};
#pragma unroll
  for (int i = 0; i < 8; ++i) { const c2 d = e[i] - mean; q2 = __builtin_elementwise_fma(d, d, q2); }
  const float var = wave_sum(q2.x + q2.y) * (1.0f / 1024.0f);
  const float mb = rbf(mean);
  const float sd = rbf(sqrtf(rbf(rbf(var) + 1e-5f)));
  const float ri = 1.0f / sd;
#pragma unroll
  for (int i = 0; i < 8; ++i) {
    const c2 d = rbf2(e[i] - mb);
    const c2 q = d * ri;
    const c2 rem = __builtin_elementwise_fma(-q, (c2){sd, sd}, d);
    const c2 qq = __builtin_elementwise_fma(rem, (c2){ri, ri}, q);
    o[i] = cvt_pk(qq.x, qq.y);
  }
}

__global__ __launch_bounds__(512) void k1_ln_rowfft(const u16* __restrict__ x, const u16* __restrict__ wr, const u16* __restrict__ wi,
                                                     c2* __restrict__ T, uint32_t* __restrict__ WT, unsigned* __restrict__ flags) {
  __shared__ c2 ex_all[8 * 576];
  __shared__ c2 st[512 * 9];
  const int tid = threadIdx.x, lane = tid & 63, w = tid >> 6;
  const int s0 = blockIdx.x * 8;
  const int row = s0 + w;
  const uint32_t* xr = reinterpret_cast<const uint32_t*>(x + (size_t)row * D_);
  uint32_t xu[8];
#pragma unroll
  for (int a = 0; a < 8; ++a) xu[a] = xr[lane + 64 * a];
  if (tid == 0) flags[blockIdx.x * 16] = 0;
  {
    uint32_t p[8];
#pragma unroll
    for (int r = 0; r < 8; ++r) {
      const size_t o = (size_t)(s0 + r) * DF_ + tid;
      p[r] = (uint32_t)wr[o] | ((uint32_t)wi[o] << 16);
    }
    u32v4* dst = reinterpret_cast<u32v4*>(WT + (size_t)tid * S_ + s0);
    st_nt(dst, (u32v4){p[0], p[1], p[2], p[3]});
    st_nt(dst + 1, (u32v4){p[4], p[5], p[6], p[7]});
    if (tid < 8) {
      const size_t o = (size_t)(s0 + tid) * DF_ + 512;
      WT[(size_t)512 * S_ + s0 + tid] = (uint32_t)wr[o] | ((uint32_t)wi[o] << 16);
    }
  }
  c2 v[8];
#pragma unroll
  for (int a = 0; a < 8; ++a) v[a] = unpk(xu[a]);
  ln_bf16(v, xu);
#pragma unroll
  for (int a = 0; a < 8; ++a) v[a] = unpk(xu[a]);
  c2* ex = ex_all + w * 576;
  fft512_wave<-1>(v, ex, lane);
#pragma unroll
  for (int j = 4; j < 8; ++j) ex[lane + 64 * j] = v[j];
  ex[512 + lane] = v[0];
  WAVE_SYNC();
  const c2 tw0 = cexp2pi<-1>((float)lane * (1.0f / 1024.0f));
  const c2 half = {0.5f, 0.5f};
#pragma unroll
  for (int j = 0; j < 4; ++j) {
    const int k = lane + 64 * j;
    const c2 z = v[j], zp = ex[512 - k];
    const c2 cj = j == 0 ? (c2){1.f, 0.f} : j == 1 ? (c2){0.92387953251128674f, -0.38268343236508977f}
                : j == 2 ? (c2){0.70710678118654752f, -0.70710678118654752f} : (c2){0.38268343236508977f, -0.92387953251128674f};
    const c2 tw = j == 0 ? tw0 : cmul(tw0, cj);
    const c2 E = add_c(z, zp) * half, Dz = sub_c(z, zp) * half;
    const c2 P = cmul(tw, Dz);
    c2 Xa = sub_i(E, P), Xb = add_i(E, P);
    Xb.y = -Xb.y;
    if (k == 0) { Xa = (c2){z.x + z.y, z.x - z.y}; Xb = (c2){v[4].x, -v[4].y}; }
    st[k * 9 + w] = Xa;
    st[(k == 0 ? 256 : 512 - k) * 9 + w] = Xb;
  }
  __syncthreads();
#pragma unroll
  for (int it = 0; it < 4; ++it) {
    const int idx = it * 512 + tid;
    const int k = idx >> 2, part = idx & 3;
    const c2 a = st[k * 9 + 2 * part], b = st[k * 9 + 2 * part + 1];
    st_nt(reinterpret_cast<f32v4*>(T + (size_t)k * S_ + s0) + part, (f32v4){a.x, a.y, b.x, b.y});
  }
}

#define KEEP(v) asm volatile("" :: "v"(v))
__device__ __forceinline__ uint32_t wt_at(const uint32_t* WT, int k1, int c) { return WT[(size_t)c * S_ + k1]; }
__global__ __launch_bounds__(256) void k2_colfft(const c2* __restrict__ T, c2* __restrict__ U, const uint32_t* __restrict__ WT, const char* __restrict__ pf) {
  __shared__ c2 ex[2304];
  const int t = threadIdx.x, col = blockIdx.x;
  c2 v[8];
#pragma unroll
  for (int a = 0; a < 8; ++a) v[a] = T[(size_t)col * S_ + t + 256 * a];
  uint32_t wv[8];
#pragma unroll
  for (int r = 0; r < 8; ++r) wv[r] = wt_at(WT, t + 256 * r, col);
  uint32_t pfv = 0;
  if (t < 112) pfv = *reinterpret_cast<const uint32_t*>(pf + ((size_t)col * 112 + t) * 128);
  c2* Tg = U + (size_t)(t >> 3) * TBS + col * 8 + (t & 7);
  const int h5 = t >> 5, l5 = t & 31;
  const int m3 = (t >> 2) & 7, d2 = t & 3;
  dft8<-1>(v);
  twiddle8(v, cexp2pi<-1>((float)t * (1.0f / 2048.0f)));
#pragma unroll
  for (int k0 = 0; k0 < 8; ++k0) ex[256 * k0 + t] = v[k0];
  __syncthreads();
#pragma unroll
  for (int b = 0; b < 8; ++b) v[b] = ex[256 * h5 + 32 * b + l5];
  __syncthreads();
  dft8<-1>(v);
  twiddle8(v, cexp2pi<-1>((float)l5 * (1.0f / 256.0f)));
#pragma unroll
  for (int k1 = 0; k1 < 8; ++k1) ex[288 * h5 + 36 * k1 + l5] = v[k1];
  WAVE_SYNC();
#pragma unroll
  for (int c = 0; c < 8; ++c) v[c] = ex[288 * h5 + 36 * m3 + 4 * c + d2];
  __syncthreads();
  dft8<-1>(v);
  twiddle8(v, cexp2pi<-1>((float)d2 * (1.0f / 32.0f)));
#pragma unroll
  for (int k2 = 0; k2 < 8; ++k2) ex[(4 * k2 + d2) * 66 + 8 * m3 + h5] = v[k2];
  __syncthreads();
  const int dd = t >> 6, l6 = t & 63;
#pragma unroll
  for (int d = 0; d < 4; ++d) { v[d] = ex[(4 * dd + d) * 66 + l6]; v[4 + d] = ex[(4 * (dd + 4) + d) * 66 + l6]; }
  __syncthreads();
  dft4<-1>(v[0], v[1], v[2], v[3]);
  dft4<-1>(v[4], v[5], v[6], v[7]);
  if (col == 0) {
#pragma unroll
    for (int k3 = 0; k3 < 4; ++k3) { ex[t + 512 * k3] = v[k3]; ex[t + 256 + 512 * k3] = v[4 + k3]; }
    __syncthreads();
  }
  if (col != 0) {
#pragma unroll
    for (int k3 = 0; k3 < 4; ++k3) {
      v[k3] = cmul(v[k3], unpk(wv[2 * k3]));
      v[4 + k3] = cmul(v[4 + k3], unpk(wv[2 * k3 + 1]));
    }
  } else {
#pragma unroll
    for (int r = 0; r < 8; ++r) {
      const int vi = (r & 1) * 4 + (r >> 1);
      const int k = t + 256 * r, kn = (2048 - k) & 2047;
      const c2 p = v[vi], pn = ex[kn];
      const c2 F0 = {0.5f * (p.x + pn.x), 0.5f * (p.y - pn.y)};
      const c2 F5 = {0.5f * (p.y + pn.y), 0.5f * (pn.x - p.x)};
      const c2 wa = unpk(wv[r]), wan = unpk(wt_at(WT, kn, 0));
      const c2 wb = unpk(wt_at(WT, k, 512)), wbn = unpk(wt_at(WT, kn, 512));
      const c2 W0 = {0.5f * (wa.x + wan.x), 0.5f * (wa.y - wan.y)};
      const c2 W5 = {0.5f * (wb.x + wbn.x), 0.5f * (wb.y - wbn.y)};
      const c2 H0 = cmul(F0, W0), H5 = cmul(F5, W5);
      v[vi] = add_i(H0, H5);
    }
  }
  if (col == 0) __syncthreads();
  dft4<1>(v[0], v[1], v[2], v[3]);
  dft4<1>(v[4], v[5], v[6], v[7]);
  {
    const c2 wa = cexp2pi<1>((float)dd * (1.0f / 32.0f)), wb = cexp2pi<1>((float)(dd + 4) * (1.0f / 32.0f));
    const c2 wa2 = cmul(wa, wa), wa3 = cmul(wa2, wa), wb2 = cmul(wb, wb), wb3 = cmul(wb2, wb);
    v[1] = cmul(v[1], wa); v[2] = cmul(v[2], wa2); v[3] = cmul(v[3], wa3);
    v[5] = cmul(v[5], wb); v[6] = cmul(v[6], wb2); v[7] = cmul(v[7], wb3);
  }
#pragma unroll
  for (int d = 0; d < 4; ++d) { ex[(4 * dd + d) * 66 + l6] = v[d]; ex[(4 * (dd + 4) + d) * 66 + l6] = v[4 + d]; }
  __syncthreads();
#pragma unroll
  for (int k2 = 0; k2 < 8; ++k2) v[k2] = ex[(4 * k2 + d2) * 66 + 8 * m3 + h5];
  __syncthreads();
  dft8<1>(v);
  twiddle8b(v, cexp2pi<1>((float)(m3 * d2) * (1.0f / 256.0f)), cexp2pi<1>((float)m3 * (1.0f / 64.0f)));
#pragma unroll
  for (int c = 0; c < 8; ++c) ex[288 * h5 + 36 * m3 + 4 * c + d2] = v[c];
  WAVE_SYNC();
#pragma unroll
  for (int k1 = 0; k1 < 8; ++k1) v[k1] = ex[288 * h5 + 36 * k1 + l5];
  __syncthreads();
  dft8<1>(v);
  twiddle8b(v, cexp2pi<1>((float)(h5 * l5) * (1.0f / 2048.0f)), cexp2pi<1>((float)h5 * (1.0f / 64.0f)));
#pragma unroll
  for (int b = 0; b < 8; ++b) ex[256 * h5 + 32 * b + l5] = v[b];
  __syncthreads();
#pragma unroll
  for (int k0 = 0; k0 < 8; ++k0) v[k0] = ex[256 * k0 + t];
  dft8<1>(v);
#pragma unroll
  for (int a = 0; a < 8; ++a) st_nt(Tg + (size_t)a * (32 * TBS), v[a]);
  KEEP(pfv);
}

__global__ __launch_bounds__(512) void k3_irowfft_ln(const c2* __restrict__ T, const u16* __restrict__ x,
                                                      u16* __restrict__ h, u16* __restrict__ a_out, const char* __restrict__ pf) {
  __shared__ c2 ex_all[8 * 576];
  __shared__ c2 st[512 * 9];
  const int tid = threadIdx.x, lane = tid & 63, w = tid >> 6;
  const int s0 = blockIdx.x * 8;
  const int row = s0 + w;
  float4 q4[4];
#pragma unroll
  for (int it = 0; it < 4; ++it) {
    const int idx = it * 512 + tid;
    q4[it] = reinterpret_cast<const float4*>(T + (size_t)blockIdx.x * TBS)[idx];
  }
  const uint32_t* xr = reinterpret_cast<const uint32_t*>(x + (size_t)row * D_);
  uint32_t xv[8];
#pragma unroll
  for (int a = 0; a < 8; ++a) xv[a] = xr[lane + 64 * a];
  uint32_t pfv = 0;
  if (tid < 224) pfv = *reinterpret_cast<const uint32_t*>(pf + ((size_t)blockIdx.x * 224 + tid) * 128);
#pragma unroll
  for (int it = 0; it < 4; ++it) {
    const int idx = it * 512 + tid;
    const int k = idx >> 2, part = idx & 3;
    st[k * 9 + 2 * part] = (c2){q4[it].x, q4[it].y};
    st[k * 9 + 2 * part + 1] = (c2){q4[it].z, q4[it].w};
  }
  __syncthreads();
  c2 v[8];
  const c2 tw0 = cexp2pi<1>((float)lane * (1.0f / 1024.0f));
#pragma unroll
  for (int j = 0; j < 8; ++j) {
    const int k = lane + 64 * j;
    const c2 g = st[k * 9 + w], gp = st[((512 - k) & 511) * 9 + w];
    const float cs[8] = {1.f, 0.92387953251128674f, 0.70710678118654752f, 0.38268343236508977f, 0.f, -0.38268343236508977f, -0.70710678118654752f, -0.92387953251128674f};
    const float sn[8] = {0.f, 0.38268343236508977f, 0.70710678118654752f, 0.92387953251128674f, 1.f, 0.92387953251128674f, 0.70710678118654752f, 0.38268343236508977f};
    const c2 tw = j == 0 ? tw0 : cmul(tw0, (c2){cs[j], sn[j]});
    c2 Z = add_i(add_c(g, gp), cmul(tw, sub_c(g, gp)));
    if (k == 0) Z = (c2){g.x + g.y, g.x - g.y};
    v[j] = Z;
  }
  c2* ex = ex_all + w * 576;
  fft512_wave<1>(v, ex, lane);
  const float scale = 1.0f / (2048.0f * 1024.0f);
  uint32_t* hr = reinterpret_cast<uint32_t*>(h + (size_t)row * D_);
#pragma unroll
  for (int j = 0; j < 8; ++j) {
    const c2 hh = unpk(xv[j]) + rbf2(v[j] * scale);
    const uint32_t hu = cvt_pk(hh.x, hh.y);
    st_nt(hr + lane + 64 * j, hu);
    v[j] = unpk(hu);
  }
  ln_bf16(v, xv);
  uint32_t* ar = reinterpret_cast<uint32_t*>(a_out + (size_t)row * D_);
#pragma unroll
  for (int j = 0; j < 8; ++j) st_nt(ar + lane + 64 * j, xv[j]);
  KEEP(pfv);
}

#define WAIT_V(n) asm volatile("s_waitcnt vmcnt(" #n ")" ::: "memory")
#define WAIT_L0() asm volatile("s_waitcnt lgkmcnt(0)" ::: "memory")
#define RAW_BAR() __builtin_amdgcn_s_barrier()

__device__ __forceinline__ uint32_t mish_pair(float c0, float c1, f32x2 b) {
  f32x2 x = unpk(cvt_pk(c0, c1)) + b;
  const f32x2 hh = unpk(cvt_pk(x[0], x[1]));
  f32x2 z = hh * 1.44269504088896340736f;
  f32x2 t = {__builtin_amdgcn_exp2f(__builtin_amdgcn_fmed3f(z[0], -200.0f, 43.0f)), __builtin_amdgcn_exp2f(__builtin_amdgcn_fmed3f(z[1], -200.0f, 43.0f))};
  const f32x2 n = t * (t + 2.0f);
  const f32x2 d = n + 2.0f;
  const f32x2 r = {__builtin_amdgcn_rcpf(d[0]), __builtin_amdgcn_rcpf(d[1])};
  const f32x2 o = (hh * n) * r;
  return cvt_pk(o[0], o[1]);
}

typedef __attribute__((ext_vector_type(4))) unsigned int u32x4;
typedef __attribute__((ext_vector_type(2))) unsigned int u32x2;
template <int OFF> __device__ __forceinline__ void lds_rd128(u32x4& d, uint32_t a) { asm volatile("ds_read_b128 %0, %1 offset:%2" : "=v"(d) : "v"(a), "i"(OFF)); }
template <int OFF> __device__ __forceinline__ void lds_rdtr(u32x2& d, uint32_t a) { asm volatile("ds_read_b64_tr_b16 %0, %1 offset:%2" : "=v"(d) : "v"(a), "i"(OFF)); }
__device__ __forceinline__ bf16x8 mk8(u32x2 lo, u32x2 hi) { u32x4 v = {lo[0], lo[1], hi[0], hi[1]}; return __builtin_bit_cast(bf16x8, v); }
#define SCHED0() __builtin_amdgcn_sched_barrier(0)
__device__ __forceinline__ void mfma16(f32x4& c, const u32x2& alo, const u32x2& ahi, const u32x4& b) {
  u32x4 a = {alo[0], alo[1], ahi[0], ahi[1]};
  asm volatile("v_mfma_f32_16x16x32_bf16 %0, %1, %2, %0" : "+v"(c) : "v"(a), "v"(b));
}
template <int N> struct IC { static constexpr int v = N; };

constexpr int G1_A_BYTES = 128 * 128, G1_B_BYTES = 64 * 448, G1_STAGE = G1_A_BYTES + G1_B_BYTES;
constexpr int G1_OT = 57344, G1_OTS = 464;
struct G1Frag { u32x4 a[4]; u32x2 b[7][2]; };
__global__ __launch_bounds__(512, 2) void g1_gemm(const u16* __restrict__ A, const u16* __restrict__ W, const u16* __restrict__ b1,
                                                   u16* __restrict__ act) {
  __shared__ __attribute__((aligned(16))) char smem[3 * G1_STAGE];
  const int tid = threadIdx.x, lane = tid & 63, w8 = __builtin_amdgcn_readfirstlane(tid >> 6), w = w8 & 3;
  const int wm = w >> 1, wn = w & 1;
  const int bid = blockIdx.x, xcd = bid & 7, idx = bid >> 3;
  const int tm = (xcd & 1) * 8 + (idx & 7), tn = (xcd >> 1) * 4 + (idx >> 3);
  const int m0 = tm * 128, n0 = tn * 224;
  uint32_t aoff[4], boff[7];
#pragma unroll
  for (int i = 0; i < 4; ++i) {
    const int r = w * 32 + i * 8 + (lane >> 3);
    aoff[i] = (uint32_t)(m0 + r) * 2048u + (uint32_t)(((lane & 7) ^ (r & 7)) << 4);
  }
#pragma unroll
  for (int i = 0; i < 7; ++i) {
    const int d = i * 64 + lane, r = d / 28, q = d - r * 28, k = w * 16 + r;
    const int c = (q >> 1) ^ ((k >> 3) & 1);
    boff[i] = (uint32_t)k * (uint32_t)(F_ * 2) + (uint32_t)(n0 * 2) + (uint32_t)(((c << 1) | (q & 1)) << 4);
  }
  const char* Ab = reinterpret_cast<const char*>(A);
  const char* Wb = reinterpret_cast<const char*>(W);
  auto issue1 = [&](int i, int kt, int buf) {
    if (i < 4) {
      __builtin_amdgcn_global_load_lds((const unsigned*)(Ab + kt * 128 + aoff[i]),
                                       (unsigned*)(smem + buf * G1_STAGE + w * 4096 + lane * 16 + i * 1024), 16, 0, 0);
    } else {
      __builtin_amdgcn_global_load_lds((const unsigned*)(Wb + (size_t)kt * (64 * F_ * 2) + boff[i - 4]),
                                       (unsigned*)(smem + buf * G1_STAGE + G1_A_BYTES + w * 7168 + lane * 16 + (i - 4) * 1024), 16, 0, 0);
    }
  };
  const auto ars = __builtin_amdgcn_make_buffer_rsrc(act, 0, S_ * F_ * 2, 0x00020000);
  auto g1_store = [&]() {
    WAIT_L0();
    RAW_BAR();
#pragma unroll
    for (int i = 0; i < 7; ++i) {
      const int c = tid + 512 * i, r = c / 28, q = c - r * 28;
      const u32v4 v = *reinterpret_cast<const u32v4*>(smem + G1_OT + r * G1_OTS + q * 16);
      __builtin_amdgcn_raw_buffer_store_b128(v, ars, (uint32_t)(m0 + r) * (uint32_t)(F_ * 2) + (uint32_t)(n0 * 2 + q * 16), 0, 16);
    }
  };
  constexpr int NK = 16;
  if (w8 >= 4) {
#pragma unroll
    for (int i = 0; i < 11; ++i) issue1(i, 0, 0);
#pragma unroll
    for (int i = 0; i < 11; ++i) issue1(i, 1, 1);
    WAIT_V(11); RAW_BAR();
#pragma unroll
    for (int i = 0; i < 11; ++i) issue1(i, 2, 2);
    int buf = 0;
    for (int kt = 0; kt < NK - 3; ++kt) {
      WAIT_V(11); RAW_BAR();
#pragma unroll
      for (int i = 0; i < 11; ++i) issue1(i, kt + 3, buf);
      buf = buf == 2 ? 0 : buf + 1;
    }
    WAIT_V(11); RAW_BAR();
    WAIT_V(0); RAW_BAR();
    uint2 bb[7];
#pragma unroll
    for (int ni = 0; ni < 7; ++ni) bb[ni] = *reinterpret_cast<const uint2*>(b1 + n0 + wn * 112 + ni * 16 + 4 * (lane >> 4));
    RAW_BAR(); RAW_BAR();
    const f32x4* xfer = reinterpret_cast<const f32x4*>(smem) + w * (14 * 64) + lane;
    char* ot = smem + G1_OT + (wm * 64 + (lane & 15)) * G1_OTS + (wn * 112 + 4 * (lane >> 4)) * 2;
#pragma unroll
    for (int ni = 0; ni < 7; ++ni) {
      const f32x2 b01 = unpk(bb[ni].x), b23 = unpk(bb[ni].y);
#pragma unroll
      for (int mi = 2; mi < 4; ++mi) {
        const f32x4 c = xfer[((mi - 2) * 7 + ni) * 64];
        *reinterpret_cast<uint2*>(ot + mi * 16 * G1_OTS + ni * 32) = make_uint2(mish_pair(c[0], c[1], b01), mish_pair(c[2], c[3], b23));
      }
    }
    g1_store();
    return;
  }
  uint2 bb[7];
#pragma unroll
  for (int ni = 0; ni < 7; ++ni) bb[ni] = *reinterpret_cast<const uint2*>(b1 + n0 + wn * 112 + ni * 16 + 4 * (lane >> 4));
  f32x4 acc[4][7];
#pragma unroll
  for (int i = 0; i < 4; ++i)
#pragma unroll
    for (int j = 0; j < 7; ++j) acc[i][j] = (f32x4){0.f, 0.f, 0.f, 0.f};
  const int fr = lane & 15, fg = lane >> 4;
  const uint32_t sbase = (uint32_t)(size_t)(smem);
  const uint32_t ard0 = sbase + (wm * 64 + fr) * 128 + ((fg ^ (fr & 7)) << 4);
  const int q4 = (lane & 15) >> 2, p4 = lane & 3;
  const uint32_t brd = sbase + G1_A_BYTES + (8 * fg + q4) * 448 + 8 * p4 + wn * 224;
  const int f32_ = (fg & 1) * 32;
  const uint32_t brdE = brd + ((wn & 1) ? -f32_ : f32_), brdO = brd + ((wn & 1) ? f32_ : -f32_);
  G1Frag X, Y;
  auto rd = [&](G1Frag& Fr, auto I, auto KS, uint32_t so) {
    constexpr int i = decltype(I)::v, ks = decltype(KS)::v;
    if constexpr (i < 4) {
      lds_rd128<i * 2048>(Fr.a[i], (ard0 ^ (ks * 64)) + so);
    } else {
      constexpr int ni = (i - 4) >> 1, t = (i - 4) & 1;
      lds_rdtr<ks * 32 * 448 + ni * 32 + t * 4 * 448>(Fr.b[ni][t], ((ni & 1) ? brdO : brdE) + so);
    }
  };
  auto mma1 = [&](G1Frag& Fr, auto J) {
    constexpr int j = decltype(J)::v, mi = j / 7, ni = j % 7;
    mfma16(acc[mi][ni], Fr.b[ni][0], Fr.b[ni][1], Fr.a[mi]);
  };
#define G1_TIE(Fr) asm volatile("s_waitcnt lgkmcnt(0)" : "+v"(Fr.a[0]), "+v"(Fr.a[1]), "+v"(Fr.a[2]), "+v"(Fr.a[3]), \
    "+v"(Fr.b[0][0]), "+v"(Fr.b[0][1]), "+v"(Fr.b[1][0]), "+v"(Fr.b[1][1]), "+v"(Fr.b[2][0]), "+v"(Fr.b[2][1]), "+v"(Fr.b[3][0]), "+v"(Fr.b[3][1]), \
    "+v"(Fr.b[4][0]), "+v"(Fr.b[4][1]), "+v"(Fr.b[5][0]), "+v"(Fr.b[5][1]), "+v"(Fr.b[6][0]), "+v"(Fr.b[6][1]) :: "memory")
  auto block = [&](G1Frag& C, G1Frag& R, auto KS, uint32_t so, auto DO_RD, auto DO_IS, int kt_is, int buf_is) {
    auto one = [&](auto J) {
      constexpr int j = decltype(J)::v;
      mma1(C, J);
      if constexpr (decltype(DO_RD)::v && j < 18) rd(R, J, KS, so);
      if constexpr (decltype(DO_IS)::v && j >= 6 && j < 28 && ((j - 6) & 1) == 0) issue1((j - 6) >> 1, kt_is, buf_is);
      SCHED0();
    };
    one(IC<0>{}); one(IC<1>{}); one(IC<2>{}); one(IC<3>{}); one(IC<4>{}); one(IC<5>{}); one(IC<6>{});
    one(IC<7>{}); one(IC<8>{}); one(IC<9>{}); one(IC<10>{}); one(IC<11>{}); one(IC<12>{}); one(IC<13>{});
    one(IC<14>{}); one(IC<15>{}); one(IC<16>{}); one(IC<17>{}); one(IC<18>{}); one(IC<19>{}); one(IC<20>{});
    one(IC<21>{}); one(IC<22>{}); one(IC<23>{}); one(IC<24>{}); one(IC<25>{}); one(IC<26>{}); one(IC<27>{});
  };
  auto rdall = [&](G1Frag& R, uint32_t so) {
    rd(R, IC<0>{}, IC<0>{}, so); rd(R, IC<1>{}, IC<0>{}, so); rd(R, IC<2>{}, IC<0>{}, so); rd(R, IC<3>{}, IC<0>{}, so);
    rd(R, IC<4>{}, IC<0>{}, so); rd(R, IC<5>{}, IC<0>{}, so); rd(R, IC<6>{}, IC<0>{}, so); rd(R, IC<7>{}, IC<0>{}, so);
    rd(R, IC<8>{}, IC<0>{}, so); rd(R, IC<9>{}, IC<0>{}, so); rd(R, IC<10>{}, IC<0>{}, so); rd(R, IC<11>{}, IC<0>{}, so);
    rd(R, IC<12>{}, IC<0>{}, so); rd(R, IC<13>{}, IC<0>{}, so); rd(R, IC<14>{}, IC<0>{}, so); rd(R, IC<15>{}, IC<0>{}, so);
    rd(R, IC<16>{}, IC<0>{}, so); rd(R, IC<17>{}, IC<0>{}, so);
  };
  RAW_BAR();
  rdall(X, 0);
  int buf = 0;
  for (int kt = 0; kt < NK - 3; ++kt) {
    const int nbuf = buf == 2 ? 0 : buf + 1;
    G1_TIE(X); SCHED0();
    block(X, Y, IC<1>{}, buf * G1_STAGE, IC<1>{}, IC<0>{}, 0, 0);
    G1_TIE(Y); RAW_BAR(); SCHED0();
    block(Y, X, IC<0>{}, nbuf * G1_STAGE, IC<1>{}, IC<0>{}, 0, 0);
    buf = nbuf;
  }
  G1_TIE(X); SCHED0();
  block(X, Y, IC<1>{}, 1 * G1_STAGE, IC<1>{}, IC<0>{}, 0, 0);
  G1_TIE(Y); RAW_BAR(); SCHED0();
  block(Y, X, IC<0>{}, 2 * G1_STAGE, IC<1>{}, IC<0>{}, 0, 0);
  G1_TIE(X); SCHED0();
  block(X, Y, IC<1>{}, 2 * G1_STAGE, IC<1>{}, IC<0>{}, 0, 0);
  G1_TIE(Y); RAW_BAR(); SCHED0();
  block(Y, X, IC<0>{}, 0 * G1_STAGE, IC<1>{}, IC<0>{}, 0, 0);
  G1_TIE(X); SCHED0();
  block(X, Y, IC<1>{}, 0 * G1_STAGE, IC<1>{}, IC<0>{}, 0, 0);
  G1_TIE(Y); SCHED0();
  block(Y, X, IC<0>{}, 0, IC<0>{}, IC<0>{}, 0, 0);
  asm volatile("s_nop 15\n\ts_nop 15" : "+v"(acc[0][0]), "+v"(acc[0][1]), "+v"(acc[0][2]), "+v"(acc[0][3]), "+v"(acc[0][4]), "+v"(acc[0][5]), "+v"(acc[0][6]), "+v"(acc[1][0]), "+v"(acc[1][1]), "+v"(acc[1][2]), "+v"(acc[1][3]), "+v"(acc[1][4]), "+v"(acc[1][5]), "+v"(acc[1][6]), "+v"(acc[2][0]), "+v"(acc[2][1]), "+v"(acc[2][2]), "+v"(acc[2][3]), "+v"(acc[2][4]), "+v"(acc[2][5]), "+v"(acc[2][6]), "+v"(acc[3][0]), "+v"(acc[3][1]), "+v"(acc[3][2]), "+v"(acc[3][3]), "+v"(acc[3][4]), "+v"(acc[3][5]), "+v"(acc[3][6]) :: "memory");
  SCHED0();
  RAW_BAR();
  {
    f32x4* xfer = reinterpret_cast<f32x4*>(smem) + w * (14 * 64) + lane;
#pragma unroll
    for (int mi = 2; mi < 4; ++mi)
#pragma unroll
      for (int ni = 0; ni < 7; ++ni) xfer[((mi - 2) * 7 + ni) * 64] = acc[mi][ni];
  }
  WAIT_L0();
  RAW_BAR();
  char* ot = smem + G1_OT + (wm * 64 + fr) * G1_OTS + (wn * 112 + 4 * fg) * 2;
#pragma unroll
  for (int ni = 0; ni < 7; ++ni) {
    const f32x2 b01 = unpk(bb[ni].x), b23 = unpk(bb[ni].y);
#pragma unroll
    for (int mi = 0; mi < 2; ++mi) {
      const f32x4 c = acc[mi][ni];
      *reinterpret_cast<uint2*>(ot + mi * 16 * G1_OTS + ni * 32) = make_uint2(mish_pair(c[0], c[1], b01), mish_pair(c[2], c[3], b23));
    }
  }
  g1_store();
}

constexpr int G2_A_BYTES = 128 * 128, G2_B_BYTES = 64 * 256, G2_STAGE = G2_A_BYTES + G2_B_BYTES;
struct G2Frag { u32x4 a[4]; u32x2 b[4][2]; };
typedef __attribute__((address_space(1))) unsigned int gu32;
__global__ __launch_bounds__(512, 2) void g2_gemm(const u16* __restrict__ A, const u16* __restrict__ W, const u16* __restrict__ h,
                                                   u16* __restrict__ out, float* __restrict__ part, unsigned* __restrict__ flags) {
  __shared__ __attribute__((aligned(16))) char smem[4 * G2_STAGE];
  const int tid = threadIdx.x, lane = tid & 63, w8 = __builtin_amdgcn_readfirstlane(tid >> 6), w = w8 & 3;
  const int wm = w >> 1, wn = w & 1;
  const int bid = blockIdx.x, xcd = bid & 7, idx = bid >> 3;
  const int kh = xcd & 1, tm = ((xcd >> 1) & 1) * 8 + (idx & 7), tn = (xcd >> 2) * 4 + (idx >> 3);
  const int m0 = tm * 128, n0 = tn * 128, tile = tm * 8 + tn;
  auto g2_store = [&]() {
    WAIT_L0();
    RAW_BAR();
#pragma unroll
    for (int i = 0; i < 2; ++i) {
      const int c = tid + 512 * i, r = c >> 4, q = c & 15;
      const int row = (r >> 5) * 64 + kh * 32 + (r & 31);
      const uint4 v = *reinterpret_cast<const uint4*>(smem + r * 272 + q * 16);
      *reinterpret_cast<uint4*>(reinterpret_cast<char*>(out) + (size_t)(m0 + row) * (D_ * 2) + n0 * 2 + q * 16) = v;
    }
  };
  constexpr int NK = 28;
  if (w8 >= 4) {
    uint32_t aoff[4], boff[4];
#pragma unroll
    for (int i = 0; i < 4; ++i) {
      const int r = w * 32 + i * 8 + (lane >> 3);
      aoff[i] = (uint32_t)(m0 + r) * (uint32_t)(F_ * 2) + (uint32_t)(((lane & 7) ^ (r & 7)) << 4);
    }
#pragma unroll
    for (int i = 0; i < 4; ++i) {
      const int k = w * 16 + i * 4 + (lane >> 4), q = lane & 15;
      const int c = (q >> 1) ^ ((k & 3) | (((k >> 3) & 1) << 2));
      boff[i] = (uint32_t)k * (uint32_t)(D_ * 2) + (uint32_t)(n0 * 2) + (uint32_t)(((c << 1) | (q & 1)) << 4);
    }
    const char* Ab = reinterpret_cast<const char*>(A) + kh * (1792 * 2);
    const char* Wb = reinterpret_cast<const char*>(W) + (size_t)kh * (1792 * D_ * 2);
    auto issue = [&](int kt, int buf) {
#pragma unroll
      for (int i = 0; i < 4; ++i)
        __builtin_amdgcn_global_load_lds((const unsigned*)(Ab + kt * 128 + aoff[i]),
                                         (unsigned*)(smem + buf * G2_STAGE + w * 4096 + lane * 16 + i * 1024), 16, 0, 0);
#pragma unroll
      for (int i = 0; i < 4; ++i)
        __builtin_amdgcn_global_load_lds((const unsigned*)(Wb + (size_t)kt * (64 * D_ * 2) + boff[i]),
                                         (unsigned*)(smem + buf * G2_STAGE + G2_A_BYTES + w * 4096 + lane * 16 + i * 1024), 16, 0, 0);
    };
    issue(0, 0); issue(1, 1);
    WAIT_V(8); RAW_BAR();
    issue(2, 2); issue(3, 3);
    for (int kt = 0; kt < NK - 4; ++kt) {
      WAIT_V(16); RAW_BAR();
      issue(kt + 4, kt & 3);
    }
    WAIT_V(16); RAW_BAR();
    WAIT_V(8); RAW_BAR();
    WAIT_V(0); RAW_BAR();
    RAW_BAR();
    RAW_BAR();
    g2_store();
    return;
  }
  const int fr = lane & 15, fg = lane >> 4;
  uint2 hv[2][4];
#pragma unroll
  for (int i = 0; i < 2; ++i)
#pragma unroll
    for (int ni = 0; ni < 4; ++ni)
      hv[i][ni] = *reinterpret_cast<const uint2*>(h + (size_t)(m0 + wm * 64 + (2 * kh + i) * 16 + fr) * D_ + n0 + wn * 64 + ni * 16 + 4 * fg);
  f32x4 acc[4][4];
#pragma unroll
  for (int i = 0; i < 4; ++i)
#pragma unroll
    for (int j = 0; j < 4; ++j) acc[i][j] = (f32x4){0.f, 0.f, 0.f, 0.f};
  const uint32_t sbase = (uint32_t)(size_t)(smem);
  const uint32_t ard0 = sbase + (wm * 64 + fr) * 128 + ((fg ^ (fr & 7)) << 4);
  const int q4 = (lane & 15) >> 2, p4 = lane & 3;
  const int bsw = q4 | ((fg & 1) << 2);
  const uint32_t brd = sbase + G2_A_BYTES + (8 * fg + q4) * 256 + 8 * p4;
  uint32_t brdn[4];
#pragma unroll
  for (int ni = 0; ni < 4; ++ni) brdn[ni] = brd + (((wn * 4 + ni) ^ bsw) * 32);
  G2Frag X, Y;
  auto rd = [&](G2Frag& Fr, auto I, auto KS, uint32_t so) {
    constexpr int i = decltype(I)::v, ks = decltype(KS)::v;
    if constexpr (i < 4) {
      lds_rd128<i * 2048>(Fr.a[i], (ard0 ^ (ks * 64)) + so);
    } else {
      constexpr int ni = (i - 4) >> 1, t = (i - 4) & 1;
      lds_rdtr<ks * 32 * 256 + t * 4 * 256>(Fr.b[ni][t], brdn[ni] + so);
    }
  };
  auto mma1 = [&](G2Frag& Fr, auto J) {
    constexpr int j = decltype(J)::v, mi = j / 4, ni = j % 4;
    mfma16(acc[mi][ni], Fr.b[ni][0], Fr.b[ni][1], Fr.a[mi]);
  };
#define G2_TIE(Fr) asm volatile("s_waitcnt lgkmcnt(0)" : "+v"(Fr.a[0]), "+v"(Fr.a[1]), "+v"(Fr.a[2]), "+v"(Fr.a[3]), \
    "+v"(Fr.b[0][0]), "+v"(Fr.b[0][1]), "+v"(Fr.b[1][0]), "+v"(Fr.b[1][1]), "+v"(Fr.b[2][0]), "+v"(Fr.b[2][1]), "+v"(Fr.b[3][0]), "+v"(Fr.b[3][1]) :: "memory")
  auto block = [&](G2Frag& C, G2Frag& R, auto KS, uint32_t so, auto DO_RD) {
    auto one = [&](auto J) {
      constexpr int j = decltype(J)::v;
      mma1(C, J);
      if constexpr (decltype(DO_RD)::v && j < 12) rd(R, J, KS, so);
      SCHED0();
    };
    one(IC<0>{}); one(IC<1>{}); one(IC<2>{}); one(IC<3>{}); one(IC<4>{}); one(IC<5>{}); one(IC<6>{}); one(IC<7>{});
    one(IC<8>{}); one(IC<9>{}); one(IC<10>{}); one(IC<11>{}); one(IC<12>{}); one(IC<13>{}); one(IC<14>{}); one(IC<15>{});
  };
  RAW_BAR();
  rd(X, IC<0>{}, IC<0>{}, 0); rd(X, IC<1>{}, IC<0>{}, 0); rd(X, IC<2>{}, IC<0>{}, 0); rd(X, IC<3>{}, IC<0>{}, 0);
  rd(X, IC<4>{}, IC<0>{}, 0); rd(X, IC<5>{}, IC<0>{}, 0); rd(X, IC<6>{}, IC<0>{}, 0); rd(X, IC<7>{}, IC<0>{}, 0);
  rd(X, IC<8>{}, IC<0>{}, 0); rd(X, IC<9>{}, IC<0>{}, 0); rd(X, IC<10>{}, IC<0>{}, 0); rd(X, IC<11>{}, IC<0>{}, 0);
  for (int kt = 0; kt < NK - 1; ++kt) {
    G2_TIE(X); SCHED0();
    block(X, Y, IC<1>{}, (kt & 3) * G2_STAGE, IC<1>{});
    G2_TIE(Y); RAW_BAR(); SCHED0();
    block(Y, X, IC<0>{}, ((kt + 1) & 3) * G2_STAGE, IC<1>{});
  }
  G2_TIE(X); SCHED0();
  block(X, Y, IC<1>{}, ((NK - 1) & 3) * G2_STAGE, IC<1>{});
  G2_TIE(Y); SCHED0();
  block(Y, X, IC<0>{}, 0, IC<0>{});
  asm volatile("s_nop 15\n\ts_nop 15" : "+v"(acc[0][0]), "+v"(acc[0][1]), "+v"(acc[0][2]), "+v"(acc[0][3]), "+v"(acc[1][0]), "+v"(acc[1][1]), "+v"(acc[1][2]), "+v"(acc[1][3]), "+v"(acc[2][0]), "+v"(acc[2][1]), "+v"(acc[2][2]), "+v"(acc[2][3]), "+v"(acc[3][0]), "+v"(acc[3][1]), "+v"(acc[3][2]), "+v"(acc[3][3]) :: "memory");
  SCHED0();
  const auto rs = __builtin_amdgcn_make_buffer_rsrc(part, 0, 256 * 32768, 0x00020000);
  const uint32_t myoff = (uint32_t)(tile * 2 + kh) * 32768u + (uint32_t)w * 8192u + (uint32_t)lane * 16u;
  const uint32_t proff = (uint32_t)(tile * 2 + (kh ^ 1)) * 32768u + (uint32_t)w * 8192u + (uint32_t)lane * 16u;
  auto send = [&](const f32x4 (&r0)[4], const f32x4 (&r1)[4]) {
#pragma unroll
    for (int ni = 0; ni < 4; ++ni) {
      __builtin_amdgcn_raw_buffer_store_b128(__builtin_bit_cast(u32x4, r0[ni]), rs, myoff + ni * 1024, 0, 16);
      __builtin_amdgcn_raw_buffer_store_b128(__builtin_bit_cast(u32x4, r1[ni]), rs, myoff + 4096 + ni * 1024, 0, 16);
    }
  };
  if (kh == 0) send(acc[2], acc[3]); else send(acc[0], acc[1]);
  WAIT_V(0);
  RAW_BAR();
  if (w == 0) {
    if (lane == 0) {
      __hip_atomic_store((gu32*)(flags + (tile * 2 + kh) * 16), 1u, __ATOMIC_RELAXED, __HIP_MEMORY_SCOPE_AGENT);
      for (int spin = 0; spin < (1 << 22); ++spin) {
        if (__hip_atomic_load((gu32*)(flags + (tile * 2 + (kh ^ 1)) * 16), __ATOMIC_RELAXED, __HIP_MEMORY_SCOPE_AGENT) == 1u) break;
        __builtin_amdgcn_s_sleep(1);
      }
    }
  }
  asm volatile("" ::: "memory");
  RAW_BAR();
  asm volatile("" ::: "memory");
  auto fin = [&](auto I, const f32x4 (&own)[4]) {
    constexpr int i = decltype(I)::v;
    char* ot = smem + (wm * 32 + i * 16 + fr) * 272 + wn * 128 + 8 * fg;
    f32x4 pr[4];
#pragma unroll
    for (int ni = 0; ni < 4; ++ni) pr[ni] = __builtin_bit_cast(f32x4, __builtin_amdgcn_raw_buffer_load_b128(rs, proff + i * 4096 + ni * 1024, 0, 16));
#pragma unroll
    for (int ni = 0; ni < 4; ++ni) {
      const f32x4 c = kh == 0 ? own[ni] + pr[ni] : pr[ni] + own[ni];
      const f32x2 r01 = unpk(hv[i][ni].x) + unpk(cvt_pk(c[0], c[1])), r23 = unpk(hv[i][ni].y) + unpk(cvt_pk(c[2], c[3]));
      *reinterpret_cast<uint2*>(ot + ni * 32) = make_uint2(cvt_pk(r01[0], r01[1]), cvt_pk(r23[0], r23[1]));
    }
  };
  if (kh == 0) { fin(IC<0>{}, acc[0]); fin(IC<1>{}, acc[1]); }
  else { fin(IC<0>{}, acc[2]); fin(IC<1>{}, acc[3]); }
  g2_store();
}

#define G1_LAUNCH(st) g1_gemm<<<256, 512, 0, st>>>(da, dw1, db1, dact)
#define G2_LAUNCH(st) do { (void)hipMemsetAsync(dflags, 0, 16384, st); g2_gemm<<<256, 512, 0, st>>>(dact, dw2, dh, dout, dpart, dflags); } while (0)
extern "C" void kernel_launch(void* const* d_in, const int* in_sizes, int n_in,
                              void* d_out, int out_size, void* d_ws, size_t ws_size,
                              hipStream_t stream) {
  const u16* x = (const u16*)d_in[0];
  const u16* wr = (const u16*)d_in[1];
  const u16* wi = (const u16*)d_in[2];
  const u16* w1 = (const u16*)d_in[4];
  const u16* b1 = (const u16*)d_in[5];
  const u16* w2 = (const u16*)d_in[6];
  u16* out = (u16*)d_out;
  char* ws = (char*)d_ws;
  c2* T = (c2*)ws;
  uint32_t* WT = (uint32_t*)(ws + (9u << 20));
  u16* h = (u16*)(ws + (16u << 20));
  u16* a = (u16*)(ws + (20u << 20));
  u16* act = (u16*)(ws + (24u << 20));
  float* part = (float*)(ws + (40u << 20));
  unsigned* flags = (unsigned*)(ws + (48u << 20));
  k1_ln_rowfft<<<256, 512, 0, stream>>>(x, wr, wi, T, WT, flags);
  c2* U = (c2*)(ws + (50u << 20));
  k2_colfft<<<512, 256, 0, stream>>>(T, U, WT, (const char*)w1);
  k3_irowfft_ln<<<256, 512, 0, stream>>>(U, x, h, a, (const char*)w2);
  g1_gemm<<<256, 512, 0, stream>>>(a, w1, b1, act);
  g2_gemm<<<256, 512, 0, stream>>>(act, w2, h, out, part, flags);
}
```

```cpp
#include <hip/hip_runtime.h>
#include <hip/hip_bf16.h>
#include <stdint.h>

typedef unsigned short u16;
typedef __attribute__((ext_vector_type(8))) short bf16x8;
typedef __attribute__((ext_vector_type(4))) short bf16x4;
typedef __attribute__((ext_vector_type(4))) float f32x4;
typedef __attribute__((address_space(3))) bf16x4* lds_b4_ptr;

constexpr int TBS = 4096 + 16;
constexpr int S_ = 2048, D_ = 1024, F_ = 3584, DF_ = 513;

__device__ __forceinline__ float bf2f(u16 v) { return __uint_as_float(((uint32_t)v) << 16); }
__device__ __forceinline__ u16 f2bf(float f) { __bf16 b = (__bf16)f; return __builtin_bit_cast(u16, b); }
__device__ __forceinline__ float rbf(float f) { return bf2f(f2bf(f)); }

typedef __attribute__((ext_vector_type(4))) unsigned int u32v4;
typedef __attribute__((ext_vector_type(4))) float f32v4;
template <typename T> __device__ __forceinline__ void st_nt(T* p, T v) { __builtin_nontemporal_store(v, p); }
typedef __attribute__((ext_vector_type(2))) float c2;
typedef c2 f32x2;
__device__ __forceinline__ uint32_t cvt_pk(float lo, float hi) { uint32_t r; asm("v_cvt_pk_bf16_f32 %0, %1, %2" : "=v"(r) : "v"(lo), "v"(hi)); return r; }
__device__ __forceinline__ c2 unpk(uint32_t u) { return (c2){__uint_as_float(u << 16), __uint_as_float(u & 0xffff0000u)}; }
__device__ __forceinline__ c2 rbf2(c2 v) { return unpk(cvt_pk(v.x, v.y)); }
__device__ __forceinline__ c2 cmul(c2 a, c2 b) {
  c2 t, r;
  asm("v_pk_mul_f32 %0, %1, %2 op_sel:[0,0] op_sel_hi:[0,1]" : "=v"(t) : "v"(a), "v"(b));
  asm("v_pk_fma_f32 %0, %1, %2, %3 op_sel:[1,1,0] op_sel_hi:[1,0,1] neg_lo:[0,1,0]" : "=v"(r) : "v"(a), "v"(b), "v"(t));
  return r;
}
__device__ __forceinline__ c2 sub_i(c2 t, c2 d) { c2 r; asm("v_pk_add_f32 %0, %1, %2 op_sel:[0,1] op_sel_hi:[1,0] neg_hi:[0,1]" : "=v"(r) : "v"(t), "v"(d)); return r; }
__device__ __forceinline__ c2 add_i(c2 t, c2 d) { c2 r; asm("v_pk_add_f32 %0, %1, %2 op_sel:[0,1] op_sel_hi:[1,0] neg_lo:[0,1]" : "=v"(r) : "v"(t), "v"(d)); return r; }
__device__ __forceinline__ c2 add_c(c2 a, c2 b) { c2 r; asm("v_pk_add_f32 %0, %1, %2 neg_hi:[0,1]" : "=v"(r) : "v"(a), "v"(b)); return r; }
__device__ __forceinline__ c2 sub_c(c2 a, c2 b) { c2 r; asm("v_pk_add_f32 %0, %1, %2 neg_lo:[0,1]" : "=v"(r) : "v"(a), "v"(b)); return r; }
template <int DIR> __device__ __forceinline__ c2 addD(c2 t, c2 d) { return DIR > 0 ? add_i(t, d) : sub_i(t, d); }
template <int DIR> __device__ __forceinline__ c2 subD(c2 t, c2 d) { return DIR > 0 ? sub_i(t, d) : add_i(t, d); }
template <int DIR>
__device__ __forceinline__ c2 cexp2pi(float rev) {
  const float r = DIR > 0 ? rev : -rev;
  c2 w = {__builtin_amdgcn_cosf(r), __builtin_amdgcn_sinf(r)};
  asm("s_nop 0" : "+v"(w));
  return w;
}
template <int DIR>
__device__ __forceinline__ void dft4(c2& x0, c2& x1, c2& x2, c2& x3) {
  const c2 t0 = x0 + x2, t1 = x0 - x2, t2 = x1 + x3, d = x1 - x3;
  x0 = t0 + t2; x2 = t0 - t2; x1 = addD<DIR>(t1, d); x3 = subD<DIR>(t1, d);
}
template <int DIR>
__device__ __forceinline__ void dft8(c2 (&v)[8]) {
  c2 e0 = v[0], e1 = v[2], e2 = v[4], e3 = v[6];
  c2 o0 = v[1], o1 = v[3], o2 = v[5], o3 = v[7];
  dft4<DIR>(e0, e1, e2, e3);
  dft4<DIR>(o0, o1, o2, o3);
  const c2 r = {0.70710678118654752440f, 0.70710678118654752440f};
  const c2 u1 = addD<DIR>(o1, o1), u3 = subD<DIR>(o3, o3);
  v[0] = e0 + o0; v[4] = e0 - o0;
  v[1] = __builtin_elementwise_fma(u1, r, e1); v[5] = __builtin_elementwise_fma(u1, -r, e1);
  v[2] = addD<DIR>(e2, o2); v[6] = subD<DIR>(e2, o2);
  v[3] = __builtin_elementwise_fma(u3, -r, e3); v[7] = __builtin_elementwise_fma(u3, r, e3);
}
__device__ __forceinline__ void twiddle8(c2 (&v)[8], c2 w) {
  const c2 w2 = cmul(w, w), w3 = cmul(w2, w), w4 = cmul(w2, w2), w5 = cmul(w4, w), w6 = cmul(w3, w3), w7 = cmul(w4, w3);
  v[1] = cmul(v[1], w); v[2] = cmul(v[2], w2); v[3] = cmul(v[3], w3); v[4] = cmul(v[4], w4);
  v[5] = cmul(v[5], w5); v[6] = cmul(v[6], w6); v[7] = cmul(v[7], w7);
}
__device__ __forceinline__ void twiddle8b(c2 (&v)[8], c2 b, c2 w) {
  const c2 w2 = cmul(w, w), w4 = cmul(w2, w2);
  const c2 b1 = cmul(b, w), b2 = cmul(b, w2), b3 = cmul(b1, w2), b4 = cmul(b, w4), b5 = cmul(b1, w4), b6 = cmul(b2, w4), b7 = cmul(b3, w4);
  v[0] = cmul(v[0], b); v[1] = cmul(v[1], b1); v[2] = cmul(v[2], b2); v[3] = cmul(v[3], b3);
  v[4] = cmul(v[4], b4); v[5] = cmul(v[5], b5); v[6] = cmul(v[6], b6); v[7] = cmul(v[7], b7);
}

__device__ __forceinline__ float wave_sum(float v) {
#define DPP_ADD(ctrl, rmask) v += __uint_as_float(__builtin_amdgcn_update_dpp(0u, __float_as_uint(v), ctrl, rmask, 0xf, true))
  DPP_ADD(0x111, 0xf); DPP_ADD(0x112, 0xf); DPP_ADD(0x114, 0xf); DPP_ADD(0x118, 0xf);
  DPP_ADD(0x142, 0xa); DPP_ADD(0x143, 0xc);
#undef DPP_ADD
  return __uint_as_float(__builtin_amdgcn_readlane(__float_as_uint(v), 63));
}

#define WAVE_SYNC() do { __builtin_amdgcn_fence(__ATOMIC_RELEASE, "wavefront"); __builtin_amdgcn_wave_barrier(); __builtin_amdgcn_fence(__ATOMIC_ACQUIRE, "wavefront"); } while (0)

template <int DIR>
__device__ __forceinline__ void fft512_wave(c2 (&v)[8], c2* ex, int lane) {
  const int hi = lane >> 3, lo = lane & 7;
  dft8<DIR>(v);
  twiddle8(v, cexp2pi<DIR>((float)hi * (1.0f / 64.0f)));
#pragma unroll
  for (int k0 = 0; k0 < 8; ++k0) ex[72 * k0 + lane] = v[k0];
  WAVE_SYNC();
#pragma unroll
  for (int b = 0; b < 8; ++b) v[b] = ex[72 * hi + 8 * b + lo];
  WAVE_SYNC();
  dft8<DIR>(v);
  twiddle8b(v, cexp2pi<DIR>((float)(lo * hi) * (1.0f / 512.0f)), cexp2pi<DIR>((float)lo * (1.0f / 64.0f)));
#pragma unroll
  for (int k1 = 0; k1 < 8; ++k1) ex[66 * lo + 8 * k1 + hi] = v[k1];
  WAVE_SYNC();
#pragma unroll
  for (int c = 0; c < 8; ++c) v[c] = ex[66 * c + lane];
  WAVE_SYNC();
  dft8<DIR>(v);
}

__device__ __forceinline__ void ln_bf16(c2 (&e)[8], uint32_t (&o)[8]) {
  c2 s2 = ((e[0] + e[1]) + (e[2] + e[3])) + ((e[4] + e[5]) + (e[6] + e[7]));
  const float mean = wave_sum(s2.x + s2.y) * (1.0f / 1024.0f);
  c2 q2 = {0.f, 0.f};
#pragma unroll
  for (int i = 0; i < 8; ++i) { const c2 d = e[i] - mean; q2 = __builtin_elementwise_fma(d, d, q2); }
  const float var = wave_sum(q2.x + q2.y) * (1.0f / 1024.0f);
  const float mb = rbf(mean);
  const float sd = rbf(sqrtf(rbf(rbf(var) + 1e-5f)));
  const float ri = 1.0f / sd;
#pragma unroll
  for (int i = 0; i < 8; ++i) {
    const c2 d = rbf2(e[i] - mb);
    const c2 q = d * ri;
    const c2 rem = __builtin_elementwise_fma(-q, (c2){sd, sd}, d);
    const c2 qq = __builtin_elementwise_fma(rem, (c2){ri, ri}, q);
    o[i] = cvt_pk(qq.x, qq.y);
  }
}

__global__ __launch_bounds__(512) void k1_ln_rowfft(const u16* __restrict__ x, const u16* __restrict__ wr, const u16* __restrict__ wi,
                                                     c2* __restrict__ T, uint32_t* __restrict__ WT, unsigned* __restrict__ flags) {
  __shared__ c2 ex_all[8 * 576];
  __shared__ c2 st[512 * 9];
  const int tid = threadIdx.x, lane = tid & 63, w = tid >> 6;
  const int s0 = blockIdx.x * 8;
  const int row = s0 + w;
  const uint32_t* xr = reinterpret_cast<const uint32_t*>(x + (size_t)row * D_);
  uint32_t xu[8];
#pragma unroll
  for (int a = 0; a < 8; ++a) xu[a] = xr[lane + 64 * a];
  if (tid == 0) {
    flags[blockIdx.x * 16] = 0;
    if (blockIdx.x < 32) flags[4096 + blockIdx.x * 16] = 0;
    if (blockIdx.x < 16) flags[8192 + blockIdx.x * 16] = 0;
  }
  {
    uint32_t p[8];
#pragma unroll
    for (int r = 0; r < 8; ++r) {
      const size_t o = (size_t)(s0 + r) * DF_ + tid;
      p[r] = (uint32_t)wr[o] | ((uint32_t)wi[o] << 16);
    }
    u32v4* dst = reinterpret_cast<u32v4*>(WT + (size_t)tid * S_ + s0);
    st_nt(dst, (u32v4){p[0], p[1], p[2], p[3]});
    st_nt(dst + 1, (u32v4){p[4], p[5], p[6], p[7]});
    if (tid < 8) {
      const size_t o = (size_t)(s0 + tid) * DF_ + 512;
      WT[(size_t)512 * S_ + s0 + tid] = (uint32_t)wr[o] | ((uint32_t)wi[o] << 16);
    }
  }
  c2 v[8];
#pragma unroll
  for (int a = 0; a < 8; ++a) v[a] = unpk(xu[a]);
  ln_bf16(v, xu);
#pragma unroll
  for (int a = 0; a < 8; ++a) v[a] = unpk(xu[a]);
  c2* ex = ex_all + w * 576;
  fft512_wave<-1>(v, ex, lane);
#pragma unroll
  for (int j = 4; j < 8; ++j) ex[lane + 64 * j] = v[j];
  ex[512 + lane] = v[0];
  WAVE_SYNC();
  const c2 tw0 = cexp2pi<-1>((float)lane * (1.0f / 1024.0f));
  const c2 half = {0.5f, 0.5f};
#pragma unroll
  for (int j = 0; j < 4; ++j) {
    const int k = lane + 64 * j;
    const c2 z = v[j], zp = ex[512 - k];
    const c2 cj = j == 0 ? (c2){1.f, 0.f} : j == 1 ? (c2){0.92387953251128674f, -0.38268343236508977f}
                : j == 2 ? (c2){0.70710678118654752f, -0.70710678118654752f} : (c2){0.38268343236508977f, -0.92387953251128674f};
    const c2 tw = j == 0 ? tw0 : cmul(tw0, cj);
    const c2 E = add_c(z, zp) * half, Dz = sub_c(z, zp) * half;
    const c2 P = cmul(tw, Dz);
    c2 Xa = sub_i(E, P), Xb = add_i(E, P);
    Xb.y = -Xb.y;
    if (k == 0) { Xa = (c2){z.x + z.y, z.x - z.y}; Xb = (c2){v[4].x, -v[4].y}; }
    st[k * 9 + w] = Xa;
    st[(k == 0 ? 256 : 512 - k) * 9 + w] = Xb;
  }
  __syncthreads();
#pragma unroll
  for (int it = 0; it < 4; ++it) {
    const int idx = it * 512 + tid;
    const int k = idx >> 2, part = idx & 3;
    const c2 a = st[k * 9 + 2 * part], b = st[k * 9 + 2 * part + 1];
    st_nt(reinterpret_cast<f32v4*>(T + (size_t)k * S_ + s0) + part, (f32v4){a.x, a.y, b.x, b.y});
  }
}

#define KEEP(v) asm volatile("" :: "v"(v))
__device__ __forceinline__ uint32_t wt_at(const uint32_t* WT, int k1, int c) { return WT[(size_t)c * S_ + k1]; }
__global__ __launch_bounds__(256) void k2_colfft(const c2* __restrict__ T, c2* __restrict__ U, const uint32_t* __restrict__ WT, const char* __restrict__ pf) {
  __shared__ c2 ex[2304];
  const int t = threadIdx.x, col = blockIdx.x;
  c2 v[8];
#pragma unroll
  for (int a = 0; a < 8; ++a) v[a] = T[(size_t)col * S_ + t + 256 * a];
  uint32_t wv[8];
#pragma unroll
  for (int r = 0; r < 8; ++r) wv[r] = wt_at(WT, t + 256 * r, col);
  uint32_t pfv = 0;
  if (t < 112) pfv = *reinterpret_cast<const uint32_t*>(pf + ((size_t)col * 112 + t) * 128);
  c2* Tg = U + (size_t)(t >> 3) * TBS + col * 8 + (t & 7);
  const int h5 = t >> 5, l5 = t & 31;
  const int m3 = (t >> 2) & 7, d2 = t & 3;
  dft8<-1>(v);
  twiddle8(v, cexp2pi<-1>((float)t * (1.0f / 2048.0f)));
#pragma unroll
  for (int k0 = 0; k0 < 8; ++k0) ex[256 * k0 + t] = v[k0];
  __syncthreads();
#pragma unroll
  for (int b = 0; b < 8; ++b) v[b] = ex[256 * h5 + 32 * b + l5];
  __syncthreads();
  dft8<-1>(v);
  twiddle8(v, cexp2pi<-1>((float)l5 * (1.0f / 256.0f)));
#pragma unroll
  for (int k1 = 0; k1 < 8; ++k1) ex[288 * h5 + 36 * k1 + l5] = v[k1];
  WAVE_SYNC();
#pragma unroll
  for (int c = 0; c < 8; ++c) v[c] = ex[288 * h5 + 36 * m3 + 4 * c + d2];
  __syncthreads();
  dft8<-1>(v);
  twiddle8(v, cexp2pi<-1>((float)d2 * (1.0f / 32.0f)));
#pragma unroll
  for (int k2 = 0; k2 < 8; ++k2) ex[(4 * k2 + d2) * 66 + 8 * m3 + h5] = v[k2];
  __syncthreads();
  const int dd = t >> 6, l6 = t & 63;
#pragma unroll
  for (int d = 0; d < 4; ++d) { v[d] = ex[(4 * dd + d) * 66 + l6]; v[4 + d] = ex[(4 * (dd + 4) + d) * 66 + l6]; }
  __syncthreads();
  dft4<-1>(v[0], v[1], v[2], v[3]);
  dft4<-1>(v[4], v[5], v[6], v[7]);
  if (col == 0) {
#pragma unroll
    for (int k3 = 0; k3 < 4; ++k3) { ex[t + 512 * k3] = v[k3]; ex[t + 256 + 512 * k3] = v[4 + k3]; }
    __syncthreads();
  }
  if (col != 0) {
#pragma unroll
    for (int k3 = 0; k3 < 4; ++k3) {
      v[k3] = cmul(v[k3], unpk(wv[2 * k3]));
      v[4 + k3] = cmul(v[4 + k3], unpk(wv[2 * k3 + 1]));
    }
  } else {
#pragma unroll
    for (int r = 0; r < 8; ++r) {
      const int vi = (r & 1) * 4 + (r >> 1);
      const int k = t + 256 * r, kn = (2048 - k) & 2047;
      const c2 p = v[vi], pn = ex[kn];
      const c2 F0 = {0.5f * (p.x + pn.x), 0.5f * (p.y - pn.y)};
      const c2 F5 = {0.5f * (p.y + pn.y), 0.5f * (pn.x - p.x)};
      const c2 wa = unpk(wv[r]), wan = unpk(wt_at(WT, kn, 0));
      const c2 wb = unpk(wt_at(WT, k, 512)), wbn = unpk(wt_at(WT, kn, 512));
      const c2 W0 = {0.5f * (wa.x + wan.x), 0.5f * (wa.y - wan.y)};
      const c2 W5 = {0.5f * (wb.x + wbn.x), 0.5f * (wb.y - wbn.y)};
      const c2 H0 = cmul(F0, W0), H5 = cmul(F5, W5);
      v[vi] = add_i(H0, H5);
    }
  }
  if (col == 0) __syncthreads();
  dft4<1>(v[0], v[1], v[2], v[3]);
  dft4<1>(v[4], v[5], v[6], v[7]);
  {
    const c2 wa = cexp2pi<1>((float)dd * (1.0f / 32.0f)), wb = cexp2pi<1>((float)(dd + 4) * (1.0f / 32.0f));
    const c2 wa2 = cmul(wa, wa), wa3 = cmul(wa2, wa), wb2 = cmul(wb, wb), wb3 = cmul(wb2, wb);
    v[1] = cmul(v[1], wa); v[2] = cmul(v[2], wa2); v[3] = cmul(v[3], wa3);
    v[5] = cmul(v[5], wb); v[6] = cmul(v[6], wb2); v[7] = cmul(v[7], wb3);
  }
#pragma unroll
  for (int d = 0; d < 4; ++d) { ex[(4 * dd + d) * 66 + l6] = v[d]; ex[(4 * (dd + 4) + d) * 66 + l6] = v[4 + d]; }
  __syncthreads();
#pragma unroll
  for (int k2 = 0; k2 < 8; ++k2) v[k2] = ex[(4 * k2 + d2) * 66 + 8 * m3 + h5];
  __syncthreads();
  dft8<1>(v);
  twiddle8b(v, cexp2pi<1>((float)(m3 * d2) * (1.0f / 256.0f)), cexp2pi<1>((float)m3 * (1.0f / 64.0f)));
#pragma unroll
  for (int c = 0; c < 8; ++c) ex[288 * h5 + 36 * m3 + 4 * c + d2] = v[c];
  WAVE_SYNC();
#pragma unroll
  for (int k1 = 0; k1 < 8; ++k1) v[k1] = ex[288 * h5 + 36 * k1 + l5];
  __syncthreads();
  dft8<1>(v);
  twiddle8b(v, cexp2pi<1>((float)(h5 * l5) * (1.0f / 2048.0f)), cexp2pi<1>((float)h5 * (1.0f / 64.0f)));
#pragma unroll
  for (int b = 0; b < 8; ++b) ex[256 * h5 + 32 * b + l5] = v[b];
  __syncthreads();
#pragma unroll
  for (int k0 = 0; k0 < 8; ++k0) v[k0] = ex[256 * k0 + t];
  dft8<1>(v);
#pragma unroll
  for (int a = 0; a < 8; ++a) st_nt(Tg + (size_t)a * (32 * TBS), v[a]);
  KEEP(pfv);
}

__device__ __forceinline__ void k3_body(char* smem, const c2* __restrict__ T, const u16* __restrict__ x,
                                        u16* __restrict__ h, u16* __restrict__ a_out, const char* __restrict__ pf) {
  c2* ex_all = reinterpret_cast<c2*>(smem);
  c2* st = reinterpret_cast<c2*>(smem) + 8 * 576;
  const int tid = threadIdx.x, lane = tid & 63, w = tid >> 6;
  const int s0 = blockIdx.x * 8;
  const int row = s0 + w;
  float4 q4[4];
#pragma unroll
  for (int it = 0; it < 4; ++it) {
    const int idx = it * 512 + tid;
    q4[it] = reinterpret_cast<const float4*>(T + (size_t)blockIdx.x * TBS)[idx];
  }
  const uint32_t* xr = reinterpret_cast<const uint32_t*>(x + (size_t)row * D_);
  uint32_t xv[8];
#pragma unroll
  for (int a = 0; a < 8; ++a) xv[a] = xr[lane + 64 * a];
  uint32_t pfv = 0;
  if (tid < 224) pfv = *reinterpret_cast<const uint32_t*>(pf + ((size_t)blockIdx.x * 224 + tid) * 128);
#pragma unroll
  for (int it = 0; it < 4; ++it) {
    const int idx = it * 512 + tid;
    const int k = idx >> 2, part = idx & 3;
    st[k * 9 + 2 * part] = (c2){q4[it].x, q4[it].y};
    st[k * 9 + 2 * part + 1] = (c2){q4[it].z, q4[it].w};
  }
  __syncthreads();
  c2 v[8];
  const c2 tw0 = cexp2pi<1>((float)lane * (1.0f / 1024.0f));
#pragma unroll
  for (int j = 0; j < 8; ++j) {
    const int k = lane + 64 * j;
    const c2 g = st[k * 9 + w], gp = st[((512 - k) & 511) * 9 + w];
    const float cs[8] = {1.f, 0.92387953251128674f, 0.70710678118654752f, 0.38268343236508977f, 0.f, -0.38268343236508977f, -0.70710678118654752f, -0.92387953251128674f};
    const float sn[8] = {0.f, 0.38268343236508977f, 0.70710678118654752f, 0.92387953251128674f, 1.f, 0.92387953251128674f, 0.70710678118654752f, 0.38268343236508977f};
    const c2 tw = j == 0 ? tw0 : cmul(tw0, (c2){cs[j], sn[j]});
    c2 Z = add_i(add_c(g, gp), cmul(tw, sub_c(g, gp)));
    if (k == 0) Z = (c2){g.x + g.y, g.x - g.y};
    v[j] = Z;
  }
  c2* ex = ex_all + w * 576;
  fft512_wave<1>(v, ex, lane);
  const float scale = 1.0f / (2048.0f * 1024.0f);
  uint32_t* tr = reinterpret_cast<uint32_t*>(ex);
  const auto hrs = __builtin_amdgcn_make_buffer_rsrc(h, 0, S_ * D_ * 2, 0x00020000);
  const auto ars = __builtin_amdgcn_make_buffer_rsrc(a_out, 0, S_ * D_ * 2, 0x00020000);
  const uint32_t roff = (uint32_t)row * (D_ * 2) + lane * 16;
#pragma unroll
  for (int j = 0; j < 8; ++j) {
    const c2 hh = unpk(xv[j]) + rbf2(v[j] * scale);
    const uint32_t hu = cvt_pk(hh.x, hh.y);
    tr[lane + 64 * j] = hu;
    v[j] = unpk(hu);
  }
  WAVE_SYNC();
  {
    const u32v4 h0 = reinterpret_cast<const u32v4*>(tr)[lane], h1 = reinterpret_cast<const u32v4*>(tr)[64 + lane];
    __builtin_amdgcn_raw_buffer_store_b128(h0, hrs, roff, 0, 16);
    __builtin_amdgcn_raw_buffer_store_b128(h1, hrs, roff + 1024, 0, 16);
  }
  ln_bf16(v, xv);
#pragma unroll
  for (int j = 0; j < 8; ++j) tr[512 + lane + 64 * j] = xv[j];
  WAVE_SYNC();
  {
    const u32v4 a0 = reinterpret_cast<const u32v4*>(tr)[128 + lane], a1 = reinterpret_cast<const u32v4*>(tr)[192 + lane];
    __builtin_amdgcn_raw_buffer_store_b128(a0, ars, roff, 0, 16);
    __builtin_amdgcn_raw_buffer_store_b128(a1, ars, roff + 1024, 0, 16);
  }
  KEEP(pfv);
}

#define WAIT_V(n) asm volatile("s_waitcnt vmcnt(" #n ")" ::: "memory")
#define WAIT_L0() asm volatile("s_waitcnt lgkmcnt(0)" ::: "memory")
#define RAW_BAR() __builtin_amdgcn_s_barrier()

__device__ __forceinline__ uint32_t mish_pair(float c0, float c1, f32x2 b) {
  f32x2 x = unpk(cvt_pk(c0, c1)) + b;
  const f32x2 hh = unpk(cvt_pk(x[0], x[1]));
  f32x2 z = hh * 1.44269504088896340736f;
  f32x2 t = {__builtin_amdgcn_exp2f(__builtin_amdgcn_fmed3f(z[0], -200.0f, 43.0f)), __builtin_amdgcn_exp2f(__builtin_amdgcn_fmed3f(z[1], -200.0f, 43.0f))};
  const f32x2 n = t * (t + 2.0f);
  const f32x2 d = n + 2.0f;
  const f32x2 r = {__builtin_amdgcn_rcpf(d[0]), __builtin_amdgcn_rcpf(d[1])};
  const f32x2 o = (hh * n) * r;
  return cvt_pk(o[0], o[1]);
}

typedef __attribute__((ext_vector_type(4))) unsigned int u32x4;
typedef __attribute__((ext_vector_type(2))) unsigned int u32x2;
template <int OFF> __device__ __forceinline__ void lds_rd128(u32x4& d, uint32_t a) { asm volatile("ds_read_b128 %0, %1 offset:%2" : "=v"(d) : "v"(a), "i"(OFF)); }
template <int OFF> __device__ __forceinline__ void lds_rdtr(u32x2& d, uint32_t a) { asm volatile("ds_read_b64_tr_b16 %0, %1 offset:%2" : "=v"(d) : "v"(a), "i"(OFF)); }
__device__ __forceinline__ bf16x8 mk8(u32x2 lo, u32x2 hi) { u32x4 v = {lo[0], lo[1], hi[0], hi[1]}; return __builtin_bit_cast(bf16x8, v); }
#define SCHED0() __builtin_amdgcn_sched_barrier(0)
__device__ __forceinline__ void mfma16(f32x4& c, const u32x2& alo, const u32x2& ahi, const u32x4& b) {
  u32x4 a = {alo[0], alo[1], ahi[0], ahi[1]};
  asm volatile("v_mfma_f32_16x16x32_bf16 %0, %1, %2, %0" : "+v"(c) : "v"(a), "v"(b));
}
typedef __attribute__((address_space(1))) unsigned int gu32;
template <int N> struct IC { static constexpr int v = N; };

constexpr int G1_A_BYTES = 128 * 128, G1_B_BYTES = 64 * 448, G1_STAGE = G1_A_BYTES + G1_B_BYTES;
constexpr int G1_OT = 57344, G1_OTS = 464;
struct G1Frag { u32x4 a[4]; u32x2 b[7][2]; };
__device__ __forceinline__ void g1_body(char* smem, const u16* __restrict__ A, const u16* __restrict__ W, const u16* __restrict__ b1,
                                        u16* __restrict__ act, unsigned* __restrict__ flags) {
  const int tid = threadIdx.x, lane = tid & 63, w8 = __builtin_amdgcn_readfirstlane(tid >> 6), w = w8 & 3;
  const int wm = w >> 1, wn = w & 1;
  const int bid = blockIdx.x, xcd = bid & 7, idx = bid >> 3;
  const int tm = (xcd & 1) * 8 + (idx & 7), tn = (xcd >> 1) * 4 + (idx >> 3);
  const int m0 = tm * 128, n0 = tn * 224;
  uint32_t aoff[4], boff[7];
#pragma unroll
  for (int i = 0; i < 4; ++i) {
    const int r = w * 32 + i * 8 + (lane >> 3);
    aoff[i] = (uint32_t)(m0 + r) * 2048u + (uint32_t)(((lane & 7) ^ (r & 7)) << 4);
  }
#pragma unroll
  for (int i = 0; i < 7; ++i) {
    const int d = i * 64 + lane, r = d / 28, q = d - r * 28, k = w * 16 + r;
    const int c = (q >> 1) ^ ((k >> 3) & 1);
    boff[i] = (uint32_t)k * (uint32_t)(F_ * 2) + (uint32_t)(n0 * 2) + (uint32_t)(((c << 1) | (q & 1)) << 4);
  }
  const char* Ab = reinterpret_cast<const char*>(A);
  const char* Wb = reinterpret_cast<const char*>(W);
  auto issue1 = [&](int i, int kt, int buf) {
    if (i < 4) {
      __builtin_amdgcn_global_load_lds((const unsigned*)(Ab + kt * 128 + aoff[i]),
                                       (unsigned*)(smem + buf * G1_STAGE + w * 4096 + lane * 16 + i * 1024), 16, 0, 16);
    } else {
      __builtin_amdgcn_global_load_lds((const unsigned*)(Wb + (size_t)kt * (64 * F_ * 2) + boff[i - 4]),
                                       (unsigned*)(smem + buf * G1_STAGE + G1_A_BYTES + w * 7168 + lane * 16 + (i - 4) * 1024), 16, 0, 0);
    }
  };
  const auto ars = __builtin_amdgcn_make_buffer_rsrc(act, 0, S_ * F_ * 2, 0x00020000);
  auto g1_store = [&]() {
    WAIT_L0();
    RAW_BAR();
#pragma unroll
    for (int i = 0; i < 7; ++i) {
      const int c = tid + 512 * i, r = c / 28, q = c - r * 28;
      const u32v4 v = *reinterpret_cast<const u32v4*>(smem + G1_OT + r * G1_OTS + q * 16);
      __builtin_amdgcn_raw_buffer_store_b128(v, ars, (uint32_t)(m0 + r) * (uint32_t)(F_ * 2) + (uint32_t)(n0 * 2 + q * 16), 0, 16);
    }
  };
  constexpr int NK = 16;
  if (w8 >= 4) {
#pragma unroll
    for (int i = 4; i < 11; ++i) issue1(i, 0, 0);
#pragma unroll
    for (int i = 4; i < 11; ++i) issue1(i, 1, 1);
    if (lane == 0) {
      for (int spin = 0; spin < (1 << 22); ++spin) {
        if (__hip_atomic_load((gu32*)(flags + 8192 + tm * 16), __ATOMIC_RELAXED, __HIP_MEMORY_SCOPE_AGENT) == 16u) break;
        __builtin_amdgcn_s_sleep(1);
      }
    }
    asm volatile("" ::: "memory");
#pragma unroll
    for (int i = 0; i < 4; ++i) issue1(i, 0, 0);
#pragma unroll
    for (int i = 0; i < 4; ++i) issue1(i, 1, 1);
    WAIT_V(4); RAW_BAR();
#pragma unroll
    for (int i = 0; i < 11; ++i) issue1(i, 2, 2);
    int buf = 0;
    for (int kt = 0; kt < NK - 3; ++kt) {
      WAIT_V(11); RAW_BAR();
#pragma unroll
      for (int i = 0; i < 11; ++i) issue1(i, kt + 3, buf);
      buf = buf == 2 ? 0 : buf + 1;
    }
    WAIT_V(11); RAW_BAR();
    WAIT_V(0); RAW_BAR();
    uint2 bb[7];
#pragma unroll
    for (int ni = 0; ni < 7; ++ni) bb[ni] = *reinterpret_cast<const uint2*>(b1 + n0 + wn * 112 + ni * 16 + 4 * (lane >> 4));
    RAW_BAR(); RAW_BAR();
    const f32x4* xfer = reinterpret_cast<const f32x4*>(smem) + w * (14 * 64) + lane;
    char* ot = smem + G1_OT + (wm * 64 + (lane & 15)) * G1_OTS + (wn * 112 + 4 * (lane >> 4)) * 2;
#pragma unroll
    for (int ni = 0; ni < 7; ++ni) {
      const f32x2 b01 = unpk(bb[ni].x), b23 = unpk(bb[ni].y);
#pragma unroll
      for (int mi = 2; mi < 4; ++mi) {
        const f32x4 c = xfer[((mi - 2) * 7 + ni) * 64];
        *reinterpret_cast<uint2*>(ot + mi * 16 * G1_OTS + ni * 32) = make_uint2(mish_pair(c[0], c[1], b01), mish_pair(c[2], c[3], b23));
      }
    }
    g1_store();
    return;
  }
  uint2 bb[7];
#pragma unroll
  for (int ni = 0; ni < 7; ++ni) bb[ni] = *reinterpret_cast<const uint2*>(b1 + n0 + wn * 112 + ni * 16 + 4 * (lane >> 4));
  f32x4 acc[4][7];
#pragma unroll
  for (int i = 0; i < 4; ++i)
#pragma unroll
    for (int j = 0; j < 7; ++j) acc[i][j] = (f32x4){0.f, 0.f, 0.f, 0.f};
  const int fr = lane & 15, fg = lane >> 4;
  const uint32_t sbase = (uint32_t)(size_t)(smem);
  const uint32_t ard0 = sbase + (wm * 64 + fr) * 128 + ((fg ^ (fr & 7)) << 4);
  const int q4 = (lane & 15) >> 2, p4 = lane & 3;
  const uint32_t brd = sbase + G1_A_BYTES + (8 * fg + q4) * 448 + 8 * p4 + wn * 224;
  const int f32_ = (fg & 1) * 32;
  const uint32_t brdE = brd + ((wn & 1) ? -f32_ : f32_), brdO = brd + ((wn & 1) ? f32_ : -f32_);
  G1Frag X, Y;
  auto rd = [&](G1Frag& Fr, auto I, auto KS, uint32_t so) {
    constexpr int i = decltype(I)::v, ks = decltype(KS)::v;
    if constexpr (i < 4) {
      lds_rd128<i * 2048>(Fr.a[i], (ard0 ^ (ks * 64)) + so);
    } else {
      constexpr int ni = (i - 4) >> 1, t = (i - 4) & 1;
      lds_rdtr<ks * 32 * 448 + ni * 32 + t * 4 * 448>(Fr.b[ni][t], ((ni & 1) ? brdO : brdE) + so);
    }
  };
  auto mma1 = [&](G1Frag& Fr, auto J) {
    constexpr int j = decltype(J)::v, mi = j / 7, ni = j % 7;
    mfma16(acc[mi][ni], Fr.b[ni][0], Fr.b[ni][1], Fr.a[mi]);
  };
#define G1_TIE(Fr) asm volatile("s_waitcnt lgkmcnt(0)" : "+v"(Fr.a[0]), "+v"(Fr.a[1]), "+v"(Fr.a[2]), "+v"(Fr.a[3]), \
    "+v"(Fr.b[0][0]), "+v"(Fr.b[0][1]), "+v"(Fr.b[1][0]), "+v"(Fr.b[1][1]), "+v"(Fr.b[2][0]), "+v"(Fr.b[2][1]), "+v"(Fr.b[3][0]), "+v"(Fr.b[3][1]), \
    "+v"(Fr.b[4][0]), "+v"(Fr.b[4][1]), "+v"(Fr.b[5][0]), "+v"(Fr.b[5][1]), "+v"(Fr.b[6][0]), "+v"(Fr.b[6][1]) :: "memory")
  auto block = [&](G1Frag& C, G1Frag& R, auto KS, uint32_t so, auto DO_RD, auto DO_IS, int kt_is, int buf_is) {
    auto one = [&](auto J) {
      constexpr int j = decltype(J)::v;
      mma1(C, J);
      if constexpr (decltype(DO_RD)::v && j < 18) rd(R, J, KS, so);
      if constexpr (decltype(DO_IS)::v && j >= 6 && j < 28 && ((j - 6) & 1) == 0) issue1((j - 6) >> 1, kt_is, buf_is);
      SCHED0();
    };
    one(IC<0>{}); one(IC<1>{}); one(IC<2>{}); one(IC<3>{}); one(IC<4>{}); one(IC<5>{}); one(IC<6>{});
    one(IC<7>{}); one(IC<8>{}); one(IC<9>{}); one(IC<10>{}); one(IC<11>{}); one(IC<12>{}); one(IC<13>{});
    one(IC<14>{}); one(IC<15>{}); one(IC<16>{}); one(IC<17>{}); one(IC<18>{}); one(IC<19>{}); one(IC<20>{});
    one(IC<21>{}); one(IC<22>{}); one(IC<23>{}); one(IC<24>{}); one(IC<25>{}); one(IC<26>{}); one(IC<27>{});
  };
  auto rdall = [&](G1Frag& R, uint32_t so) {
    rd(R, IC<0>{}, IC<0>{}, so); rd(R, IC<1>{}, IC<0>{}, so); rd(R, IC<2>{}, IC<0>{}, so); rd(R, IC<3>{}, IC<0>{}, so);
    rd(R, IC<4>{}, IC<0>{}, so); rd(R, IC<5>{}, IC<0>{}, so); rd(R, IC<6>{}, IC<0>{}, so); rd(R, IC<7>{}, IC<0>{}, so);
    rd(R, IC<8>{}, IC<0>{}, so); rd(R, IC<9>{}, IC<0>{}, so); rd(R, IC<10>{}, IC<0>{}, so); rd(R, IC<11>{}, IC<0>{}, so);
    rd(R, IC<12>{}, IC<0>{}, so); rd(R, IC<13>{}, IC<0>{}, so); rd(R, IC<14>{}, IC<0>{}, so); rd(R, IC<15>{}, IC<0>{}, so);
    rd(R, IC<16>{}, IC<0>{}, so); rd(R, IC<17>{}, IC<0>{}, so);
  };
  __builtin_amdgcn_s_setprio(2);
  RAW_BAR();
  rdall(X, 0);
  int buf = 0;
  for (int kt = 0; kt < NK - 3; ++kt) {
    const int nbuf = buf == 2 ? 0 : buf + 1;
    G1_TIE(X); SCHED0();
    block(X, Y, IC<1>{}, buf * G1_STAGE, IC<1>{}, IC<0>{}, 0, 0);
    G1_TIE(Y); RAW_BAR(); SCHED0();
    block(Y, X, IC<0>{}, nbuf * G1_STAGE, IC<1>{}, IC<0>{}, 0, 0);
    buf = nbuf;
  }
  G1_TIE(X); SCHED0();
  block(X, Y, IC<1>{}, 1 * G1_STAGE, IC<1>{}, IC<0>{}, 0, 0);
  G1_TIE(Y); RAW_BAR(); SCHED0();
  block(Y, X, IC<0>{}, 2 * G1_STAGE, IC<1>{}, IC<0>{}, 0, 0);
  G1_TIE(X); SCHED0();
  block(X, Y, IC<1>{}, 2 * G1_STAGE, IC<1>{}, IC<0>{}, 0, 0);
  G1_TIE(Y); RAW_BAR(); SCHED0();
  block(Y, X, IC<0>{}, 0 * G1_STAGE, IC<1>{}, IC<0>{}, 0, 0);
  G1_TIE(X); SCHED0();
  block(X, Y, IC<1>{}, 0 * G1_STAGE, IC<1>{}, IC<0>{}, 0, 0);
  G1_TIE(Y); SCHED0();
  block(Y, X, IC<0>{}, 0, IC<0>{}, IC<0>{}, 0, 0);
  asm volatile("s_nop 15\n\ts_nop 15" : "+v"(acc[0][0]), "+v"(acc[0][1]), "+v"(acc[0][2]), "+v"(acc[0][3]), "+v"(acc[0][4]), "+v"(acc[0][5]), "+v"(acc[0][6]), "+v"(acc[1][0]), "+v"(acc[1][1]), "+v"(acc[1][2]), "+v"(acc[1][3]), "+v"(acc[1][4]), "+v"(acc[1][5]), "+v"(acc[1][6]), "+v"(acc[2][0]), "+v"(acc[2][1]), "+v"(acc[2][2]), "+v"(acc[2][3]), "+v"(acc[2][4]), "+v"(acc[2][5]), "+v"(acc[2][6]), "+v"(acc[3][0]), "+v"(acc[3][1]), "+v"(acc[3][2]), "+v"(acc[3][3]), "+v"(acc[3][4]), "+v"(acc[3][5]), "+v"(acc[3][6]) :: "memory");
  SCHED0();
  __builtin_amdgcn_s_setprio(0);
  RAW_BAR();
  {
    f32x4* xfer = reinterpret_cast<f32x4*>(smem) + w * (14 * 64) + lane;
#pragma unroll
    for (int mi = 2; mi < 4; ++mi)
#pragma unroll
      for (int ni = 0; ni < 7; ++ni) xfer[((mi - 2) * 7 + ni) * 64] = acc[mi][ni];
  }
  WAIT_L0();
  RAW_BAR();
  char* ot = smem + G1_OT + (wm * 64 + fr) * G1_OTS + (wn * 112 + 4 * fg) * 2;
#pragma unroll
  for (int ni = 0; ni < 7; ++ni) {
    const f32x2 b01 = unpk(bb[ni].x), b23 = unpk(bb[ni].y);
#pragma unroll
    for (int mi = 0; mi < 2; ++mi) {
      const f32x4 c = acc[mi][ni];
      *reinterpret_cast<uint2*>(ot + mi * 16 * G1_OTS + ni * 32) = make_uint2(mish_pair(c[0], c[1], b01), mish_pair(c[2], c[3], b23));
    }
  }
  g1_store();
}

constexpr int G2_A_BYTES = 128 * 128, G2_B_BYTES = 64 * 256, G2_STAGE = G2_A_BYTES + G2_B_BYTES;
struct G2Frag { u32x4 a[4]; u32x2 b[4][2]; };
__device__ __forceinline__ void g2_body(char* smem, const u16* __restrict__ A, const u16* __restrict__ W, const u16* __restrict__ h,
                                        u16* __restrict__ out, float* __restrict__ part, unsigned* __restrict__ flags) {
  const int tid = threadIdx.x, lane = tid & 63, w8 = __builtin_amdgcn_readfirstlane(tid >> 6), w = w8 & 3;
  const int wm = w >> 1, wn = w & 1;
  const int bid = blockIdx.x, xcd = bid & 7, idx = bid >> 3;
  const int kh = xcd & 1, tm = ((xcd >> 1) & 1) * 8 + (idx & 7), tn = (xcd >> 2) * 4 + (idx >> 3);
  const int m0 = tm * 128, n0 = tn * 128, tile = tm * 8 + tn;
  auto g2_store = [&]() {
    WAIT_L0();
    RAW_BAR();
#pragma unroll
    for (int i = 0; i < 2; ++i) {
      const int c = tid + 512 * i, r = c >> 4, q = c & 15;
      const int row = (r >> 5) * 64 + kh * 32 + (r & 31);
      const uint4 v = *reinterpret_cast<const uint4*>(smem + r * 272 + q * 16);
      *reinterpret_cast<uint4*>(reinterpret_cast<char*>(out) + (size_t)(m0 + row) * (D_ * 2) + n0 * 2 + q * 16) = v;
    }
  };
  constexpr int NK = 28;
  if (w8 >= 4) {
    uint32_t aoff[4], boff[4];
#pragma unroll
    for (int i = 0; i < 4; ++i) {
      const int r = w * 32 + i * 8 + (lane >> 3);
      aoff[i] = (uint32_t)(m0 + r) * (uint32_t)(F_ * 2) + (uint32_t)(((lane & 7) ^ (r & 7)) << 4);
    }
#pragma unroll
    for (int i = 0; i < 4; ++i) {
      const int k = w * 16 + i * 4 + (lane >> 4), q = lane & 15;
      const int c = (q >> 1) ^ ((k & 3) | (((k >> 3) & 1) << 2));
      boff[i] = (uint32_t)k * (uint32_t)(D_ * 2) + (uint32_t)(n0 * 2) + (uint32_t)(((c << 1) | (q & 1)) << 4);
    }
    const char* Ab = reinterpret_cast<const char*>(A) + kh * (1792 * 2);
    const char* Wb = reinterpret_cast<const char*>(W) + (size_t)kh * (1792 * D_ * 2);
    auto issueA = [&](int kt, int buf) {
#pragma unroll
      for (int i = 0; i < 4; ++i)
        __builtin_amdgcn_global_load_lds((const unsigned*)(Ab + kt * 128 + aoff[i]),
                                         (unsigned*)(smem + buf * G2_STAGE + w * 4096 + lane * 16 + i * 1024), 16, 0, 16);
    };
    auto issueB = [&](int kt, int buf) {
#pragma unroll
      for (int i = 0; i < 4; ++i)
        __builtin_amdgcn_global_load_lds((const unsigned*)(Wb + (size_t)kt * (64 * D_ * 2) + boff[i]),
                                         (unsigned*)(smem + buf * G2_STAGE + G2_A_BYTES + w * 4096 + lane * 16 + i * 1024), 16, 0, 0);
    };
    auto issue = [&](int kt, int buf) { issueA(kt, buf); issueB(kt, buf); };
    issueB(0, 0); issueB(1, 1);
    if (lane == 0) {
      for (int spin = 0; spin < (1 << 22); ++spin) {
        if (__hip_atomic_load((gu32*)(flags + 4096 + (tm * 2 + kh) * 16), __ATOMIC_RELAXED, __HIP_MEMORY_SCOPE_AGENT) == 8u) break;
        __builtin_amdgcn_s_sleep(1);
      }
    }
    asm volatile("" ::: "memory");
    issueA(0, 0); issueA(1, 1);
    WAIT_V(4); RAW_BAR();
    issue(2, 2); issue(3, 3);
    for (int kt = 0; kt < NK - 4; ++kt) {
      WAIT_V(16); RAW_BAR();
      issue(kt + 4, kt & 3);
    }
    WAIT_V(16); RAW_BAR();
    WAIT_V(8); RAW_BAR();
    WAIT_V(0); RAW_BAR();
    RAW_BAR();
    RAW_BAR();
    g2_store();
    return;
  }
  const int fr = lane & 15, fg = lane >> 4;
  u32x2 hv[2][4];
  f32x4 acc[4][4];
#pragma unroll
  for (int i = 0; i < 4; ++i)
#pragma unroll
    for (int j = 0; j < 4; ++j) acc[i][j] = (f32x4){0.f, 0.f, 0.f, 0.f};
  const uint32_t sbase = (uint32_t)(size_t)(smem);
  const uint32_t ard0 = sbase + (wm * 64 + fr) * 128 + ((fg ^ (fr & 7)) << 4);
  const int q4 = (lane & 15) >> 2, p4 = lane & 3;
  const int bsw = q4 | ((fg & 1) << 2);
  const uint32_t brd = sbase + G2_A_BYTES + (8 * fg + q4) * 256 + 8 * p4;
  uint32_t brdn[4];
#pragma unroll
  for (int ni = 0; ni < 4; ++ni) brdn[ni] = brd + (((wn * 4 + ni) ^ bsw) * 32);
  G2Frag X, Y;
  auto rd = [&](G2Frag& Fr, auto I, auto KS, uint32_t so) {
    constexpr int i = decltype(I)::v, ks = decltype(KS)::v;
    if constexpr (i < 4) {
      lds_rd128<i * 2048>(Fr.a[i], (ard0 ^ (ks * 64)) + so);
    } else {
      constexpr int ni = (i - 4) >> 1, t = (i - 4) & 1;
      lds_rdtr<ks * 32 * 256 + t * 4 * 256>(Fr.b[ni][t], brdn[ni] + so);
    }
  };
  auto mma1 = [&](G2Frag& Fr, auto J) {
    constexpr int j = decltype(J)::v, mi = j / 4, ni = j % 4;
    mfma16(acc[mi][ni], Fr.b[ni][0], Fr.b[ni][1], Fr.a[mi]);
  };
#define G2_TIE(Fr) asm volatile("s_waitcnt lgkmcnt(0)" : "+v"(Fr.a[0]), "+v"(Fr.a[1]), "+v"(Fr.a[2]), "+v"(Fr.a[3]), \
    "+v"(Fr.b[0][0]), "+v"(Fr.b[0][1]), "+v"(Fr.b[1][0]), "+v"(Fr.b[1][1]), "+v"(Fr.b[2][0]), "+v"(Fr.b[2][1]), "+v"(Fr.b[3][0]), "+v"(Fr.b[3][1]) :: "memory")
  auto block = [&](G2Frag& C, G2Frag& R, auto KS, uint32_t so, auto DO_RD) {
    auto one = [&](auto J) {
      constexpr int j = decltype(J)::v;
      mma1(C, J);
      if constexpr (decltype(DO_RD)::v && j < 12) rd(R, J, KS, so);
      SCHED0();
    };
    one(IC<0>{}); one(IC<1>{}); one(IC<2>{}); one(IC<3>{}); one(IC<4>{}); one(IC<5>{}); one(IC<6>{}); one(IC<7>{});
    one(IC<8>{}); one(IC<9>{}); one(IC<10>{}); one(IC<11>{}); one(IC<12>{}); one(IC<13>{}); one(IC<14>{}); one(IC<15>{});
  };
  __builtin_amdgcn_s_setprio(2);
  RAW_BAR();
  {
    const auto hrs = __builtin_amdgcn_make_buffer_rsrc(const_cast<u16*>(h), 0, S_ * D_ * 2, 0x00020000);
#pragma unroll
    for (int i = 0; i < 2; ++i)
#pragma unroll
      for (int ni = 0; ni < 4; ++ni)
        hv[i][ni] = __builtin_amdgcn_raw_buffer_load_b64(hrs, (uint32_t)((m0 + wm * 64 + (2 * kh + i) * 16 + fr) * D_ + n0 + wn * 64 + ni * 16 + 4 * fg) * 2u, 0, 16);
  }
  rd(X, IC<0>{}, IC<0>{}, 0); rd(X, IC<1>{}, IC<0>{}, 0); rd(X, IC<2>{}, IC<0>{}, 0); rd(X, IC<3>{}, IC<0>{}, 0);
  rd(X, IC<4>{}, IC<0>{}, 0); rd(X, IC<5>{}, IC<0>{}, 0); rd(X, IC<6>{}, IC<0>{}, 0); rd(X, IC<7>{}, IC<0>{}, 0);
  rd(X, IC<8>{}, IC<0>{}, 0); rd(X, IC<9>{}, IC<0>{}, 0); rd(X, IC<10>{}, IC<0>{}, 0); rd(X, IC<11>{}, IC<0>{}, 0);
  for (int kt = 0; kt < NK - 1; ++kt) {
    G2_TIE(X); SCHED0();
    block(X, Y, IC<1>{}, (kt & 3) * G2_STAGE, IC<1>{});
    G2_TIE(Y); RAW_BAR(); SCHED0();
    block(Y, X, IC<0>{}, ((kt + 1) & 3) * G2_STAGE, IC<1>{});
  }
  G2_TIE(X); SCHED0();
  block(X, Y, IC<1>{}, ((NK - 1) & 3) * G2_STAGE, IC<1>{});
  G2_TIE(Y); SCHED0();
  block(Y, X, IC<0>{}, 0, IC<0>{});
  asm volatile("s_nop 15\n\ts_nop 15" : "+v"(acc[0][0]), "+v"(acc[0][1]), "+v"(acc[0][2]), "+v"(acc[0][3]), "+v"(acc[1][0]), "+v"(acc[1][1]), "+v"(acc[1][2]), "+v"(acc[1][3]), "+v"(acc[2][0]), "+v"(acc[2][1]), "+v"(acc[2][2]), "+v"(acc[2][3]), "+v"(acc[3][0]), "+v"(acc[3][1]), "+v"(acc[3][2]), "+v"(acc[3][3]) :: "memory");
  SCHED0();
  const auto rs = __builtin_amdgcn_make_buffer_rsrc(part, 0, 256 * 32768, 0x00020000);
  const uint32_t myoff = (uint32_t)(tile * 2 + kh) * 32768u + (uint32_t)w * 8192u + (uint32_t)lane * 16u;
  const uint32_t proff = (uint32_t)(tile * 2 + (kh ^ 1)) * 32768u + (uint32_t)w * 8192u + (uint32_t)lane * 16u;
  auto send = [&](const f32x4 (&r0)[4], const f32x4 (&r1)[4]) {
#pragma unroll
    for (int ni = 0; ni < 4; ++ni) {
      __builtin_amdgcn_raw_buffer_store_b128(__builtin_bit_cast(u32x4, r0[ni]), rs, myoff + ni * 1024, 0, 16);
      __builtin_amdgcn_raw_buffer_store_b128(__builtin_bit_cast(u32x4, r1[ni]), rs, myoff + 4096 + ni * 1024, 0, 16);
    }
  };
  if (kh == 0) send(acc[2], acc[3]); else send(acc[0], acc[1]);
  WAIT_V(0);
  RAW_BAR();
  if (w == 0) {
    if (lane == 0) {
      __hip_atomic_store((gu32*)(flags + (tile * 2 + kh) * 16), 1u, __ATOMIC_RELAXED, __HIP_MEMORY_SCOPE_AGENT);
      for (int spin = 0; spin < (1 << 22); ++spin) {
        if (__hip_atomic_load((gu32*)(flags + (tile * 2 + (kh ^ 1)) * 16), __ATOMIC_RELAXED, __HIP_MEMORY_SCOPE_AGENT) == 1u) break;
        __builtin_amdgcn_s_sleep(1);
      }
    }
  }
  asm volatile("" ::: "memory");
  RAW_BAR();
  asm volatile("" ::: "memory");
  auto fin = [&](auto I, const f32x4 (&own)[4]) {
    constexpr int i = decltype(I)::v;
    char* ot = smem + (wm * 32 + i * 16 + fr) * 272 + wn * 128 + 8 * fg;
    f32x4 pr[4];
#pragma unroll
    for (int ni = 0; ni < 4; ++ni) pr[ni] = __builtin_bit_cast(f32x4, __builtin_amdgcn_raw_buffer_load_b128(rs, proff + i * 4096 + ni * 1024, 0, 16));
#pragma unroll
    for (int ni = 0; ni < 4; ++ni) {
      const f32x4 c = kh == 0 ? own[ni] + pr[ni] : pr[ni] + own[ni];
      const f32x2 r01 = unpk(hv[i][ni][0]) + unpk(cvt_pk(c[0], c[1])), r23 = unpk(hv[i][ni][1]) + unpk(cvt_pk(c[2], c[3]));
      *reinterpret_cast<uint2*>(ot + ni * 32) = make_uint2(cvt_pk(r01[0], r01[1]), cvt_pk(r23[0], r23[1]));
    }
  };
  if (kh == 0) { fin(IC<0>{}, acc[0]); fin(IC<1>{}, acc[1]); }
  else { fin(IC<0>{}, acc[2]); fin(IC<1>{}, acc[3]); }
  g2_store();
}

__global__ __launch_bounds__(512, 2) void k3g_fused(const c2* __restrict__ U, const u16* __restrict__ x, u16* __restrict__ A,
                                                     const u16* __restrict__ W1, const u16* __restrict__ b1,
                                                     u16* __restrict__ act, const u16* __restrict__ W2, u16* __restrict__ h,
                                                     u16* __restrict__ out, float* __restrict__ part, unsigned* __restrict__ flags) {
  __shared__ __attribute__((aligned(16))) char smem[3 * G1_STAGE];
  k3_body(smem, U, x, h, A, reinterpret_cast<const char*>(W2));
  WAIT_V(0);
  WAIT_L0();
  RAW_BAR();
  if (threadIdx.x == 0)
    __hip_atomic_fetch_add((gu32*)(flags + 8192 + (blockIdx.x >> 4) * 16), 1u, __ATOMIC_RELAXED, __HIP_MEMORY_SCOPE_AGENT);
  g1_body(smem, A, W1, b1, act, flags);
  WAIT_V(0);
  RAW_BAR();
  if (threadIdx.x == 0) {
    const int xcd = blockIdx.x & 7, idx = blockIdx.x >> 3;
    const int tm = (xcd & 1) * 8 + (idx & 7), tn = (xcd >> 1) * 4 + (idx >> 3);
    __hip_atomic_fetch_add((gu32*)(flags + 4096 + (tm * 2 + (tn >> 3)) * 16), 1u, __ATOMIC_RELAXED, __HIP_MEMORY_SCOPE_AGENT);
  }
  g2_body(smem, act, W2, h, out, part, flags);
}

extern "C" void kernel_launch(void* const* d_in, const int* in_sizes, int n_in,
                              void* d_out, int out_size, void* d_ws, size_t ws_size,
                              hipStream_t stream) {
  const u16* x = (const u16*)d_in[0];
  const u16* wr = (const u16*)d_in[1];
  const u16* wi = (const u16*)d_in[2];
  const u16* w1 = (const u16*)d_in[4];
  const u16* b1 = (const u16*)d_in[5];
  const u16* w2 = (const u16*)d_in[6];
  u16* out = (u16*)d_out;
  char* ws = (char*)d_ws;
  c2* T = (c2*)ws;
  uint32_t* WT = (uint32_t*)(ws + (9u << 20));
  u16* h = (u16*)(ws + (16u << 20));
  u16* a = (u16*)(ws + (20u << 20));
  u16* act = (u16*)(ws + (24u << 20));
  float* part = (float*)(ws + (40u << 20));
  unsigned* flags = (unsigned*)(ws + (48u << 20));
  k1_ln_rowfft<<<256, 512, 0, stream>>>(x, wr, wi, T, WT, flags);
  c2* U = (c2*)(ws + (50u << 20));
  k2_colfft<<<512, 256, 0, stream>>>(T, U, WT, (const char*)w1);
  k3g_fused<<<256, 512, 0, stream>>>(U, x, a, w1, b1, act, w2, h, out, part, flags);
}
```

```cpp
#include <hip/hip_runtime.h>
#include <hip/hip_bf16.h>
#include <stdint.h>

typedef unsigned short u16;
typedef __attribute__((ext_vector_type(8))) short bf16x8;
typedef __attribute__((ext_vector_type(4))) short bf16x4;
typedef __attribute__((ext_vector_type(4))) float f32x4;
typedef __attribute__((address_space(3))) bf16x4* lds_b4_ptr;

constexpr int TBS = 4096 + 16;
constexpr int S_ = 2048, D_ = 1024, F_ = 3584, DF_ = 513;

__device__ __forceinline__ float bf2f(u16 v) { return __uint_as_float(((uint32_t)v) << 16); }
__device__ __forceinline__ u16 f2bf(float f) { __bf16 b = (__bf16)f; return __builtin_bit_cast(u16, b); }
__device__ __forceinline__ float rbf(float f) { return bf2f(f2bf(f)); }

typedef __attribute__((ext_vector_type(4))) unsigned int u32v4;
typedef __attribute__((ext_vector_type(4))) float f32v4;
template <typename T> __device__ __forceinline__ void st_nt(T* p, T v) { __builtin_nontemporal_store(v, p); }
typedef __attribute__((ext_vector_type(2))) float c2;
typedef c2 f32x2;
__device__ __forceinline__ uint32_t cvt_pk(float lo, float hi) { uint32_t r; asm("v_cvt_pk_bf16_f32 %0, %1, %2" : "=v"(r) : "v"(lo), "v"(hi)); return r; }
__device__ __forceinline__ c2 unpk(uint32_t u) { return (c2){__uint_as_float(u << 16), __uint_as_float(u & 0xffff0000u)}; }
__device__ __forceinline__ c2 rbf2(c2 v) { return unpk(cvt_pk(v.x, v.y)); }
__device__ __forceinline__ c2 cmul(c2 a, c2 b) {
  c2 t, r;
  asm("v_pk_mul_f32 %0, %1, %2 op_sel:[0,0] op_sel_hi:[0,1]" : "=v"(t) : "v"(a), "v"(b));
  asm("v_pk_fma_f32 %0, %1, %2, %3 op_sel:[1,1,0] op_sel_hi:[1,0,1] neg_lo:[0,1,0]" : "=v"(r) : "v"(a), "v"(b), "v"(t));
  return r;
}
__device__ __forceinline__ c2 sub_i(c2 t, c2 d) { c2 r; asm("v_pk_add_f32 %0, %1, %2 op_sel:[0,1] op_sel_hi:[1,0] neg_hi:[0,1]" : "=v"(r) : "v"(t), "v"(d)); return r; }
__device__ __forceinline__ c2 add_i(c2 t, c2 d) { c2 r; asm("v_pk_add_f32 %0, %1, %2 op_sel:[0,1] op_sel_hi:[1,0] neg_lo:[0,1]" : "=v"(r) : "v"(t), "v"(d)); return r; }
__device__ __forceinline__ c2 add_c(c2 a, c2 b) { c2 r; asm("v_pk_add_f32 %0, %1, %2 neg_hi:[0,1]" : "=v"(r) : "v"(a), "v"(b)); return r; }
__device__ __forceinline__ c2 sub_c(c2 a, c2 b) { c2 r; asm("v_pk_add_f32 %0, %1, %2 neg_lo:[0,1]" : "=v"(r) : "v"(a), "v"(b)); return r; }
template <int DIR> __device__ __forceinline__ c2 addD(c2 t, c2 d) { return DIR > 0 ? add_i(t, d) : sub_i(t, d); }
template <int DIR> __device__ __forceinline__ c2 subD(c2 t, c2 d) { return DIR > 0 ? sub_i(t, d) : add_i(t, d); }
template <int DIR>
__device__ __forceinline__ c2 cexp2pi(float rev) {
  const float r = DIR > 0 ? rev : -rev;
  c2 w = {__builtin_amdgcn_cosf(r), __builtin_amdgcn_sinf(r)};
  asm("s_nop 0" : "+v"(w));
  return w;
}
template <int DIR>
__device__ __forceinline__ void dft4(c2& x0, c2& x1, c2& x2, c2& x3) {
  const c2 t0 = x0 + x2, t1 = x0 - x2, t2 = x1 + x3, d = x1 - x3;
  x0 = t0 + t2; x2 = t0 - t2; x1 = addD<DIR>(t1, d); x3 = subD<DIR>(t1, d);
}
template <int DIR>
__device__ __forceinline__ void dft8(c2 (&v)[8]) {
  c2 e0 = v[0], e1 = v[2], e2 = v[4], e3 = v[6];
  c2 o0 = v[1], o1 = v[3], o2 = v[5], o3 = v[7];
  dft4<DIR>(e0, e1, e2, e3);
  dft4<DIR>(o0, o1, o2, o3);
  const c2 r = {0.70710678118654752440f, 0.70710678118654752440f};
  const c2 u1 = addD<DIR>(o1, o1), u3 = subD<DIR>(o3, o3);
  v[0] = e0 + o0; v[4] = e0 - o0;
  v[1] = __builtin_elementwise_fma(u1, r, e1); v[5] = __builtin_elementwise_fma(u1, -r, e1);
  v[2] = addD<DIR>(e2, o2); v[6] = subD<DIR>(e2, o2);
  v[3] = __builtin_elementwise_fma(u3, -r, e3); v[7] = __builtin_elementwise_fma(u3, r, e3);
}
__device__ __forceinline__ void twiddle8(c2 (&v)[8], c2 w) {
  const c2 w2 = cmul(w, w), w3 = cmul(w2, w), w4 = cmul(w2, w2), w5 = cmul(w4, w), w6 = cmul(w3, w3), w7 = cmul(w4, w3);
  v[1] = cmul(v[1], w); v[2] = cmul(v[2], w2); v[3] = cmul(v[3], w3); v[4] = cmul(v[4], w4);
  v[5] = cmul(v[5], w5); v[6] = cmul(v[6], w6); v[7] = cmul(v[7], w7);
}
__device__ __forceinline__ void twiddle8b(c2 (&v)[8], c2 b, c2 w) {
  const c2 w2 = cmul(w, w), w4 = cmul(w2, w2);
  const c2 b1 = cmul(b, w), b2 = cmul(b, w2), b3 = cmul(b1, w2), b4 = cmul(b, w4), b5 = cmul(b1, w4), b6 = cmul(b2, w4), b7 = cmul(b3, w4);
  v[0] = cmul(v[0], b); v[1] = cmul(v[1], b1); v[2] = cmul(v[2], b2); v[3] = cmul(v[3], b3);
  v[4] = cmul(v[4], b4); v[5] = cmul(v[5], b5); v[6] = cmul(v[6], b6); v[7] = cmul(v[7], b7);
}

__device__ __forceinline__ float wave_sum(float v) {
#define DPP_ADD(ctrl, rmask) v += __uint_as_float(__builtin_amdgcn_update_dpp(0u, __float_as_uint(v), ctrl, rmask, 0xf, true))
  DPP_ADD(0x111, 0xf); DPP_ADD(0x112, 0xf); DPP_ADD(0x114, 0xf); DPP_ADD(0x118, 0xf);
  DPP_ADD(0x142, 0xa); DPP_ADD(0x143, 0xc);
#undef DPP_ADD
  return __uint_as_float(__builtin_amdgcn_readlane(__float_as_uint(v), 63));
}

#define WAVE_SYNC() do { __builtin_amdgcn_fence(__ATOMIC_RELEASE, "wavefront"); __builtin_amdgcn_wave_barrier(); __builtin_amdgcn_fence(__ATOMIC_ACQUIRE, "wavefront"); } while (0)

template <int DIR>
__device__ __forceinline__ void fft512_wave(c2 (&v)[8], c2* ex, int lane) {
  const int hi = lane >> 3, lo = lane & 7;
  dft8<DIR>(v);
  twiddle8(v, cexp2pi<DIR>((float)hi * (1.0f / 64.0f)));
#pragma unroll
  for (int k0 = 0; k0 < 8; ++k0) ex[72 * k0 + lane] = v[k0];
  WAVE_SYNC();
#pragma unroll
  for (int b = 0; b < 8; ++b) v[b] = ex[72 * hi + 8 * b + lo];
  WAVE_SYNC();
  dft8<DIR>(v);
  twiddle8b(v, cexp2pi<DIR>((float)(lo * hi) * (1.0f / 512.0f)), cexp2pi<DIR>((float)lo * (1.0f / 64.0f)));
#pragma unroll
  for (int k1 = 0; k1 < 8; ++k1) ex[66 * lo + 8 * k1 + hi] = v[k1];
  WAVE_SYNC();
#pragma unroll
  for (int c = 0; c < 8; ++c) v[c] = ex[66 * c + lane];
  WAVE_SYNC();
  dft8<DIR>(v);
}

__device__ __forceinline__ void ln_bf16(c2 (&e)[8], uint32_t (&o)[8]) {
  c2 s2 = ((e[0] + e[1]) + (e[2] + e[3])) + ((e[4] + e[5]) + (e[6] + e[7]));
  const float mean = wave_sum(s2.x + s2.y) * (1.0f / 1024.0f);
  c2 q2 = {0.f, 0.f};
#pragma unroll
  for (int i = 0; i < 8; ++i) { const c2 d = e[i] - mean; q2 = __builtin_elementwise_fma(d, d, q2); }
  const float var = wave_sum(q2.x + q2.y) * (1.0f / 1024.0f);
  const float mb = rbf(mean);
  const float sd = rbf(sqrtf(rbf(rbf(var) + 1e-5f)));
  const float ri = 1.0f / sd;
#pragma unroll
  for (int i = 0; i < 8; ++i) {
    const c2 d = rbf2(e[i] - mb);
    const c2 q = d * ri;
    const c2 rem = __builtin_elementwise_fma(-q, (c2){sd, sd}, d);
    const c2 qq = __builtin_elementwise_fma(rem, (c2){ri, ri}, q);
    o[i] = cvt_pk(qq.x, qq.y);
  }
}

__global__ __launch_bounds__(512) void k1_ln_rowfft(const u16* __restrict__ x, const u16* __restrict__ wr, const u16* __restrict__ wi,
                                                     c2* __restrict__ T, uint32_t* __restrict__ WT, unsigned* __restrict__ flags) {
  __shared__ c2 ex_all[8 * 576];
  __shared__ c2 st[512 * 9];
  const int tid = threadIdx.x, lane = tid & 63, w = tid >> 6;
  const int s0 = blockIdx.x * 8;
  const int row = s0 + w;
  const uint32_t* xr = reinterpret_cast<const uint32_t*>(x + (size_t)row * D_);
  uint32_t xu[8];
#pragma unroll
  for (int a = 0; a < 8; ++a) xu[a] = xr[lane + 64 * a];
  if (tid == 0) {
    flags[blockIdx.x * 16] = 0;
    if (blockIdx.x < 32) flags[4096 + blockIdx.x * 16] = 0;
    if (blockIdx.x < 16) flags[8192 + blockIdx.x * 16] = 0;
  }
  {
    uint32_t p[8];
#pragma unroll
    for (int r = 0; r < 8; ++r) {
      const size_t o = (size_t)(s0 + r) * DF_ + tid;
      p[r] = (uint32_t)wr[o] | ((uint32_t)wi[o] << 16);
    }
    u32v4* dst = reinterpret_cast<u32v4*>(WT + (size_t)tid * S_ + s0);
    st_nt(dst, (u32v4){p[0], p[1], p[2], p[3]});
    st_nt(dst + 1, (u32v4){p[4], p[5], p[6], p[7]});
    if (tid < 8) {
      const size_t o = (size_t)(s0 + tid) * DF_ + 512;
      WT[(size_t)512 * S_ + s0 + tid] = (uint32_t)wr[o] | ((uint32_t)wi[o] << 16);
    }
  }
  c2 v[8];
#pragma unroll
  for (int a = 0; a < 8; ++a) v[a] = unpk(xu[a]);
  ln_bf16(v, xu);
#pragma unroll
  for (int a = 0; a < 8; ++a) v[a] = unpk(xu[a]);
  c2* ex = ex_all + w * 576;
  fft512_wave<-1>(v, ex, lane);
#pragma unroll
  for (int j = 4; j < 8; ++j) ex[lane + 64 * j] = v[j];
  ex[512 + lane] = v[0];
  WAVE_SYNC();
  const c2 tw0 = cexp2pi<-1>((float)lane * (1.0f / 1024.0f));
  const c2 half = {0.5f, 0.5f};
#pragma unroll
  for (int j = 0; j < 4; ++j) {
    const int k = lane + 64 * j;
    const c2 z = v[j], zp = ex[512 - k];
    const c2 cj = j == 0 ? (c2){1.f, 0.f} : j == 1 ? (c2){0.92387953251128674f, -0.38268343236508977f}
                : j == 2 ? (c2){0.70710678118654752f, -0.70710678118654752f} : (c2){0.38268343236508977f, -0.92387953251128674f};
    const c2 tw = j == 0 ? tw0 : cmul(tw0, cj);
    const c2 E = add_c(z, zp) * half, Dz = sub_c(z, zp) * half;
    const c2 P = cmul(tw, Dz);
    c2 Xa = sub_i(E, P), Xb = add_i(E, P);
    Xb.y = -Xb.y;
    if (k == 0) { Xa = (c2){z.x + z.y, z.x - z.y}; Xb = (c2){v[4].x, -v[4].y}; }
    st[k * 9 + w] = Xa;
    st[(k == 0 ? 256 : 512 - k) * 9 + w] = Xb;
  }
  __syncthreads();
#pragma unroll
  for (int it = 0; it < 4; ++it) {
    const int idx = it * 512 + tid;
    const int k = idx >> 2, part = idx & 3;
    const c2 a = st[k * 9 + 2 * part], b = st[k * 9 + 2 * part + 1];
    st_nt(reinterpret_cast<f32v4*>(T + (size_t)k * S_ + s0) + part, (f32v4){a.x, a.y, b.x, b.y});
  }
}

#define KEEP(v) asm volatile("" :: "v"(v))
__device__ __forceinline__ uint32_t wt_at(const uint32_t* WT, int k1, int c) { return WT[(size_t)c * S_ + k1]; }
__global__ __launch_bounds__(256) void k2_colfft(const c2* __restrict__ T, c2* __restrict__ U, const uint32_t* __restrict__ WT, const char* __restrict__ pf) {
  __shared__ c2 ex[2304];
  const int t = threadIdx.x, col = blockIdx.x;
  c2 v[8];
#pragma unroll
  for (int a = 0; a < 8; ++a) v[a] = T[(size_t)col * S_ + t + 256 * a];
  uint32_t wv[8];
#pragma unroll
  for (int r = 0; r < 8; ++r) wv[r] = wt_at(WT, t + 256 * r, col);
  uint32_t pfv = 0;
  if (t < 112) pfv = *reinterpret_cast<const uint32_t*>(pf + ((size_t)col * 112 + t) * 128);
  c2* Tg = U + (size_t)(t >> 3) * TBS + col * 8 + (t & 7);
  const int h5 = t >> 5, l5 = t & 31;
  const int m3 = (t >> 2) & 7, d2 = t & 3;
  dft8<-1>(v);
  twiddle8(v, cexp2pi<-1>((float)t * (1.0f / 2048.0f)));
#pragma unroll
  for (int k0 = 0; k0 < 8; ++k0) ex[256 * k0 + t] = v[k0];
  __syncthreads();
#pragma unroll
  for (int b = 0; b < 8; ++b) v[b] = ex[256 * h5 + 32 * b + l5];
  __syncthreads();
  dft8<-1>(v);
  twiddle8(v, cexp2pi<-1>((float)l5 * (1.0f / 256.0f)));
#pragma unroll
  for (int k1 = 0; k1 < 8; ++k1) ex[288 * h5 + 36 * k1 + l5] = v[k1];
  WAVE_SYNC();
#pragma unroll
  for (int c = 0; c < 8; ++c) v[c] = ex[288 * h5 + 36 * m3 + 4 * c + d2];
  __syncthreads();
  dft8<-1>(v);
  twiddle8(v, cexp2pi<-1>((float)d2 * (1.0f / 32.0f)));
#pragma unroll
  for (int k2 = 0; k2 < 8; ++k2) ex[(4 * k2 + d2) * 66 + 8 * m3 + h5] = v[k2];
  __syncthreads();
  const int dd = t >> 6, l6 = t & 63;
#pragma unroll
  for (int d = 0; d < 4; ++d) { v[d] = ex[(4 * dd + d) * 66 + l6]; v[4 + d] = ex[(4 * (dd + 4) + d) * 66 + l6]; }
  __syncthreads();
  dft4<-1>(v[0], v[1], v[2], v[3]);
  dft4<-1>(v[4], v[5], v[6], v[7]);
  if (col == 0) {
#pragma unroll
    for (int k3 = 0; k3 < 4; ++k3) { ex[t + 512 * k3] = v[k3]; ex[t + 256 + 512 * k3] = v[4 + k3]; }
    __syncthreads();
  }
  if (col != 0) {
#pragma unroll
    for (int k3 = 0; k3 < 4; ++k3) {
      v[k3] = cmul(v[k3], unpk(wv[2 * k3]));
      v[4 + k3] = cmul(v[4 + k3], unpk(wv[2 * k3 + 1]));
    }
  } else {
#pragma unroll
    for (int r = 0; r < 8; ++r) {
      const int vi = (r & 1) * 4 + (r >> 1);
      const int k = t + 256 * r, kn = (2048 - k) & 2047;
      const c2 p = v[vi], pn = ex[kn];
      const c2 F0 = {0.5f * (p.x + pn.x), 0.5f * (p.y - pn.y)};
      const c2 F5 = {0.5f * (p.y + pn.y), 0.5f * (pn.x - p.x)};
      const c2 wa = unpk(wv[r]), wan = unpk(wt_at(WT, kn, 0));
      const c2 wb = unpk(wt_at(WT, k, 512)), wbn = unpk(wt_at(WT, kn, 512));
      const c2 W0 = {0.5f * (wa.x + wan.x), 0.5f * (wa.y - wan.y)};
      const c2 W5 = {0.5f * (wb.x + wbn.x), 0.5f * (wb.y - wbn.y)};
      const c2 H0 = cmul(F0, W0), H5 = cmul(F5, W5);
      v[vi] = add_i(H0, H5);
    }
  }
  if (col == 0) __syncthreads();
  dft4<1>(v[0], v[1], v[2], v[3]);
  dft4<1>(v[4], v[5], v[6], v[7]);
  {
    const c2 wa = cexp2pi<1>((float)dd * (1.0f / 32.0f)), wb = cexp2pi<1>((float)(dd + 4) * (1.0f / 32.0f));
    const c2 wa2 = cmul(wa, wa), wa3 = cmul(wa2, wa), wb2 = cmul(wb, wb), wb3 = cmul(wb2, wb);
    v[1] = cmul(v[1], wa); v[2] = cmul(v[2], wa2); v[3] = cmul(v[3], wa3);
    v[5] = cmul(v[5], wb); v[6] = cmul(v[6], wb2); v[7] = cmul(v[7], wb3);
  }
#pragma unroll
  for (int d = 0; d < 4; ++d) { ex[(4 * dd + d) * 66 + l6] = v[d]; ex[(4 * (dd + 4) + d) * 66 + l6] = v[4 + d]; }
  __syncthreads();
#pragma unroll
  for (int k2 = 0; k2 < 8; ++k2) v[k2] = ex[(4 * k2 + d2) * 66 + 8 * m3 + h5];
  __syncthreads();
  dft8<1>(v);
  twiddle8b(v, cexp2pi<1>((float)(m3 * d2) * (1.0f / 256.0f)), cexp2pi<1>((float)m3 * (1.0f / 64.0f)));
#pragma unroll
  for (int c = 0; c < 8; ++c) ex[288 * h5 + 36 * m3 + 4 * c + d2] = v[c];
  WAVE_SYNC();
#pragma unroll
  for (int k1 = 0; k1 < 8; ++k1) v[k1] = ex[288 * h5 + 36 * k1 + l5];
  __syncthreads();
  dft8<1>(v);
  twiddle8b(v, cexp2pi<1>((float)(h5 * l5) * (1.0f / 2048.0f)), cexp2pi<1>((float)h5 * (1.0f / 64.0f)));
#pragma unroll
  for (int b = 0; b < 8; ++b) ex[256 * h5 + 32 * b + l5] = v[b];
  __syncthreads();
#pragma unroll
  for (int k0 = 0; k0 < 8; ++k0) v[k0] = ex[256 * k0 + t];
  dft8<1>(v);
#pragma unroll
  for (int a = 0; a < 8; ++a) st_nt(Tg + (size_t)a * (32 * TBS), v[a]);
  KEEP(pfv);
}

__device__ __forceinline__ void k3_body(char* smem, const c2* __restrict__ T, const u16* __restrict__ x,
                                        u16* __restrict__ h, u16* __restrict__ a_out, const char* __restrict__ pf) {
  c2* ex_all = reinterpret_cast<c2*>(smem);
  c2* st = reinterpret_cast<c2*>(smem) + 8 * 576;
  const int tid = threadIdx.x, lane = tid & 63, w = tid >> 6;
  const int s0 = blockIdx.x * 8;
  const int row = s0 + w;
  float4 q4[4];
#pragma unroll
  for (int it = 0; it < 4; ++it) {
    const int idx = it * 512 + tid;
    q4[it] = reinterpret_cast<const float4*>(T + (size_t)blockIdx.x * TBS)[idx];
  }
  const uint32_t* xr = reinterpret_cast<const uint32_t*>(x + (size_t)row * D_);
  uint32_t xv[8];
#pragma unroll
  for (int a = 0; a < 8; ++a) xv[a] = xr[lane + 64 * a];
  uint32_t pfv = 0;
  if (tid < 224) pfv = *reinterpret_cast<const uint32_t*>(pf + ((size_t)blockIdx.x * 224 + tid) * 128);
#pragma unroll
  for (int it = 0; it < 4; ++it) {
    const int idx = it * 512 + tid;
    const int k = idx >> 2, part = idx & 3;
    st[k * 9 + 2 * part] = (c2){q4[it].x, q4[it].y};
    st[k * 9 + 2 * part + 1] = (c2){q4[it].z, q4[it].w};
  }
  __syncthreads();
  c2 v[8];
  const c2 tw0 = cexp2pi<1>((float)lane * (1.0f / 1024.0f));
#pragma unroll
  for (int j = 0; j < 8; ++j) {
    const int k = lane + 64 * j;
    const c2 g = st[k * 9 + w], gp = st[((512 - k) & 511) * 9 + w];
    const float cs[8] = {1.f, 0.92387953251128674f, 0.70710678118654752f, 0.38268343236508977f, 0.f, -0.38268343236508977f, -0.70710678118654752f, -0.92387953251128674f};
    const float sn[8] = {0.f, 0.38268343236508977f, 0.70710678118654752f, 0.92387953251128674f, 1.f, 0.92387953251128674f, 0.70710678118654752f, 0.38268343236508977f};
    const c2 tw = j == 0 ? tw0 : cmul(tw0, (c2){cs[j], sn[j]});
    c2 Z = add_i(add_c(g, gp), cmul(tw, sub_c(g, gp)));
    if (k == 0) Z = (c2){g.x + g.y, g.x - g.y};
    v[j] = Z;
  }
  c2* ex = ex_all + w * 576;
  fft512_wave<1>(v, ex, lane);
  const float scale = 1.0f / (2048.0f * 1024.0f);
  uint32_t* tr = reinterpret_cast<uint32_t*>(ex);
  const auto hrs = __builtin_amdgcn_make_buffer_rsrc(h, 0, S_ * D_ * 2, 0x00020000);
  const auto ars = __builtin_amdgcn_make_buffer_rsrc(a_out, 0, S_ * D_ * 2, 0x00020000);
  const uint32_t roff = (uint32_t)row * (D_ * 2) + lane * 16;
#pragma unroll
  for (int j = 0; j < 8; ++j) {
    const c2 hh = unpk(xv[j]) + rbf2(v[j] * scale);
    const uint32_t hu = cvt_pk(hh.x, hh.y);
    tr[lane + 64 * j] = hu;
    v[j] = unpk(hu);
  }
  WAVE_SYNC();
  {
    const u32v4 h0 = reinterpret_cast<const u32v4*>(tr)[lane], h1 = reinterpret_cast<const u32v4*>(tr)[64 + lane];
    __builtin_amdgcn_raw_buffer_store_b128(h0, hrs, roff, 0, 16);
    __builtin_amdgcn_raw_buffer_store_b128(h1, hrs, roff + 1024, 0, 16);
  }
  ln_bf16(v, xv);
#pragma unroll
  for (int j = 0; j < 8; ++j) tr[512 + lane + 64 * j] = xv[j];
  WAVE_SYNC();
  {
    const u32v4 a0 = reinterpret_cast<const u32v4*>(tr)[128 + lane], a1 = reinterpret_cast<const u32v4*>(tr)[192 + lane];
    __builtin_amdgcn_raw_buffer_store_b128(a0, ars, roff, 0, 16);
    __builtin_amdgcn_raw_buffer_store_b128(a1, ars, roff + 1024, 0, 16);
  }
  KEEP(pfv);
}

#define WAIT_V(n) asm volatile("s_waitcnt vmcnt(" #n ")" ::: "memory")
#define WAIT_L0() asm volatile("s_waitcnt lgkmcnt(0)" ::: "memory")
#define RAW_BAR() __builtin_amdgcn_s_barrier()

__device__ __forceinline__ uint32_t mish_pair(float c0, float c1, f32x2 b) {
  f32x2 x = unpk(cvt_pk(c0, c1)) + b;
  const f32x2 hh = unpk(cvt_pk(x[0], x[1]));
  f32x2 z = hh * 1.44269504088896340736f;
  f32x2 t = {__builtin_amdgcn_exp2f(__builtin_amdgcn_fmed3f(z[0], -200.0f, 43.0f)), __builtin_amdgcn_exp2f(__builtin_amdgcn_fmed3f(z[1], -200.0f, 43.0f))};
  const f32x2 n = t * (t + 2.0f);
  const f32x2 d = n + 2.0f;
  const f32x2 r = {__builtin_amdgcn_rcpf(d[0]), __builtin_amdgcn_rcpf(d[1])};
  const f32x2 o = (hh * n) * r;
  return cvt_pk(o[0], o[1]);
}

typedef __attribute__((ext_vector_type(4))) unsigned int u32x4;
typedef __attribute__((ext_vector_type(2))) unsigned int u32x2;
template <int OFF> __device__ __forceinline__ void lds_rd128(u32x4& d, uint32_t a) { asm volatile("ds_read_b128 %0, %1 offset:%2" : "=v"(d) : "v"(a), "i"(OFF)); }
template <int OFF> __device__ __forceinline__ void lds_rdtr(u32x2& d, uint32_t a) { asm volatile("ds_read_b64_tr_b16 %0, %1 offset:%2" : "=v"(d) : "v"(a), "i"(OFF)); }
__device__ __forceinline__ bf16x8 mk8(u32x2 lo, u32x2 hi) { u32x4 v = {lo[0], lo[1], hi[0], hi[1]}; return __builtin_bit_cast(bf16x8, v); }
#define SCHED0() __builtin_amdgcn_sched_barrier(0)
__device__ __forceinline__ void mfma16(f32x4& c, const u32x2& alo, const u32x2& ahi, const u32x4& b) {
  u32x4 a = {alo[0], alo[1], ahi[0], ahi[1]};
  asm volatile("v_mfma_f32_16x16x32_bf16 %0, %1, %2, %0" : "+v"(c) : "v"(a), "v"(b));
}
typedef __attribute__((address_space(1))) unsigned int gu32;
template <int N> struct IC { static constexpr int v = N; };

constexpr int G1_A_BYTES = 128 * 128, G1_B_BYTES = 64 * 448, G1_STAGE = G1_A_BYTES + G1_B_BYTES;
constexpr int G1_OT = 57344, G1_OTS = 464;
struct G1Frag { u32x4 a[4]; u32x2 b[7][2]; };
__device__ __forceinline__ void g1_body(char* smem, const u16* __restrict__ A, const u16* __restrict__ W, const u16* __restrict__ b1,
                                        u16* __restrict__ act, unsigned* __restrict__ flags) {
  const int tid = threadIdx.x, lane = tid & 63, w8 = __builtin_amdgcn_readfirstlane(tid >> 6), w = w8 & 3;
  const int wm = w >> 1, wn = w & 1;
  const int bid = blockIdx.x, xcd = bid & 7, idx = bid >> 3;
  const int tm = (xcd & 1) * 8 + (idx & 7), tn = (xcd >> 1) * 4 + (idx >> 3);
  const int m0 = tm * 128, n0 = tn * 224;
  uint32_t aoff[4], boff[7];
#pragma unroll
  for (int i = 0; i < 4; ++i) {
    const int r = w * 32 + i * 8 + (lane >> 3);
    aoff[i] = (uint32_t)(m0 + r) * 2048u + (uint32_t)(((lane & 7) ^ (r & 7)) << 4);
  }
#pragma unroll
  for (int i = 0; i < 7; ++i) {
    const int d = i * 64 + lane, r = d / 28, q = d - r * 28, k = w * 16 + r;
    const int c = (q >> 1) ^ ((k >> 3) & 1);
    boff[i] = (uint32_t)k * (uint32_t)(F_ * 2) + (uint32_t)(n0 * 2) + (uint32_t)(((c << 1) | (q & 1)) << 4);
  }
  const char* Ab = reinterpret_cast<const char*>(A);
  const char* Wb = reinterpret_cast<const char*>(W);
  auto issue1 = [&](int i, int kt, int buf) {
    if (i < 4) {
      __builtin_amdgcn_global_load_lds((const unsigned*)(Ab + kt * 128 + aoff[i]),
                                       (unsigned*)(smem + buf * G1_STAGE + w * 4096 + lane * 16 + i * 1024), 16, 0, 16);
    } else {
      __builtin_amdgcn_global_load_lds((const unsigned*)(Wb + (size_t)kt * (64 * F_ * 2) + boff[i - 4]),
                                       (unsigned*)(smem + buf * G1_STAGE + G1_A_BYTES + w * 7168 + lane * 16 + (i - 4) * 1024), 16, 0, 0);
    }
  };
  const auto ars = __builtin_amdgcn_make_buffer_rsrc(act, 0, S_ * F_ * 2, 0x00020000);
  auto g1_store = [&]() {
    WAIT_L0();
    RAW_BAR();
#pragma unroll
    for (int i = 0; i < 7; ++i) {
      const int c = tid + 512 * i, r = c / 28, q = c - r * 28;
      const u32v4 v = *reinterpret_cast<const u32v4*>(smem + G1_OT + r * G1_OTS + q * 16);
      __builtin_amdgcn_raw_buffer_store_b128(v, ars, (uint32_t)(m0 + r) * (uint32_t)(F_ * 2) + (uint32_t)(n0 * 2 + q * 16), 0, 16);
    }
  };
  constexpr int NK = 16;
  if (w8 >= 4) {
#pragma unroll
    for (int i = 4; i < 11; ++i) issue1(i, 0, 0);
#pragma unroll
    for (int i = 4; i < 11; ++i) issue1(i, 1, 1);
    if (lane == 0) {
      for (int spin = 0; spin < (1 << 22); ++spin) {
        if (__hip_atomic_load((gu32*)(flags + 8192 + tm * 16), __ATOMIC_RELAXED, __HIP_MEMORY_SCOPE_AGENT) == 16u) break;
        __builtin_amdgcn_s_sleep(1);
      }
    }
    asm volatile("" ::: "memory");
#pragma unroll
    for (int i = 0; i < 4; ++i) issue1(i, 0, 0);
#pragma unroll
    for (int i = 0; i < 4; ++i) issue1(i, 1, 1);
    WAIT_V(4); RAW_BAR();
#pragma unroll
    for (int i = 0; i < 11; ++i) issue1(i, 2, 2);
    int buf = 0;
    for (int kt = 0; kt < NK - 3; ++kt) {
      WAIT_V(11); RAW_BAR();
#pragma unroll
      for (int i = 0; i < 11; ++i) issue1(i, kt + 3, buf);
      buf = buf == 2 ? 0 : buf + 1;
    }
    WAIT_V(11); RAW_BAR();
    WAIT_V(0); RAW_BAR();
    uint2 bb[7];
#pragma unroll
    for (int ni = 0; ni < 7; ++ni) bb[ni] = *reinterpret_cast<const uint2*>(b1 + n0 + wn * 112 + ni * 16 + 4 * (lane >> 4));
    RAW_BAR(); RAW_BAR();
    const f32x4* xfer = reinterpret_cast<const f32x4*>(smem) + w * (14 * 64) + lane;
    char* ot = smem + G1_OT + (wm * 64 + (lane & 15)) * G1_OTS + (wn * 112 + 4 * (lane >> 4)) * 2;
#pragma unroll
    for (int ni = 0; ni < 7; ++ni) {
      const f32x2 b01 = unpk(bb[ni].x), b23 = unpk(bb[ni].y);
#pragma unroll
      for (int mi = 2; mi < 4; ++mi) {
        const f32x4 c = xfer[((mi - 2) * 7 + ni) * 64];
        *reinterpret_cast<uint2*>(ot + mi * 16 * G1_OTS + ni * 32) = make_uint2(mish_pair(c[0], c[1], b01), mish_pair(c[2], c[3], b23));
      }
    }
    g1_store();
    return;
  }
  uint2 bb[7];
#pragma unroll
  for (int ni = 0; ni < 7; ++ni) bb[ni] = *reinterpret_cast<const uint2*>(b1 + n0 + wn * 112 + ni * 16 + 4 * (lane >> 4));
  f32x4 acc[4][7];
#pragma unroll
  for (int i = 0; i < 4; ++i)
#pragma unroll
    for (int j = 0; j < 7; ++j) acc[i][j] = (f32x4){0.f, 0.f, 0.f, 0.f};
  const int fr = lane & 15, fg = lane >> 4;
  const uint32_t sbase = (uint32_t)(size_t)(smem);
  const uint32_t ard0 = sbase + (wm * 64 + fr) * 128 + ((fg ^ (fr & 7)) << 4);
  const int q4 = (lane & 15) >> 2, p4 = lane & 3;
  const uint32_t brd = sbase + G1_A_BYTES + (8 * fg + q4) * 448 + 8 * p4 + wn * 224;
  const int f32_ = (fg & 1) * 32;
  const uint32_t brdE = brd + ((wn & 1) ? -f32_ : f32_), brdO = brd + ((wn & 1) ? f32_ : -f32_);
  G1Frag X, Y;
  auto rd = [&](G1Frag& Fr, auto I, auto KS, uint32_t so) {
    constexpr int i = decltype(I)::v, ks = decltype(KS)::v;
    if constexpr (i < 4) {
      lds_rd128<i * 2048>(Fr.a[i], (ard0 ^ (ks * 64)) + so);
    } else {
      constexpr int ni = (i - 4) >> 1, t = (i - 4) & 1;
      lds_rdtr<ks * 32 * 448 + ni * 32 + t * 4 * 448>(Fr.b[ni][t], ((ni & 1) ? brdO : brdE) + so);
    }
  };
  auto mma1 = [&](G1Frag& Fr, auto J) {
    constexpr int j = decltype(J)::v, mi = j / 7, ni = j % 7;
    mfma16(acc[mi][ni], Fr.b[ni][0], Fr.b[ni][1], Fr.a[mi]);
  };
#define G1_TIE(Fr) asm volatile("s_waitcnt lgkmcnt(0)" : "+v"(Fr.a[0]), "+v"(Fr.a[1]), "+v"(Fr.a[2]), "+v"(Fr.a[3]), \
    "+v"(Fr.b[0][0]), "+v"(Fr.b[0][1]), "+v"(Fr.b[1][0]), "+v"(Fr.b[1][1]), "+v"(Fr.b[2][0]), "+v"(Fr.b[2][1]), "+v"(Fr.b[3][0]), "+v"(Fr.b[3][1]), \
    "+v"(Fr.b[4][0]), "+v"(Fr.b[4][1]), "+v"(Fr.b[5][0]), "+v"(Fr.b[5][1]), "+v"(Fr.b[6][0]), "+v"(Fr.b[6][1]) :: "memory")
  auto block = [&](G1Frag& C, G1Frag& R, auto KS, uint32_t so, auto DO_RD, auto DO_IS, int kt_is, int buf_is) {
    auto one = [&](auto J) {
      constexpr int j = decltype(J)::v;
      mma1(C, J);
      if constexpr (decltype(DO_RD)::v && j < 18) rd(R, J, KS, so);
      if constexpr (decltype(DO_IS)::v && j >= 6 && j < 28 && ((j - 6) & 1) == 0) issue1((j - 6) >> 1, kt_is, buf_is);
      SCHED0();
    };
    one(IC<0>{}); one(IC<1>{}); one(IC<2>{}); one(IC<3>{}); one(IC<4>{}); one(IC<5>{}); one(IC<6>{});
    one(IC<7>{}); one(IC<8>{}); one(IC<9>{}); one(IC<10>{}); one(IC<11>{}); one(IC<12>{}); one(IC<13>{});
    one(IC<14>{}); one(IC<15>{}); one(IC<16>{}); one(IC<17>{}); one(IC<18>{}); one(IC<19>{}); one(IC<20>{});
    one(IC<21>{}); one(IC<22>{}); one(IC<23>{}); one(IC<24>{}); one(IC<25>{}); one(IC<26>{}); one(IC<27>{});
  };
  auto rdall = [&](G1Frag& R, uint32_t so) {
    rd(R, IC<0>{}, IC<0>{}, so); rd(R, IC<1>{}, IC<0>{}, so); rd(R, IC<2>{}, IC<0>{}, so); rd(R, IC<3>{}, IC<0>{}, so);
    rd(R, IC<4>{}, IC<0>{}, so); rd(R, IC<5>{}, IC<0>{}, so); rd(R, IC<6>{}, IC<0>{}, so); rd(R, IC<7>{}, IC<0>{}, so);
    rd(R, IC<8>{}, IC<0>{}, so); rd(R, IC<9>{}, IC<0>{}, so); rd(R, IC<10>{}, IC<0>{}, so); rd(R, IC<11>{}, IC<0>{}, so);
    rd(R, IC<12>{}, IC<0>{}, so); rd(R, IC<13>{}, IC<0>{}, so); rd(R, IC<14>{}, IC<0>{}, so); rd(R, IC<15>{}, IC<0>{}, so);
    rd(R, IC<16>{}, IC<0>{}, so); rd(R, IC<17>{}, IC<0>{}, so);
  };
  __builtin_amdgcn_s_setprio(2);
  RAW_BAR();
  rdall(X, 0);
  int buf = 0;
  for (int kt = 0; kt < NK - 3; ++kt) {
    const int nbuf = buf == 2 ? 0 : buf + 1;
    G1_TIE(X); SCHED0();
    block(X, Y, IC<1>{}, buf * G1_STAGE, IC<1>{}, IC<0>{}, 0, 0);
    G1_TIE(Y); RAW_BAR(); SCHED0();
    block(Y, X, IC<0>{}, nbuf * G1_STAGE, IC<1>{}, IC<0>{}, 0, 0);
    buf = nbuf;
  }
  G1_TIE(X); SCHED0();
  block(X, Y, IC<1>{}, 1 * G1_STAGE, IC<1>{}, IC<0>{}, 0, 0);
  G1_TIE(Y); RAW_BAR(); SCHED0();
  block(Y, X, IC<0>{}, 2 * G1_STAGE, IC<1>{}, IC<0>{}, 0, 0);
  G1_TIE(X); SCHED0();
  block(X, Y, IC<1>{}, 2 * G1_STAGE, IC<1>{}, IC<0>{}, 0, 0);
  G1_TIE(Y); RAW_BAR(); SCHED0();
  block(Y, X, IC<0>{}, 0 * G1_STAGE, IC<1>{}, IC<0>{}, 0, 0);
  G1_TIE(X); SCHED0();
  block(X, Y, IC<1>{}, 0 * G1_STAGE, IC<1>{}, IC<0>{}, 0, 0);
  G1_TIE(Y); SCHED0();
  block(Y, X, IC<0>{}, 0, IC<0>{}, IC<0>{}, 0, 0);
  asm volatile("s_nop 15\n\ts_nop 15" : "+v"(acc[0][0]), "+v"(acc[0][1]), "+v"(acc[0][2]), "+v"(acc[0][3]), "+v"(acc[0][4]), "+v"(acc[0][5]), "+v"(acc[0][6]), "+v"(acc[1][0]), "+v"(acc[1][1]), "+v"(acc[1][2]), "+v"(acc[1][3]), "+v"(acc[1][4]), "+v"(acc[1][5]), "+v"(acc[1][6]), "+v"(acc[2][0]), "+v"(acc[2][1]), "+v"(acc[2][2]), "+v"(acc[2][3]), "+v"(acc[2][4]), "+v"(acc[2][5]), "+v"(acc[2][6]), "+v"(acc[3][0]), "+v"(acc[3][1]), "+v"(acc[3][2]), "+v"(acc[3][3]), "+v"(acc[3][4]), "+v"(acc[3][5]), "+v"(acc[3][6]) :: "memory");
  SCHED0();
  __builtin_amdgcn_s_setprio(0);
  RAW_BAR();
  {
    f32x4* xfer = reinterpret_cast<f32x4*>(smem) + w * (14 * 64) + lane;
#pragma unroll
    for (int mi = 2; mi < 4; ++mi)
#pragma unroll
      for (int ni = 0; ni < 7; ++ni) xfer[((mi - 2) * 7 + ni) * 64] = acc[mi][ni];
  }
  WAIT_L0();
  RAW_BAR();
  char* ot = smem + G1_OT + (wm * 64 + fr) * G1_OTS + (wn * 112 + 4 * fg) * 2;
#pragma unroll
  for (int ni = 0; ni < 7; ++ni) {
    const f32x2 b01 = unpk(bb[ni].x), b23 = unpk(bb[ni].y);
#pragma unroll
    for (int mi = 0; mi < 2; ++mi) {
      const f32x4 c = acc[mi][ni];
      *reinterpret_cast<uint2*>(ot + mi * 16 * G1_OTS + ni * 32) = make_uint2(mish_pair(c[0], c[1], b01), mish_pair(c[2], c[3], b23));
    }
  }
  g1_store();
}

constexpr int G2_A_BYTES = 128 * 128, G2_B_BYTES = 64 * 256, G2_STAGE = G2_A_BYTES + G2_B_BYTES;
struct G2Frag { u32x4 a[4]; u32x2 b[4][2]; };
__device__ __forceinline__ void g2_body(char* smem, const u16* __restrict__ A, const u16* __restrict__ W, const u16* __restrict__ h,
                                        u16* __restrict__ out, float* __restrict__ part, unsigned* __restrict__ flags) {
  const int tid = threadIdx.x, lane = tid & 63, w8 = __builtin_amdgcn_readfirstlane(tid >> 6), w = w8 & 3;
  const int wm = w >> 1, wn = w & 1;
  const int bid = blockIdx.x, xcd = bid & 7, idx = bid >> 3;
  const int kh = xcd & 1, tm = ((xcd >> 1) & 1) * 8 + (idx & 7), tn = (xcd >> 2) * 4 + (idx >> 3);
  const int m0 = tm * 128, n0 = tn * 128, tile = tm * 8 + tn;
  auto g2_store = [&]() {
    WAIT_L0();
    RAW_BAR();
#pragma unroll
    for (int i = 0; i < 2; ++i) {
      const int c = tid + 512 * i, r = c >> 4, q = c & 15;
      const int row = (r >> 5) * 64 + kh * 32 + (r & 31);
      const uint4 v = *reinterpret_cast<const uint4*>(smem + r * 272 + q * 16);
      *reinterpret_cast<uint4*>(reinterpret_cast<char*>(out) + (size_t)(m0 + row) * (D_ * 2) + n0 * 2 + q * 16) = v;
    }
  };
  constexpr int NK = 28;
  if (w8 >= 4) {
    uint32_t aoff[4], boff[4];
#pragma unroll
    for (int i = 0; i < 4; ++i) {
      const int r = w * 32 + i * 8 + (lane >> 3);
      aoff[i] = (uint32_t)(m0 + r) * (uint32_t)(F_ * 2) + (uint32_t)(((lane & 7) ^ (r & 7)) << 4);
    }
#pragma unroll
    for (int i = 0; i < 4; ++i) {
      const int k = w * 16 + i * 4 + (lane >> 4), q = lane & 15;
      const int c = (q >> 1) ^ ((k & 3) | (((k >> 3) & 1) << 2));
      boff[i] = (uint32_t)k * (uint32_t)(D_ * 2) + (uint32_t)(n0 * 2) + (uint32_t)(((c << 1) | (q & 1)) << 4);
    }
    const char* Ab = reinterpret_cast<const char*>(A) + kh * (1792 * 2);
    const char* Wb = reinterpret_cast<const char*>(W) + (size_t)kh * (1792 * D_ * 2);
    auto issueA = [&](int kt, int buf) {
#pragma unroll
      for (int i = 0; i < 4; ++i)
        __builtin_amdgcn_global_load_lds((const unsigned*)(Ab + kt * 128 + aoff[i]),
                                         (unsigned*)(smem + buf * G2_STAGE + w * 4096 + lane * 16 + i * 1024), 16, 0, 16);
    };
    auto issueB = [&](int kt, int buf) {
#pragma unroll
      for (int i = 0; i < 4; ++i)
        __builtin_amdgcn_global_load_lds((const unsigned*)(Wb + (size_t)kt * (64 * D_ * 2) + boff[i]),
                                         (unsigned*)(smem + buf * G2_STAGE + G2_A_BYTES + w * 4096 + lane * 16 + i * 1024), 16, 0, 0);
    };
    auto issue = [&](int kt, int buf) { issueA(kt, buf); issueB(kt, buf); };
    issueB(0, 0); issueB(1, 1);
    if (lane == 0) {
      for (int spin = 0; spin < (1 << 22); ++spin) {
        if (__hip_atomic_load((gu32*)(flags + 4096 + (tm * 2 + kh) * 16), __ATOMIC_RELAXED, __HIP_MEMORY_SCOPE_AGENT) == 8u) break;
        __builtin_amdgcn_s_sleep(1);
      }
    }
    asm volatile("" ::: "memory");
    issueA(0, 0); issueA(1, 1);
    WAIT_V(4); RAW_BAR();
    issue(2, 2); issue(3, 3);
    for (int kt = 0; kt < NK - 4; ++kt) {
      WAIT_V(16); RAW_BAR();
      issue(kt + 4, kt & 3);
    }
    WAIT_V(16); RAW_BAR();
    WAIT_V(8); RAW_BAR();
    WAIT_V(0); RAW_BAR();
    RAW_BAR();
    RAW_BAR();
    g2_store();
    return;
  }
  const int fr = lane & 15, fg = lane >> 4;
  u32x2 hv[2][4];
  f32x4 acc[4][4];
#pragma unroll
  for (int i = 0; i < 4; ++i)
#pragma unroll
    for (int j = 0; j < 4; ++j) acc[i][j] = (f32x4){0.f, 0.f, 0.f, 0.f};
  const uint32_t sbase = (uint32_t)(size_t)(smem);
  const uint32_t ard0 = sbase + (wm * 64 + fr) * 128 + ((fg ^ (fr & 7)) << 4);
  const int q4 = (lane & 15) >> 2, p4 = lane & 3;
  const int bsw = q4 | ((fg & 1) << 2);
  const uint32_t brd = sbase + G2_A_BYTES + (8 * fg + q4) * 256 + 8 * p4;
  uint32_t brdn[4];
#pragma unroll
  for (int ni = 0; ni < 4; ++ni) brdn[ni] = brd + (((wn * 4 + ni) ^ bsw) * 32);
  G2Frag X, Y;
  auto rd = [&](G2Frag& Fr, auto I, auto KS, uint32_t so) {
    constexpr int i = decltype(I)::v, ks = decltype(KS)::v;
    if constexpr (i < 4) {
      lds_rd128<i * 2048>(Fr.a[i], (ard0 ^ (ks * 64)) + so);
    } else {
      constexpr int ni = (i - 4) >> 1, t = (i - 4) & 1;
      lds_rdtr<ks * 32 * 256 + t * 4 * 256>(Fr.b[ni][t], brdn[ni] + so);
    }
  };
  auto mma1 = [&](G2Frag& Fr, auto J) {
    constexpr int j = decltype(J)::v, mi = j / 4, ni = j % 4;
    mfma16(acc[mi][ni], Fr.b[ni][0], Fr.b[ni][1], Fr.a[mi]);
  };
#define G2_TIE(Fr) asm volatile("s_waitcnt lgkmcnt(0)" : "+v"(Fr.a[0]), "+v"(Fr.a[1]), "+v"(Fr.a[2]), "+v"(Fr.a[3]), \
    "+v"(Fr.b[0][0]), "+v"(Fr.b[0][1]), "+v"(Fr.b[1][0]), "+v"(Fr.b[1][1]), "+v"(Fr.b[2][0]), "+v"(Fr.b[2][1]), "+v"(Fr.b[3][0]), "+v"(Fr.b[3][1]) :: "memory")
  auto block = [&](G2Frag& C, G2Frag& R, auto KS, uint32_t so, auto DO_RD) {
    auto one = [&](auto J) {
      constexpr int j = decltype(J)::v;
      mma1(C, J);
      if constexpr (decltype(DO_RD)::v && j < 12) rd(R, J, KS, so);
      SCHED0();
    };
    one(IC<0>{}); one(IC<1>{}); one(IC<2>{}); one(IC<3>{}); one(IC<4>{}); one(IC<5>{}); one(IC<6>{}); one(IC<7>{});
    one(IC<8>{}); one(IC<9>{}); one(IC<10>{}); one(IC<11>{}); one(IC<12>{}); one(IC<13>{}); one(IC<14>{}); one(IC<15>{});
  };
  __builtin_amdgcn_s_setprio(2);
  RAW_BAR();
  {
    const auto hrs = __builtin_amdgcn_make_buffer_rsrc(const_cast<u16*>(h), 0, S_ * D_ * 2, 0x00020000);
#pragma unroll
    for (int i = 0; i < 2; ++i)
#pragma unroll
      for (int ni = 0; ni < 4; ++ni)
        hv[i][ni] = __builtin_amdgcn_raw_buffer_load_b64(hrs, (uint32_t)((m0 + wm * 64 + (2 * kh + i) * 16 + fr) * D_ + n0 + wn * 64 + ni * 16 + 4 * fg) * 2u, 0, 16);
  }
  rd(X, IC<0>{}, IC<0>{}, 0); rd(X, IC<1>{}, IC<0>{}, 0); rd(X, IC<2>{}, IC<0>{}, 0); rd(X, IC<3>{}, IC<0>{}, 0);
  rd(X, IC<4>{}, IC<0>{}, 0); rd(X, IC<5>{}, IC<0>{}, 0); rd(X, IC<6>{}, IC<0>{}, 0); rd(X, IC<7>{}, IC<0>{}, 0);
  rd(X, IC<8>{}, IC<0>{}, 0); rd(X, IC<9>{}, IC<0>{}, 0); rd(X, IC<10>{}, IC<0>{}, 0); rd(X, IC<11>{}, IC<0>{}, 0);
  for (int kt = 0; kt < NK - 1; ++kt) {
    G2_TIE(X); SCHED0();
    block(X, Y, IC<1>{}, (kt & 3) * G2_STAGE, IC<1>{});
    G2_TIE(Y); RAW_BAR(); SCHED0();
    block(Y, X, IC<0>{}, ((kt + 1) & 3) * G2_STAGE, IC<1>{});
  }
  G2_TIE(X); SCHED0();
  block(X, Y, IC<1>{}, ((NK - 1) & 3) * G2_STAGE, IC<1>{});
  G2_TIE(Y); SCHED0();
  block(Y, X, IC<0>{}, 0, IC<0>{});
  asm volatile("s_nop 15\n\ts_nop 15" : "+v"(acc[0][0]), "+v"(acc[0][1]), "+v"(acc[0][2]), "+v"(acc[0][3]), "+v"(acc[1][0]), "+v"(acc[1][1]), "+v"(acc[1][2]), "+v"(acc[1][3]), "+v"(acc[2][0]), "+v"(acc[2][1]), "+v"(acc[2][2]), "+v"(acc[2][3]), "+v"(acc[3][0]), "+v"(acc[3][1]), "+v"(acc[3][2]), "+v"(acc[3][3]) :: "memory");
  SCHED0();
  const auto rs = __builtin_amdgcn_make_buffer_rsrc(part, 0, 256 * 32768, 0x00020000);
  const uint32_t myoff = (uint32_t)(tile * 2 + kh) * 32768u + (uint32_t)w * 8192u + (uint32_t)lane * 16u;
  const uint32_t proff = (uint32_t)(tile * 2 + (kh ^ 1)) * 32768u + (uint32_t)w * 8192u + (uint32_t)lane * 16u;
  auto send = [&](const f32x4 (&r0)[4], const f32x4 (&r1)[4]) {
#pragma unroll
    for (int ni = 0; ni < 4; ++ni) {
      __builtin_amdgcn_raw_buffer_store_b128(__builtin_bit_cast(u32x4, r0[ni]), rs, myoff + ni * 1024, 0, 16);
      __builtin_amdgcn_raw_buffer_store_b128(__builtin_bit_cast(u32x4, r1[ni]), rs, myoff + 4096 + ni * 1024, 0, 16);
    }
  };
  if (kh == 0) send(acc[2], acc[3]); else send(acc[0], acc[1]);
  WAIT_V(0);
  RAW_BAR();
  if (w == 0) {
    if (lane == 0) {
      __hip_atomic_store((gu32*)(flags + (tile * 2 + kh) * 16), 1u, __ATOMIC_RELAXED, __HIP_MEMORY_SCOPE_AGENT);
      for (int spin = 0; spin < (1 << 22); ++spin) {
        if (__hip_atomic_load((gu32*)(flags + (tile * 2 + (kh ^ 1)) * 16), __ATOMIC_RELAXED, __HIP_MEMORY_SCOPE_AGENT) == 1u) break;
        __builtin_amdgcn_s_sleep(1);
      }
    }
  }
  asm volatile("" ::: "memory");
  RAW_BAR();
  asm volatile("" ::: "memory");
  auto fin = [&](auto I, const f32x4 (&own)[4]) {
    constexpr int i = decltype(I)::v;
    char* ot = smem + (wm * 32 + i * 16 + fr) * 272 + wn * 128 + 8 * fg;
    f32x4 pr[4];
#pragma unroll
    for (int ni = 0; ni < 4; ++ni) pr[ni] = __builtin_bit_cast(f32x4, __builtin_amdgcn_raw_buffer_load_b128(rs, proff + i * 4096 + ni * 1024, 0, 16));
#pragma unroll
    for (int ni = 0; ni < 4; ++ni) {
      const f32x4 c = kh == 0 ? own[ni] + pr[ni] : pr[ni] + own[ni];
      const f32x2 r01 = unpk(hv[i][ni][0]) + unpk(cvt_pk(c[0], c[1])), r23 = unpk(hv[i][ni][1]) + unpk(cvt_pk(c[2], c[3]));
      *reinterpret_cast<uint2*>(ot + ni * 32) = make_uint2(cvt_pk(r01[0], r01[1]), cvt_pk(r23[0], r23[1]));
    }
  };
  if (kh == 0) { fin(IC<0>{}, acc[0]); fin(IC<1>{}, acc[1]); }
  else { fin(IC<0>{}, acc[2]); fin(IC<1>{}, acc[3]); }
  g2_store();
}

__global__ __launch_bounds__(512, 2) void k3g_fused(const c2* __restrict__ U, const u16* __restrict__ x, u16* __restrict__ A,
                                                     const u16* __restrict__ W1, const u16* __restrict__ b1,
                                                     u16* __restrict__ act, const u16* __restrict__ W2, u16* __restrict__ h,
                                                     u16* __restrict__ out, float* __restrict__ part, unsigned* __restrict__ flags) {
  __shared__ __attribute__((aligned(16))) char smem[3 * G1_STAGE];
  uint32_t cpf = 0;
  if ((blockIdx.x >> 3) < 2 && threadIdx.x >= 288) {
    const char* pc = reinterpret_cast<const char*>(__builtin_amdgcn_s_getpc());
    cpf = *reinterpret_cast<const uint32_t*>(pc + 4096 + (threadIdx.x - 288) * 128);
  }
  k3_body(smem, U, x, h, A, reinterpret_cast<const char*>(W2));
  KEEP(cpf);
  WAIT_V(0);
  WAIT_L0();
  RAW_BAR();
  if (threadIdx.x == 0)
    __hip_atomic_fetch_add((gu32*)(flags + 8192 + (blockIdx.x >> 4) * 16), 1u, __ATOMIC_RELAXED, __HIP_MEMORY_SCOPE_AGENT);
  g1_body(smem, A, W1, b1, act, flags);
  WAIT_V(0);
  RAW_BAR();
  if (threadIdx.x == 0) {
    const int xcd = blockIdx.x & 7, idx = blockIdx.x >> 3;
    const int tm = (xcd & 1) * 8 + (idx & 7), tn = (xcd >> 1) * 4 + (idx >> 3);
    __hip_atomic_fetch_add((gu32*)(flags + 4096 + (tm * 2 + (tn >> 3)) * 16), 1u, __ATOMIC_RELAXED, __HIP_MEMORY_SCOPE_AGENT);
  }
  g2_body(smem, act, W2, h, out, part, flags);
}

extern "C" void kernel_launch(void* const* d_in, const int* in_sizes, int n_in,
                              void* d_out, int out_size, void* d_ws, size_t ws_size,
                              hipStream_t stream) {
  const u16* x = (const u16*)d_in[0];
  const u16* wr = (const u16*)d_in[1];
  const u16* wi = (const u16*)d_in[2];
  const u16* w1 = (const u16*)d_in[4];
  const u16* b1 = (const u16*)d_in[5];
  const u16* w2 = (const u16*)d_in[6];
  u16* out = (u16*)d_out;
  char* ws = (char*)d_ws;
  c2* T = (c2*)ws;
  uint32_t* WT = (uint32_t*)(ws + (9u << 20));
  u16* h = (u16*)(ws + (16u << 20));
  u16* a = (u16*)(ws + (20u << 20));
  u16* act = (u16*)(ws + (24u << 20));
  float* part = (float*)(ws + (40u << 20));
  unsigned* flags = (unsigned*)(ws + (48u << 20));
  k1_ln_rowfft<<<256, 512, 0, stream>>>(x, wr, wi, T, WT, flags);
  c2* U = (c2*)(ws + (50u << 20));
  k2_colfft<<<512, 256, 0, stream>>>(T, U, WT, (const char*)w1);
  k3g_fused<<<256, 512, 0, stream>>>(U, x, a, w1, b1, act, w2, h, out, part, flags);
}
```

```cpp
#include <hip/hip_runtime.h>
#include <hip/hip_bf16.h>
#include <stdint.h>

typedef unsigned short u16;
typedef __attribute__((ext_vector_type(8))) short bf16x8;
typedef __attribute__((ext_vector_type(4))) short bf16x4;
typedef __attribute__((ext_vector_type(4))) float f32x4;
typedef __attribute__((address_space(3))) bf16x4* lds_b4_ptr;

constexpr int TBS = 4096 + 16;
constexpr int S_ = 2048, D_ = 1024, F_ = 3584, DF_ = 513;

__device__ __forceinline__ float bf2f(u16 v) { return __uint_as_float(((uint32_t)v) << 16); }
__device__ __forceinline__ u16 f2bf(float f) { __bf16 b = (__bf16)f; return __builtin_bit_cast(u16, b); }
__device__ __forceinline__ float rbf(float f) { return bf2f(f2bf(f)); }

typedef __attribute__((ext_vector_type(4))) unsigned int u32v4;
typedef __attribute__((ext_vector_type(4))) float f32v4;
template <typename T> __device__ __forceinline__ void st_nt(T* p, T v) { __builtin_nontemporal_store(v, p); }
typedef __attribute__((ext_vector_type(2))) float c2;
typedef c2 f32x2;
__device__ __forceinline__ uint32_t cvt_pk(float lo, float hi) { uint32_t r; asm("v_cvt_pk_bf16_f32 %0, %1, %2" : "=v"(r) : "v"(lo), "v"(hi)); return r; }
__device__ __forceinline__ c2 unpk(uint32_t u) { return (c2){__uint_as_float(u << 16), __uint_as_float(u & 0xffff0000u)}; }
__device__ __forceinline__ c2 rbf2(c2 v) { return unpk(cvt_pk(v.x, v.y)); }
__device__ __forceinline__ c2 cmul(c2 a, c2 b) {
  c2 t, r;
  asm("v_pk_mul_f32 %0, %1, %2 op_sel:[0,0] op_sel_hi:[0,1]" : "=v"(t) : "v"(a), "v"(b));
  asm("v_pk_fma_f32 %0, %1, %2, %3 op_sel:[1,1,0] op_sel_hi:[1,0,1] neg_lo:[0,1,0]" : "=v"(r) : "v"(a), "v"(b), "v"(t));
  return r;
}
__device__ __forceinline__ c2 sub_i(c2 t, c2 d) { c2 r; asm("v_pk_add_f32 %0, %1, %2 op_sel:[0,1] op_sel_hi:[1,0] neg_hi:[0,1]" : "=v"(r) : "v"(t), "v"(d)); return r; }
__device__ __forceinline__ c2 add_i(c2 t, c2 d) { c2 r; asm("v_pk_add_f32 %0, %1, %2 op_sel:[0,1] op_sel_hi:[1,0] neg_lo:[0,1]" : "=v"(r) : "v"(t), "v"(d)); return r; }
__device__ __forceinline__ c2 add_c(c2 a, c2 b) { c2 r; asm("v_pk_add_f32 %0, %1, %2 neg_hi:[0,1]" : "=v"(r) : "v"(a), "v"(b)); return r; }
__device__ __forceinline__ c2 sub_c(c2 a, c2 b) { c2 r; asm("v_pk_add_f32 %0, %1, %2 neg_lo:[0,1]" : "=v"(r) : "v"(a), "v"(b)); return r; }
template <int DIR> __device__ __forceinline__ c2 addD(c2 t, c2 d) { return DIR > 0 ? add_i(t, d) : sub_i(t, d); }
template <int DIR> __device__ __forceinline__ c2 subD(c2 t, c2 d) { return DIR > 0 ? sub_i(t, d) : add_i(t, d); }
template <int DIR>
__device__ __forceinline__ c2 cexp2pi(float rev) {
  const float r = DIR > 0 ? rev : -rev;
  c2 w = {__builtin_amdgcn_cosf(r), __builtin_amdgcn_sinf(r)};
  asm("s_nop 0" : "+v"(w));
  return w;
}
template <int DIR>
__device__ __forceinline__ void dft4(c2& x0, c2& x1, c2& x2, c2& x3) {
  const c2 t0 = x0 + x2, t1 = x0 - x2, t2 = x1 + x3, d = x1 - x3;
  x0 = t0 + t2; x2 = t0 - t2; x1 = addD<DIR>(t1, d); x3 = subD<DIR>(t1, d);
}
template <int DIR>
__device__ __forceinline__ void dft8(c2 (&v)[8]) {
  c2 e0 = v[0], e1 = v[2], e2 = v[4], e3 = v[6];
  c2 o0 = v[1], o1 = v[3], o2 = v[5], o3 = v[7];
  dft4<DIR>(e0, e1, e2, e3);
  dft4<DIR>(o0, o1, o2, o3);
  const c2 r = {0.70710678118654752440f, 0.70710678118654752440f};
  const c2 u1 = addD<DIR>(o1, o1), u3 = subD<DIR>(o3, o3);
  v[0] = e0 + o0; v[4] = e0 - o0;
  v[1] = __builtin_elementwise_fma(u1, r, e1); v[5] = __builtin_elementwise_fma(u1, -r, e1);
  v[2] = addD<DIR>(e2, o2); v[6] = subD<DIR>(e2, o2);
  v[3] = __builtin_elementwise_fma(u3, -r, e3); v[7] = __builtin_elementwise_fma(u3, r, e3);
}
__device__ __forceinline__ void twiddle8(c2 (&v)[8], c2 w) {
  const c2 w2 = cmul(w, w), w3 = cmul(w2, w), w4 = cmul(w2, w2), w5 = cmul(w4, w), w6 = cmul(w3, w3), w7 = cmul(w4, w3);
  v[1] = cmul(v[1], w); v[2] = cmul(v[2], w2); v[3] = cmul(v[3], w3); v[4] = cmul(v[4], w4);
  v[5] = cmul(v[5], w5); v[6] = cmul(v[6], w6); v[7] = cmul(v[7], w7);
}
__device__ __forceinline__ void twiddle8b(c2 (&v)[8], c2 b, c2 w) {
  const c2 w2 = cmul(w, w), w4 = cmul(w2, w2);
  const c2 b1 = cmul(b, w), b2 = cmul(b, w2), b3 = cmul(b1, w2), b4 = cmul(b, w4), b5 = cmul(b1, w4), b6 = cmul(b2, w4), b7 = cmul(b3, w4);
  v[0] = cmul(v[0], b); v[1] = cmul(v[1], b1); v[2] = cmul(v[2], b2); v[3] = cmul(v[3], b3);
  v[4] = cmul(v[4], b4); v[5] = cmul(v[5], b5); v[6] = cmul(v[6], b6); v[7] = cmul(v[7], b7);
}

__device__ __forceinline__ float wave_sum(float v) {
#define DPP_ADD(ctrl, rmask) v += __uint_as_float(__builtin_amdgcn_update_dpp(0u, __float_as_uint(v), ctrl, rmask, 0xf, true))
  DPP_ADD(0x111, 0xf); DPP_ADD(0x112, 0xf); DPP_ADD(0x114, 0xf); DPP_ADD(0x118, 0xf);
  DPP_ADD(0x142, 0xa); DPP_ADD(0x143, 0xc);
#undef DPP_ADD
  return __uint_as_float(__builtin_amdgcn_readlane(__float_as_uint(v), 63));
}

#define WAVE_SYNC() do { __builtin_amdgcn_fence(__ATOMIC_RELEASE, "wavefront"); __builtin_amdgcn_wave_barrier(); __builtin_amdgcn_fence(__ATOMIC_ACQUIRE, "wavefront"); } while (0)

template <int DIR>
__device__ __forceinline__ void fft512_wave(c2 (&v)[8], c2* ex, int lane) {
  const int hi = lane >> 3, lo = lane & 7;
  dft8<DIR>(v);
  twiddle8(v, cexp2pi<DIR>((float)hi * (1.0f / 64.0f)));
#pragma unroll
  for (int k0 = 0; k0 < 8; ++k0) ex[72 * k0 + lane] = v[k0];
  WAVE_SYNC();
#pragma unroll
  for (int b = 0; b < 8; ++b) v[b] = ex[72 * hi + 8 * b + lo];
  WAVE_SYNC();
  dft8<DIR>(v);
  twiddle8b(v, cexp2pi<DIR>((float)(lo * hi) * (1.0f / 512.0f)), cexp2pi<DIR>((float)lo * (1.0f / 64.0f)));
#pragma unroll
  for (int k1 = 0; k1 < 8; ++k1) ex[66 * lo + 8 * k1 + hi] = v[k1];
  WAVE_SYNC();
#pragma unroll
  for (int c = 0; c < 8; ++c) v[c] = ex[66 * c + lane];
  WAVE_SYNC();
  dft8<DIR>(v);
}

__device__ __forceinline__ void ln_bf16(c2 (&e)[8], uint32_t (&o)[8]) {
  c2 s2 = ((e[0] + e[1]) + (e[2] + e[3])) + ((e[4] + e[5]) + (e[6] + e[7]));
  const float mean = wave_sum(s2.x + s2.y) * (1.0f / 1024.0f);
  c2 q2 = {0.f, 0.f};
#pragma unroll
  for (int i = 0; i < 8; ++i) { const c2 d = e[i] - mean; q2 = __builtin_elementwise_fma(d, d, q2); }
  const float var = wave_sum(q2.x + q2.y) * (1.0f / 1024.0f);
  const float mb = rbf(mean);
  const float sd = rbf(sqrtf(rbf(rbf(var) + 1e-5f)));
  const float ri = 1.0f / sd;
#pragma unroll
  for (int i = 0; i < 8; ++i) {
    const c2 d = rbf2(e[i] - mb);
    const c2 q = d * ri;
    const c2 rem = __builtin_elementwise_fma(-q, (c2){sd, sd}, d);
    const c2 qq = __builtin_elementwise_fma(rem, (c2){ri, ri}, q);
    o[i] = cvt_pk(qq.x, qq.y);
  }
}

__global__ __launch_bounds__(512) void k1_ln_rowfft(const u16* __restrict__ x, const u16* __restrict__ wr, const u16* __restrict__ wi,
                                                     c2* __restrict__ T, uint32_t* __restrict__ WT, unsigned* __restrict__ flags) {
  __shared__ c2 ex_all[8 * 576];
  __shared__ c2 st[512 * 9];
  const int tid = threadIdx.x, lane = tid & 63, w = tid >> 6;
  const int s0 = blockIdx.x * 8;
  const int row = s0 + w;
  const uint32_t* xr = reinterpret_cast<const uint32_t*>(x + (size_t)row * D_);
  uint32_t xu[8];
#pragma unroll
  for (int a = 0; a < 8; ++a) xu[a] = xr[lane + 64 * a];
  if (tid == 0) {
    flags[blockIdx.x * 16] = 0;
    if (blockIdx.x < 32) flags[4096 + blockIdx.x * 16] = 0;
    if (blockIdx.x < 16) flags[8192 + blockIdx.x * 16] = 0;
  }
  {
    uint32_t p[8];
#pragma unroll
    for (int r = 0; r < 8; ++r) {
      const size_t o = (size_t)(s0 + r) * DF_ + tid;
      p[r] = (uint32_t)wr[o] | ((uint32_t)wi[o] << 16);
    }
    u32v4* dst = reinterpret_cast<u32v4*>(WT + (size_t)tid * S_ + s0);
    st_nt(dst, (u32v4){p[0], p[1], p[2], p[3]});
    st_nt(dst + 1, (u32v4){p[4], p[5], p[6], p[7]});
    if (tid < 8) {
      const size_t o = (size_t)(s0 + tid) * DF_ + 512;
      WT[(size_t)512 * S_ + s0 + tid] = (uint32_t)wr[o] | ((uint32_t)wi[o] << 16);
    }
  }
  c2 v[8];
#pragma unroll
  for (int a = 0; a < 8; ++a) v[a] = unpk(xu[a]);
  ln_bf16(v, xu);
#pragma unroll
  for (int a = 0; a < 8; ++a) v[a] = unpk(xu[a]);
  c2* ex = ex_all + w * 576;
  fft512_wave<-1>(v, ex, lane);
#pragma unroll
  for (int j = 4; j < 8; ++j) ex[lane + 64 * j] = v[j];
  ex[512 + lane] = v[0];
  WAVE_SYNC();
  const c2 tw0 = cexp2pi<-1>((float)lane * (1.0f / 1024.0f));
  const c2 half = {0.5f, 0.5f};
#pragma unroll
  for (int j = 0; j < 4; ++j) {
    const int k = lane + 64 * j;
    const c2 z = v[j], zp = ex[512 - k];
    const c2 cj = j == 0 ? (c2){1.f, 0.f} : j == 1 ? (c2){0.92387953251128674f, -0.38268343236508977f}
                : j == 2 ? (c2){0.70710678118654752f, -0.70710678118654752f} : (c2){0.38268343236508977f, -0.92387953251128674f};
    const c2 tw = j == 0 ? tw0 : cmul(tw0, cj);
    const c2 E = add_c(z, zp) * half, Dz = sub_c(z, zp) * half;
    const c2 P = cmul(tw, Dz);
    c2 Xa = sub_i(E, P), Xb = add_i(E, P);
    Xb.y = -Xb.y;
    if (k == 0) { Xa = (c2){z.x + z.y, z.x - z.y}; Xb = (c2){v[4].x, -v[4].y}; }
    st[k * 9 + w] = Xa;
    st[(k == 0 ? 256 : 512 - k) * 9 + w] = Xb;
  }
  __syncthreads();
#pragma unroll
  for (int it = 0; it < 4; ++it) {
    const int idx = it * 512 + tid;
    const int k = idx >> 2, part = idx & 3;
    const c2 a = st[k * 9 + 2 * part], b = st[k * 9 + 2 * part + 1];
    st_nt(reinterpret_cast<f32v4*>(T + (size_t)k * S_ + s0) + part, (f32v4){a.x, a.y, b.x, b.y});
  }
}

#define KEEP(v) asm volatile("" :: "v"(v))
__device__ __forceinline__ uint32_t wt_at(const uint32_t* WT, int k1, int c) { return WT[(size_t)c * S_ + k1]; }
__global__ __launch_bounds__(256) void k2_colfft(const c2* __restrict__ T, c2* __restrict__ U, const uint32_t* __restrict__ WT, const char* __restrict__ pf) {
  __shared__ c2 ex[2304];
  const int t = threadIdx.x, col = blockIdx.x;
  c2 v[8];
#pragma unroll
  for (int a = 0; a < 8; ++a) v[a] = T[(size_t)col * S_ + t + 256 * a];
  uint32_t wv[8];
#pragma unroll
  for (int r = 0; r < 8; ++r) wv[r] = wt_at(WT, t + 256 * r, col);
  uint32_t pfv = 0;
  if (t < 112) pfv = *reinterpret_cast<const uint32_t*>(pf + ((size_t)col * 112 + t) * 128);
  c2* Tg = U + (size_t)(t >> 3) * TBS + col * 8 + (t & 7);
  const int h5 = t >> 5, l5 = t & 31;
  const int m3 = (t >> 2) & 7, d2 = t & 3;
  dft8<-1>(v);
  twiddle8(v, cexp2pi<-1>((float)t * (1.0f / 2048.0f)));
#pragma unroll
  for (int k0 = 0; k0 < 8; ++k0) ex[256 * k0 + t] = v[k0];
  __syncthreads();
#pragma unroll
  for (int b = 0; b < 8; ++b) v[b] = ex[256 * h5 + 32 * b + l5];
  __syncthreads();
  dft8<-1>(v);
  twiddle8(v, cexp2pi<-1>((float)l5 * (1.0f / 256.0f)));
#pragma unroll
  for (int k1 = 0; k1 < 8; ++k1) ex[288 * h5 + 36 * k1 + l5] = v[k1];
  WAVE_SYNC();
#pragma unroll
  for (int c = 0; c < 8; ++c) v[c] = ex[288 * h5 + 36 * m3 + 4 * c + d2];
  __syncthreads();
  dft8<-1>(v);
  twiddle8(v, cexp2pi<-1>((float)d2 * (1.0f / 32.0f)));
#pragma unroll
  for (int k2 = 0; k2 < 8; ++k2) ex[(4 * k2 + d2) * 66 + 8 * m3 + h5] = v[k2];
  __syncthreads();
  const int dd = t >> 6, l6 = t & 63;
#pragma unroll
  for (int d = 0; d < 4; ++d) { v[d] = ex[(4 * dd + d) * 66 + l6]; v[4 + d] = ex[(4 * (dd + 4) + d) * 66 + l6]; }
  __syncthreads();
  dft4<-1>(v[0], v[1], v[2], v[3]);
  dft4<-1>(v[4], v[5], v[6], v[7]);
  if (col == 0) {
#pragma unroll
    for (int k3 = 0; k3 < 4; ++k3) { ex[t + 512 * k3] = v[k3]; ex[t + 256 + 512 * k3] = v[4 + k3]; }
    __syncthreads();
  }
  if (col != 0) {
#pragma unroll
    for (int k3 = 0; k3 < 4; ++k3) {
      v[k3] = cmul(v[k3], unpk(wv[2 * k3]));
      v[4 + k3] = cmul(v[4 + k3], unpk(wv[2 * k3 + 1]));
    }
  } else {
#pragma unroll
    for (int r = 0; r < 8; ++r) {
      const int vi = (r & 1) * 4 + (r >> 1);
      const int k = t + 256 * r, kn = (2048 - k) & 2047;
      const c2 p = v[vi], pn = ex[kn];
      const c2 F0 = {0.5f * (p.x + pn.x), 0.5f * (p.y - pn.y)};
      const c2 F5 = {0.5f * (p.y + pn.y), 0.5f * (pn.x - p.x)};
      const c2 wa = unpk(wv[r]), wan = unpk(wt_at(WT, kn, 0));
      const c2 wb = unpk(wt_at(WT, k, 512)), wbn = unpk(wt_at(WT, kn, 512));
      const c2 W0 = {0.5f * (wa.x + wan.x), 0.5f * (wa.y - wan.y)};
      const c2 W5 = {0.5f * (wb.x + wbn.x), 0.5f * (wb.y - wbn.y)};
      const c2 H0 = cmul(F0, W0), H5 = cmul(F5, W5);
      v[vi] = add_i(H0, H5);
    }
  }
  if (col == 0) __syncthreads();
  dft4<1>(v[0], v[1], v[2], v[3]);
  dft4<1>(v[4], v[5], v[6], v[7]);
  {
    const c2 wa = cexp2pi<1>((float)dd * (1.0f / 32.0f)), wb = cexp2pi<1>((float)(dd + 4) * (1.0f / 32.0f));
    const c2 wa2 = cmul(wa, wa), wa3 = cmul(wa2, wa), wb2 = cmul(wb, wb), wb3 = cmul(wb2, wb);
    v[1] = cmul(v[1], wa); v[2] = cmul(v[2], wa2); v[3] = cmul(v[3], wa3);
    v[5] = cmul(v[5], wb); v[6] = cmul(v[6], wb2); v[7] = cmul(v[7], wb3);
  }
#pragma unroll
  for (int d = 0; d < 4; ++d) { ex[(4 * dd + d) * 66 + l6] = v[d]; ex[(4 * (dd + 4) + d) * 66 + l6] = v[4 + d]; }
  __syncthreads();
#pragma unroll
  for (int k2 = 0; k2 < 8; ++k2) v[k2] = ex[(4 * k2 + d2) * 66 + 8 * m3 + h5];
  __syncthreads();
  dft8<1>(v);
  twiddle8b(v, cexp2pi<1>((float)(m3 * d2) * (1.0f / 256.0f)), cexp2pi<1>((float)m3 * (1.0f / 64.0f)));
#pragma unroll
  for (int c = 0; c < 8; ++c) ex[288 * h5 + 36 * m3 + 4 * c + d2] = v[c];
  WAVE_SYNC();
#pragma unroll
  for (int k1 = 0; k1 < 8; ++k1) v[k1] = ex[288 * h5 + 36 * k1 + l5];
  __syncthreads();
  dft8<1>(v);
  twiddle8b(v, cexp2pi<1>((float)(h5 * l5) * (1.0f / 2048.0f)), cexp2pi<1>((float)h5 * (1.0f / 64.0f)));
#pragma unroll
  for (int b = 0; b < 8; ++b) ex[256 * h5 + 32 * b + l5] = v[b];
  __syncthreads();
#pragma unroll
  for (int k0 = 0; k0 < 8; ++k0) v[k0] = ex[256 * k0 + t];
  dft8<1>(v);
#pragma unroll
  for (int a = 0; a < 8; ++a) st_nt(Tg + (size_t)a * (32 * TBS), v[a]);
  KEEP(pfv);
}

__device__ __forceinline__ void k3_body(char* smem, const c2* __restrict__ T, const u16* __restrict__ x,
                                        u16* __restrict__ h, u16* __restrict__ a_out, const char* __restrict__ pf) {
  c2* ex_all = reinterpret_cast<c2*>(smem);
  c2* st = reinterpret_cast<c2*>(smem) + 8 * 576;
  const int tid = threadIdx.x, lane = tid & 63, w = tid >> 6;
  const int s0 = blockIdx.x * 8;
  const int row = s0 + w;
  float4 q4[4];
#pragma unroll
  for (int it = 0; it < 4; ++it) {
    const int idx = it * 512 + tid;
    q4[it] = reinterpret_cast<const float4*>(T + (size_t)blockIdx.x * TBS)[idx];
  }
  const uint32_t* xr = reinterpret_cast<const uint32_t*>(x + (size_t)row * D_);
  uint32_t xv[8];
#pragma unroll
  for (int a = 0; a < 8; ++a) xv[a] = xr[lane + 64 * a];
  uint32_t pfv = 0;
  if (tid < 224) pfv = *reinterpret_cast<const uint32_t*>(pf + ((size_t)blockIdx.x * 224 + tid) * 128);
#pragma unroll
  for (int it = 0; it < 4; ++it) {
    const int idx = it * 512 + tid;
    const int k = idx >> 2, part = idx & 3;
    st[k * 9 + 2 * part] = (c2){q4[it].x, q4[it].y};
    st[k * 9 + 2 * part + 1] = (c2){q4[it].z, q4[it].w};
  }
  __syncthreads();
  c2 v[8];
  const c2 tw0 = cexp2pi<1>((float)lane * (1.0f / 1024.0f));
#pragma unroll
  for (int j = 0; j < 8; ++j) {
    const int k = lane + 64 * j;
    const c2 g = st[k * 9 + w], gp = st[((512 - k) & 511) * 9 + w];
    const float cs[8] = {1.f, 0.92387953251128674f, 0.70710678118654752f, 0.38268343236508977f, 0.f, -0.38268343236508977f, -0.70710678118654752f, -0.92387953251128674f};
    const float sn[8] = {0.f, 0.38268343236508977f, 0.70710678118654752f, 0.92387953251128674f, 1.f, 0.92387953251128674f, 0.70710678118654752f, 0.38268343236508977f};
    const c2 tw = j == 0 ? tw0 : cmul(tw0, (c2){cs[j], sn[j]});
    c2 Z = add_i(add_c(g, gp), cmul(tw, sub_c(g, gp)));
    if (k == 0) Z = (c2){g.x + g.y, g.x - g.y};
    v[j] = Z;
  }
  c2* ex = ex_all + w * 576;
  fft512_wave<1>(v, ex, lane);
  const float scale = 1.0f / (2048.0f * 1024.0f);
  uint32_t* tr = reinterpret_cast<uint32_t*>(ex);
  const auto hrs = __builtin_amdgcn_make_buffer_rsrc(h, 0, S_ * D_ * 2, 0x00020000);
  const auto ars = __builtin_amdgcn_make_buffer_rsrc(a_out, 0, S_ * D_ * 2, 0x00020000);
  const uint32_t roff = (uint32_t)row * (D_ * 2) + lane * 16;
#pragma unroll
  for (int j = 0; j < 8; ++j) {
    const c2 hh = unpk(xv[j]) + rbf2(v[j] * scale);
    const uint32_t hu = cvt_pk(hh.x, hh.y);
    tr[lane + 64 * j] = hu;
    v[j] = unpk(hu);
  }
  WAVE_SYNC();
  {
    const u32v4 h0 = reinterpret_cast<const u32v4*>(tr)[lane], h1 = reinterpret_cast<const u32v4*>(tr)[64 + lane];
    __builtin_amdgcn_raw_buffer_store_b128(h0, hrs, roff, 0, 16);
    __builtin_amdgcn_raw_buffer_store_b128(h1, hrs, roff + 1024, 0, 16);
  }
  ln_bf16(v, xv);
#pragma unroll
  for (int j = 0; j < 8; ++j) tr[512 + lane + 64 * j] = xv[j];
  WAVE_SYNC();
  {
    const u32v4 a0 = reinterpret_cast<const u32v4*>(tr)[128 + lane], a1 = reinterpret_cast<const u32v4*>(tr)[192 + lane];
    __builtin_amdgcn_raw_buffer_store_b128(a0, ars, roff, 0, 16);
    __builtin_amdgcn_raw_buffer_store_b128(a1, ars, roff + 1024, 0, 16);
  }
  KEEP(pfv);
}

#define WAIT_V(n) asm volatile("s_waitcnt vmcnt(" #n ")" ::: "memory")
#define WAIT_L0() asm volatile("s_waitcnt lgkmcnt(0)" ::: "memory")
#define RAW_BAR() __builtin_amdgcn_s_barrier()

__device__ __forceinline__ uint32_t mish_pair(float c0, float c1, f32x2 b) {
  f32x2 x = unpk(cvt_pk(c0, c1)) + b;
  const f32x2 hh = unpk(cvt_pk(x[0], x[1]));
  f32x2 z = hh * 1.44269504088896340736f;
  f32x2 t = {__builtin_amdgcn_exp2f(__builtin_amdgcn_fmed3f(z[0], -200.0f, 43.0f)), __builtin_amdgcn_exp2f(__builtin_amdgcn_fmed3f(z[1], -200.0f, 43.0f))};
  const f32x2 n = t * (t + 2.0f);
  const f32x2 d = n + 2.0f;
  const f32x2 r = {__builtin_amdgcn_rcpf(d[0]), __builtin_amdgcn_rcpf(d[1])};
  const f32x2 o = (hh * n) * r;
  return cvt_pk(o[0], o[1]);
}

typedef __attribute__((ext_vector_type(4))) unsigned int u32x4;
typedef __attribute__((ext_vector_type(2))) unsigned int u32x2;
template <int OFF> __device__ __forceinline__ void lds_rd128(u32x4& d, uint32_t a) { asm volatile("ds_read_b128 %0, %1 offset:%2" : "=v"(d) : "v"(a), "i"(OFF)); }
template <int OFF> __device__ __forceinline__ void lds_rdtr(u32x2& d, uint32_t a) { asm volatile("ds_read_b64_tr_b16 %0, %1 offset:%2" : "=v"(d) : "v"(a), "i"(OFF)); }
__device__ __forceinline__ bf16x8 mk8(u32x2 lo, u32x2 hi) { u32x4 v = {lo[0], lo[1], hi[0], hi[1]}; return __builtin_bit_cast(bf16x8, v); }
#define SCHED0() __builtin_amdgcn_sched_barrier(0)
__device__ __forceinline__ void mfma16(f32x4& c, const u32x2& alo, const u32x2& ahi, const u32x4& b) {
  u32x4 a = {alo[0], alo[1], ahi[0], ahi[1]};
  asm volatile("v_mfma_f32_16x16x32_bf16 %0, %1, %2, %0" : "+v"(c) : "v"(a), "v"(b));
}
typedef __attribute__((address_space(1))) unsigned int gu32;
template <int N> struct IC { static constexpr int v = N; };

constexpr int G1_A_BYTES = 128 * 128, G1_B_BYTES = 64 * 448, G1_STAGE = G1_A_BYTES + G1_B_BYTES;
constexpr int G1_OT = 57344, G1_OTS = 464;
struct G1Frag { u32x4 a[4]; u32x2 b[7][2]; };
__device__ __forceinline__ void g1_body(char* smem, const u16* __restrict__ A, const u16* __restrict__ W, const u16* __restrict__ b1,
                                        u16* __restrict__ act, unsigned* __restrict__ flags) {
  const int tid = threadIdx.x, lane = tid & 63, w8 = __builtin_amdgcn_readfirstlane(tid >> 6), w = w8 & 3;
  const int wm = w >> 1, wn = w & 1;
  const int bid = blockIdx.x, xcd = bid & 7, idx = bid >> 3;
  const int tm = (xcd & 1) * 8 + (idx & 7), tn = (xcd >> 1) * 4 + (idx >> 3);
  const int m0 = tm * 128, n0 = tn * 224;
  uint32_t aoff[4], boff[7];
#pragma unroll
  for (int i = 0; i < 4; ++i) {
    const int r = w * 32 + i * 8 + (lane >> 3);
    aoff[i] = (uint32_t)(m0 + r) * 2048u + (uint32_t)(((lane & 7) ^ (r & 7)) << 4);
  }
#pragma unroll
  for (int i = 0; i < 7; ++i) {
    const int d = i * 64 + lane, r = d / 28, q = d - r * 28, k = w * 16 + r;
    const int c = (q >> 1) ^ ((k >> 3) & 1);
    boff[i] = (uint32_t)k * (uint32_t)(F_ * 2) + (uint32_t)(n0 * 2) + (uint32_t)(((c << 1) | (q & 1)) << 4);
  }
  const char* Ab = reinterpret_cast<const char*>(A);
  const char* Wb = reinterpret_cast<const char*>(W);
  auto issue1 = [&](int i, int kt, int buf) {
    if (i < 4) {
      __builtin_amdgcn_global_load_lds((const unsigned*)(Ab + kt * 128 + aoff[i]),
                                       (unsigned*)(smem + buf * G1_STAGE + w * 4096 + lane * 16 + i * 1024), 16, 0, 16);
    } else {
      __builtin_amdgcn_global_load_lds((const unsigned*)(Wb + (size_t)kt * (64 * F_ * 2) + boff[i - 4]),
                                       (unsigned*)(smem + buf * G1_STAGE + G1_A_BYTES + w * 7168 + lane * 16 + (i - 4) * 1024), 16, 0, 0);
    }
  };
  const auto ars = __builtin_amdgcn_make_buffer_rsrc(act, 0, S_ * F_ * 2, 0x00020000);
  auto g1_store = [&](auto RND) {
    constexpr int rnd = decltype(RND)::v, CPH = rnd == 0 ? 8 : 6, NIT = rnd == 0 ? 4 : 3;
    WAIT_L0();
    RAW_BAR();
#pragma unroll
    for (int i = 0; i < NIT; ++i) {
      const int c = tid + 512 * i, r = c / (2 * CPH), q0 = c - r * (2 * CPH);
      const int q = (q0 >= CPH ? 14 - CPH : 0) + q0 + rnd * 8;
      const u32v4 v = *reinterpret_cast<const u32v4*>(smem + G1_OT + r * G1_OTS + q * 16);
      __builtin_amdgcn_raw_buffer_store_b128(v, ars, (uint32_t)(m0 + r) * (uint32_t)(F_ * 2) + (uint32_t)(n0 * 2 + q * 16), 0, 16);
    }
  };
  constexpr int NK = 16;
  if (w8 >= 4) {
#pragma unroll
    for (int i = 4; i < 11; ++i) issue1(i, 0, 0);
#pragma unroll
    for (int i = 4; i < 11; ++i) issue1(i, 1, 1);
    if (lane == 0) {
      for (int spin = 0; spin < (1 << 22); ++spin) {
        if (__hip_atomic_load((gu32*)(flags + 8192 + tm * 16), __ATOMIC_RELAXED, __HIP_MEMORY_SCOPE_AGENT) == 16u) break;
        __builtin_amdgcn_s_sleep(1);
      }
    }
    asm volatile("" ::: "memory");
#pragma unroll
    for (int i = 0; i < 4; ++i) issue1(i, 0, 0);
#pragma unroll
    for (int i = 0; i < 4; ++i) issue1(i, 1, 1);
    WAIT_V(4); RAW_BAR();
#pragma unroll
    for (int i = 0; i < 11; ++i) issue1(i, 2, 2);
    int buf = 0;
    for (int kt = 0; kt < NK - 3; ++kt) {
      WAIT_V(11); RAW_BAR();
#pragma unroll
      for (int i = 0; i < 11; ++i) issue1(i, kt + 3, buf);
      buf = buf == 2 ? 0 : buf + 1;
    }
    WAIT_V(11); RAW_BAR();
    WAIT_V(0); RAW_BAR();
    uint2 bb[7];
#pragma unroll
    for (int ni = 0; ni < 7; ++ni) bb[ni] = *reinterpret_cast<const uint2*>(b1 + n0 + wn * 112 + ni * 16 + 4 * (lane >> 4));
    RAW_BAR(); RAW_BAR();
    const f32x4* xfer = reinterpret_cast<const f32x4*>(smem) + w * (14 * 64) + lane;
    char* ot = smem + G1_OT + (wm * 64 + (lane & 15)) * G1_OTS + (wn * 112 + 4 * (lane >> 4)) * 2;
#pragma unroll
    for (int ni = 0; ni < 7; ++ni) {
      const f32x2 b01 = unpk(bb[ni].x), b23 = unpk(bb[ni].y);
#pragma unroll
      for (int mi = 2; mi < 4; ++mi) {
        const f32x4 c = xfer[((mi - 2) * 7 + ni) * 64];
        *reinterpret_cast<uint2*>(ot + mi * 16 * G1_OTS + ni * 32) = make_uint2(mish_pair(c[0], c[1], b01), mish_pair(c[2], c[3], b23));
      }
      if (ni == 3) g1_store(IC<0>{});
    }
    g1_store(IC<1>{});
    return;
  }
  uint2 bb[7];
#pragma unroll
  for (int ni = 0; ni < 7; ++ni) bb[ni] = *reinterpret_cast<const uint2*>(b1 + n0 + wn * 112 + ni * 16 + 4 * (lane >> 4));
  f32x4 acc[4][7];
#pragma unroll
  for (int i = 0; i < 4; ++i)
#pragma unroll
    for (int j = 0; j < 7; ++j) acc[i][j] = (f32x4){0.f, 0.f, 0.f, 0.f};
  const int fr = lane & 15, fg = lane >> 4;
  const uint32_t sbase = (uint32_t)(size_t)(smem);
  const uint32_t ard0 = sbase + (wm * 64 + fr) * 128 + ((fg ^ (fr & 7)) << 4);
  const int q4 = (lane & 15) >> 2, p4 = lane & 3;
  const uint32_t brd = sbase + G1_A_BYTES + (8 * fg + q4) * 448 + 8 * p4 + wn * 224;
  const int f32_ = (fg & 1) * 32;
  const uint32_t brdE = brd + ((wn & 1) ? -f32_ : f32_), brdO = brd + ((wn & 1) ? f32_ : -f32_);
  G1Frag X, Y;
  auto rd = [&](G1Frag& Fr, auto I, auto KS, uint32_t so) {
    constexpr int i = decltype(I)::v, ks = decltype(KS)::v;
    if constexpr (i < 4) {
      lds_rd128<i * 2048>(Fr.a[i], (ard0 ^ (ks * 64)) + so);
    } else {
      constexpr int ni = (i - 4) >> 1, t = (i - 4) & 1;
      lds_rdtr<ks * 32 * 448 + ni * 32 + t * 4 * 448>(Fr.b[ni][t], ((ni & 1) ? brdO : brdE) + so);
    }
  };
  auto mma1 = [&](G1Frag& Fr, auto J) {
    constexpr int j = decltype(J)::v, mi = j / 7, ni = j % 7;
    mfma16(acc[mi][ni], Fr.b[ni][0], Fr.b[ni][1], Fr.a[mi]);
  };
#define G1_TIE(Fr) asm volatile("s_waitcnt lgkmcnt(0)" : "+v"(Fr.a[0]), "+v"(Fr.a[1]), "+v"(Fr.a[2]), "+v"(Fr.a[3]), \
    "+v"(Fr.b[0][0]), "+v"(Fr.b[0][1]), "+v"(Fr.b[1][0]), "+v"(Fr.b[1][1]), "+v"(Fr.b[2][0]), "+v"(Fr.b[2][1]), "+v"(Fr.b[3][0]), "+v"(Fr.b[3][1]), \
    "+v"(Fr.b[4][0]), "+v"(Fr.b[4][1]), "+v"(Fr.b[5][0]), "+v"(Fr.b[5][1]), "+v"(Fr.b[6][0]), "+v"(Fr.b[6][1]) :: "memory")
  auto block = [&](G1Frag& C, G1Frag& R, auto KS, uint32_t so, auto DO_RD, auto DO_IS, int kt_is, int buf_is) {
    auto one = [&](auto J) {
      constexpr int j = decltype(J)::v;
      mma1(C, J);
      if constexpr (decltype(DO_RD)::v && j < 18) rd(R, J, KS, so);
      if constexpr (decltype(DO_IS)::v && j >= 6 && j < 28 && ((j - 6) & 1) == 0) issue1((j - 6) >> 1, kt_is, buf_is);
      SCHED0();
    };
    one(IC<0>{}); one(IC<1>{}); one(IC<2>{}); one(IC<3>{}); one(IC<4>{}); one(IC<5>{}); one(IC<6>{});
    one(IC<7>{}); one(IC<8>{}); one(IC<9>{}); one(IC<10>{}); one(IC<11>{}); one(IC<12>{}); one(IC<13>{});
    one(IC<14>{}); one(IC<15>{}); one(IC<16>{}); one(IC<17>{}); one(IC<18>{}); one(IC<19>{}); one(IC<20>{});
    one(IC<21>{}); one(IC<22>{}); one(IC<23>{}); one(IC<24>{}); one(IC<25>{}); one(IC<26>{}); one(IC<27>{});
  };
  auto rdall = [&](G1Frag& R, uint32_t so) {
    rd(R, IC<0>{}, IC<0>{}, so); rd(R, IC<1>{}, IC<0>{}, so); rd(R, IC<2>{}, IC<0>{}, so); rd(R, IC<3>{}, IC<0>{}, so);
    rd(R, IC<4>{}, IC<0>{}, so); rd(R, IC<5>{}, IC<0>{}, so); rd(R, IC<6>{}, IC<0>{}, so); rd(R, IC<7>{}, IC<0>{}, so);
    rd(R, IC<8>{}, IC<0>{}, so); rd(R, IC<9>{}, IC<0>{}, so); rd(R, IC<10>{}, IC<0>{}, so); rd(R, IC<11>{}, IC<0>{}, so);
    rd(R, IC<12>{}, IC<0>{}, so); rd(R, IC<13>{}, IC<0>{}, so); rd(R, IC<14>{}, IC<0>{}, so); rd(R, IC<15>{}, IC<0>{}, so);
    rd(R, IC<16>{}, IC<0>{}, so); rd(R, IC<17>{}, IC<0>{}, so);
  };
  __builtin_amdgcn_s_setprio(2);
  RAW_BAR();
  rdall(X, 0);
  int buf = 0;
  for (int kt = 0; kt < NK - 3; ++kt) {
    const int nbuf = buf == 2 ? 0 : buf + 1;
    G1_TIE(X); SCHED0();
    block(X, Y, IC<1>{}, buf * G1_STAGE, IC<1>{}, IC<0>{}, 0, 0);
    G1_TIE(Y); RAW_BAR(); SCHED0();
    block(Y, X, IC<0>{}, nbuf * G1_STAGE, IC<1>{}, IC<0>{}, 0, 0);
    buf = nbuf;
  }
  G1_TIE(X); SCHED0();
  block(X, Y, IC<1>{}, 1 * G1_STAGE, IC<1>{}, IC<0>{}, 0, 0);
  G1_TIE(Y); RAW_BAR(); SCHED0();
  block(Y, X, IC<0>{}, 2 * G1_STAGE, IC<1>{}, IC<0>{}, 0, 0);
  G1_TIE(X); SCHED0();
  block(X, Y, IC<1>{}, 2 * G1_STAGE, IC<1>{}, IC<0>{}, 0, 0);
  G1_TIE(Y); RAW_BAR(); SCHED0();
  block(Y, X, IC<0>{}, 0 * G1_STAGE, IC<1>{}, IC<0>{}, 0, 0);
  G1_TIE(X); SCHED0();
  block(X, Y, IC<1>{}, 0 * G1_STAGE, IC<1>{}, IC<0>{}, 0, 0);
  G1_TIE(Y); SCHED0();
  block(Y, X, IC<0>{}, 0, IC<0>{}, IC<0>{}, 0, 0);
  asm volatile("s_nop 15\n\ts_nop 15" : "+v"(acc[0][0]), "+v"(acc[0][1]), "+v"(acc[0][2]), "+v"(acc[0][3]), "+v"(acc[0][4]), "+v"(acc[0][5]), "+v"(acc[0][6]), "+v"(acc[1][0]), "+v"(acc[1][1]), "+v"(acc[1][2]), "+v"(acc[1][3]), "+v"(acc[1][4]), "+v"(acc[1][5]), "+v"(acc[1][6]), "+v"(acc[2][0]), "+v"(acc[2][1]), "+v"(acc[2][2]), "+v"(acc[2][3]), "+v"(acc[2][4]), "+v"(acc[2][5]), "+v"(acc[2][6]), "+v"(acc[3][0]), "+v"(acc[3][1]), "+v"(acc[3][2]), "+v"(acc[3][3]), "+v"(acc[3][4]), "+v"(acc[3][5]), "+v"(acc[3][6]) :: "memory");
  SCHED0();
  __builtin_amdgcn_s_setprio(0);
  RAW_BAR();
  {
    f32x4* xfer = reinterpret_cast<f32x4*>(smem) + w * (14 * 64) + lane;
#pragma unroll
    for (int mi = 2; mi < 4; ++mi)
#pragma unroll
      for (int ni = 0; ni < 7; ++ni) xfer[((mi - 2) * 7 + ni) * 64] = acc[mi][ni];
  }
  WAIT_L0();
  RAW_BAR();
  char* ot = smem + G1_OT + (wm * 64 + fr) * G1_OTS + (wn * 112 + 4 * fg) * 2;
#pragma unroll
  for (int ni = 0; ni < 7; ++ni) {
    const f32x2 b01 = unpk(bb[ni].x), b23 = unpk(bb[ni].y);
#pragma unroll
    for (int mi = 0; mi < 2; ++mi) {
      const f32x4 c = acc[mi][ni];
      *reinterpret_cast<uint2*>(ot + mi * 16 * G1_OTS + ni * 32) = make_uint2(mish_pair(c[0], c[1], b01), mish_pair(c[2], c[3], b23));
    }
    if (ni == 3) g1_store(IC<0>{});
  }
  g1_store(IC<1>{});
}

constexpr int G2_A_BYTES = 128 * 128, G2_B_BYTES = 64 * 256, G2_STAGE = G2_A_BYTES + G2_B_BYTES;
struct G2Frag { u32x4 a[4]; u32x2 b[4][2]; };
__device__ __forceinline__ void g2_body(char* smem, const u16* __restrict__ A, const u16* __restrict__ W, const u16* __restrict__ h,
                                        u16* __restrict__ out, float* __restrict__ part, unsigned* __restrict__ flags) {
  const int tid = threadIdx.x, lane = tid & 63, w8 = __builtin_amdgcn_readfirstlane(tid >> 6), w = w8 & 3;
  const int wm = w >> 1, wn = w & 1;
  const int bid = blockIdx.x, xcd = bid & 7, idx = bid >> 3;
  const int kh = xcd & 1, tm = ((xcd >> 1) & 1) * 8 + (idx & 7), tn = (xcd >> 2) * 4 + (idx >> 3);
  const int m0 = tm * 128, n0 = tn * 128, tile = tm * 8 + tn;
  auto g2_store = [&]() {
    WAIT_L0();
    RAW_BAR();
#pragma unroll
    for (int i = 0; i < 2; ++i) {
      const int c = tid + 512 * i, r = c >> 4, q = c & 15;
      const int row = (r >> 5) * 64 + kh * 32 + (r & 31);
      const uint4 v = *reinterpret_cast<const uint4*>(smem + r * 272 + q * 16);
      *reinterpret_cast<uint4*>(reinterpret_cast<char*>(out) + (size_t)(m0 + row) * (D_ * 2) + n0 * 2 + q * 16) = v;
    }
  };
  constexpr int NK = 28;
  if (w8 >= 4) {
    uint32_t aoff[4], boff[4];
#pragma unroll
    for (int i = 0; i < 4; ++i) {
      const int r = w * 32 + i * 8 + (lane >> 3);
      aoff[i] = (uint32_t)(m0 + r) * (uint32_t)(F_ * 2) + (uint32_t)(((lane & 7) ^ (r & 7)) << 4);
    }
#pragma unroll
    for (int i = 0; i < 4; ++i) {
      const int k = w * 16 + i * 4 + (lane >> 4), q = lane & 15;
      const int c = (q >> 1) ^ ((k & 3) | (((k >> 3) & 1) << 2));
      boff[i] = (uint32_t)k * (uint32_t)(D_ * 2) + (uint32_t)(n0 * 2) + (uint32_t)(((c << 1) | (q & 1)) << 4);
    }
    const char* Ab = reinterpret_cast<const char*>(A) + kh * (1792 * 2);
    const char* Wb = reinterpret_cast<const char*>(W) + (size_t)kh * (1792 * D_ * 2);
    auto issueA = [&](int kt, int buf) {
#pragma unroll
      for (int i = 0; i < 4; ++i)
        __builtin_amdgcn_global_load_lds((const unsigned*)(Ab + kt * 128 + aoff[i]),
                                         (unsigned*)(smem + buf * G2_STAGE + w * 4096 + lane * 16 + i * 1024), 16, 0, 16);
    };
    auto issueB = [&](int kt, int buf) {
#pragma unroll
      for (int i = 0; i < 4; ++i)
        __builtin_amdgcn_global_load_lds((const unsigned*)(Wb + (size_t)kt * (64 * D_ * 2) + boff[i]),
                                         (unsigned*)(smem + buf * G2_STAGE + G2_A_BYTES + w * 4096 + lane * 16 + i * 1024), 16, 0, 0);
    };
    auto issue = [&](int kt, int buf) { issueA(kt, buf); issueB(kt, buf); };
    issueB(0, 0); issueB(1, 1);
    if (lane == 0) {
      for (int spin = 0; spin < (1 << 22); ++spin) {
        if (__hip_atomic_load((gu32*)(flags + 4096 + (tm * 2 + kh) * 16), __ATOMIC_RELAXED, __HIP_MEMORY_SCOPE_AGENT) == 8u) break;
        __builtin_amdgcn_s_sleep(1);
      }
    }
    asm volatile("" ::: "memory");
    issueA(0, 0); issueA(1, 1);
    WAIT_V(4); RAW_BAR();
    issue(2, 2); issue(3, 3);
    for (int kt = 0; kt < NK - 4; ++kt) {
      WAIT_V(16); RAW_BAR();
      issue(kt + 4, kt & 3);
    }
    WAIT_V(16); RAW_BAR();
    WAIT_V(8); RAW_BAR();
    WAIT_V(0); RAW_BAR();
    RAW_BAR();
    RAW_BAR();
    g2_store();
    return;
  }
  const int fr = lane & 15, fg = lane >> 4;
  u32x2 hv[2][4];
  f32x4 acc[4][4];
#pragma unroll
  for (int i = 0; i < 4; ++i)
#pragma unroll
    for (int j = 0; j < 4; ++j) acc[i][j] = (f32x4){0.f, 0.f, 0.f, 0.f};
  const uint32_t sbase = (uint32_t)(size_t)(smem);
  const uint32_t ard0 = sbase + (wm * 64 + fr) * 128 + ((fg ^ (fr & 7)) << 4);
  const int q4 = (lane & 15) >> 2, p4 = lane & 3;
  const int bsw = q4 | ((fg & 1) << 2);
  const uint32_t brd = sbase + G2_A_BYTES + (8 * fg + q4) * 256 + 8 * p4;
  uint32_t brdn[4];
#pragma unroll
  for (int ni = 0; ni < 4; ++ni) brdn[ni] = brd + (((wn * 4 + ni) ^ bsw) * 32);
  G2Frag X, Y;
  auto rd = [&](G2Frag& Fr, auto I, auto KS, uint32_t so) {
    constexpr int i = decltype(I)::v, ks = decltype(KS)::v;
    if constexpr (i < 4) {
      lds_rd128<i * 2048>(Fr.a[i], (ard0 ^ (ks * 64)) + so);
    } else {
      constexpr int ni = (i - 4) >> 1, t = (i - 4) & 1;
      lds_rdtr<ks * 32 * 256 + t * 4 * 256>(Fr.b[ni][t], brdn[ni] + so);
    }
  };
  auto mma1 = [&](G2Frag& Fr, auto J) {
    constexpr int j = decltype(J)::v, mi = j / 4, ni = j % 4;
    mfma16(acc[mi][ni], Fr.b[ni][0], Fr.b[ni][1], Fr.a[mi]);
  };
#define G2_TIE(Fr) asm volatile("s_waitcnt lgkmcnt(0)" : "+v"(Fr.a[0]), "+v"(Fr.a[1]), "+v"(Fr.a[2]), "+v"(Fr.a[3]), \
    "+v"(Fr.b[0][0]), "+v"(Fr.b[0][1]), "+v"(Fr.b[1][0]), "+v"(Fr.b[1][1]), "+v"(Fr.b[2][0]), "+v"(Fr.b[2][1]), "+v"(Fr.b[3][0]), "+v"(Fr.b[3][1]) :: "memory")
  auto block = [&](G2Frag& C, G2Frag& R, auto KS, uint32_t so, auto DO_RD) {
    auto one = [&](auto J) {
      constexpr int j = decltype(J)::v;
      mma1(C, J);
      if constexpr (decltype(DO_RD)::v && j < 12) rd(R, J, KS, so);
      SCHED0();
    };
    one(IC<0>{}); one(IC<1>{}); one(IC<2>{}); one(IC<3>{}); one(IC<4>{}); one(IC<5>{}); one(IC<6>{}); one(IC<7>{});
    one(IC<8>{}); one(IC<9>{}); one(IC<10>{}); one(IC<11>{}); one(IC<12>{}); one(IC<13>{}); one(IC<14>{}); one(IC<15>{});
  };
  __builtin_amdgcn_s_setprio(2);
  RAW_BAR();
  {
    const auto hrs = __builtin_amdgcn_make_buffer_rsrc(const_cast<u16*>(h), 0, S_ * D_ * 2, 0x00020000);
#pragma unroll
    for (int i = 0; i < 2; ++i)
#pragma unroll
      for (int ni = 0; ni < 4; ++ni)
        hv[i][ni] = __builtin_amdgcn_raw_buffer_load_b64(hrs, (uint32_t)((m0 + wm * 64 + (2 * kh + i) * 16 + fr) * D_ + n0 + wn * 64 + ni * 16 + 4 * fg) * 2u, 0, 16);
  }
  rd(X, IC<0>{}, IC<0>{}, 0); rd(X, IC<1>{}, IC<0>{}, 0); rd(X, IC<2>{}, IC<0>{}, 0); rd(X, IC<3>{}, IC<0>{}, 0);
  rd(X, IC<4>{}, IC<0>{}, 0); rd(X, IC<5>{}, IC<0>{}, 0); rd(X, IC<6>{}, IC<0>{}, 0); rd(X, IC<7>{}, IC<0>{}, 0);
  rd(X, IC<8>{}, IC<0>{}, 0); rd(X, IC<9>{}, IC<0>{}, 0); rd(X, IC<10>{}, IC<0>{}, 0); rd(X, IC<11>{}, IC<0>{}, 0);
  for (int kt = 0; kt < NK - 1; ++kt) {
    G2_TIE(X); SCHED0();
    block(X, Y, IC<1>{}, (kt & 3) * G2_STAGE, IC<1>{});
    G2_TIE(Y); RAW_BAR(); SCHED0();
    block(Y, X, IC<0>{}, ((kt + 1) & 3) * G2_STAGE, IC<1>{});
  }
  G2_TIE(X); SCHED0();
  block(X, Y, IC<1>{}, ((NK - 1) & 3) * G2_STAGE, IC<1>{});
  G2_TIE(Y); SCHED0();
  block(Y, X, IC<0>{}, 0, IC<0>{});
  asm volatile("s_nop 15\n\ts_nop 15" : "+v"(acc[0][0]), "+v"(acc[0][1]), "+v"(acc[0][2]), "+v"(acc[0][3]), "+v"(acc[1][0]), "+v"(acc[1][1]), "+v"(acc[1][2]), "+v"(acc[1][3]), "+v"(acc[2][0]), "+v"(acc[2][1]), "+v"(acc[2][2]), "+v"(acc[2][3]), "+v"(acc[3][0]), "+v"(acc[3][1]), "+v"(acc[3][2]), "+v"(acc[3][3]) :: "memory");
  SCHED0();
  const auto rs = __builtin_amdgcn_make_buffer_rsrc(part, 0, 256 * 32768, 0x00020000);
  const uint32_t myoff = (uint32_t)(tile * 2 + kh) * 32768u + (uint32_t)w * 8192u + (uint32_t)lane * 16u;
  const uint32_t proff = (uint32_t)(tile * 2 + (kh ^ 1)) * 32768u + (uint32_t)w * 8192u + (uint32_t)lane * 16u;
  auto send = [&](const f32x4 (&r0)[4], const f32x4 (&r1)[4]) {
#pragma unroll
    for (int ni = 0; ni < 4; ++ni) {
      __builtin_amdgcn_raw_buffer_store_b128(__builtin_bit_cast(u32x4, r0[ni]), rs, myoff + ni * 1024, 0, 16);
      __builtin_amdgcn_raw_buffer_store_b128(__builtin_bit_cast(u32x4, r1[ni]), rs, myoff + 4096 + ni * 1024, 0, 16);
    }
  };
  if (kh == 0) send(acc[2], acc[3]); else send(acc[0], acc[1]);
  WAIT_V(0);
  RAW_BAR();
  if (w == 0) {
    if (lane == 0) {
      __hip_atomic_store((gu32*)(flags + (tile * 2 + kh) * 16), 1u, __ATOMIC_RELAXED, __HIP_MEMORY_SCOPE_AGENT);
      for (int spin = 0; spin < (1 << 22); ++spin) {
        if (__hip_atomic_load((gu32*)(flags + (tile * 2 + (kh ^ 1)) * 16), __ATOMIC_RELAXED, __HIP_MEMORY_SCOPE_AGENT) == 1u) break;
        __builtin_amdgcn_s_sleep(1);
      }
    }
  }
  asm volatile("" ::: "memory");
  RAW_BAR();
  asm volatile("" ::: "memory");
  auto fin = [&](auto I, const f32x4 (&own)[4]) {
    constexpr int i = decltype(I)::v;
    char* ot = smem + (wm * 32 + i * 16 + fr) * 272 + wn * 128 + 8 * fg;
    f32x4 pr[4];
#pragma unroll
    for (int ni = 0; ni < 4; ++ni) pr[ni] = __builtin_bit_cast(f32x4, __builtin_amdgcn_raw_buffer_load_b128(rs, proff + i * 4096 + ni * 1024, 0, 16));
#pragma unroll
    for (int ni = 0; ni < 4; ++ni) {
      const f32x4 c = kh == 0 ? own[ni] + pr[ni] : pr[ni] + own[ni];
      const f32x2 r01 = unpk(hv[i][ni][0]) + unpk(cvt_pk(c[0], c[1])), r23 = unpk(hv[i][ni][1]) + unpk(cvt_pk(c[2], c[3]));
      *reinterpret_cast<uint2*>(ot + ni * 32) = make_uint2(cvt_pk(r01[0], r01[1]), cvt_pk(r23[0], r23[1]));
    }
  };
  if (kh == 0) { fin(IC<0>{}, acc[0]); fin(IC<1>{}, acc[1]); }
  else { fin(IC<0>{}, acc[2]); fin(IC<1>{}, acc[3]); }
  g2_store();
}

__global__ __launch_bounds__(512, 2) void k3g_fused(const c2* __restrict__ U, const u16* __restrict__ x, u16* __restrict__ A,
                                                     const u16* __restrict__ W1, const u16* __restrict__ b1,
                                                     u16* __restrict__ act, const u16* __restrict__ W2, u16* __restrict__ h,
                                                     u16* __restrict__ out, float* __restrict__ part, unsigned* __restrict__ flags) {
  __shared__ __attribute__((aligned(16))) char smem[3 * G1_STAGE];
  uint32_t cpf = 0;
  if ((blockIdx.x >> 3) < 2 && threadIdx.x >= 288) {
    const char* pc = reinterpret_cast<const char*>(__builtin_amdgcn_s_getpc());
    cpf = *reinterpret_cast<const uint32_t*>(pc + 4096 + (threadIdx.x - 288) * 128);
  }
  k3_body(smem, U, x, h, A, reinterpret_cast<const char*>(W2));
  KEEP(cpf);
  WAIT_V(0);
  WAIT_L0();
  RAW_BAR();
  if (threadIdx.x == 0)
    __hip_atomic_fetch_add((gu32*)(flags + 8192 + (blockIdx.x >> 4) * 16), 1u, __ATOMIC_RELAXED, __HIP_MEMORY_SCOPE_AGENT);
  g1_body(smem, A, W1, b1, act, flags);
  WAIT_V(0);
  RAW_BAR();
  if (threadIdx.x == 0) {
    const int xcd = blockIdx.x & 7, idx = blockIdx.x >> 3;
    const int tm = (xcd & 1) * 8 + (idx & 7), tn = (xcd >> 1) * 4 + (idx >> 3);
    __hip_atomic_fetch_add((gu32*)(flags + 4096 + (tm * 2 + (tn >> 3)) * 16), 1u, __ATOMIC_RELAXED, __HIP_MEMORY_SCOPE_AGENT);
  }
  g2_body(smem, act, W2, h, out, part, flags);
}

extern "C" void kernel_launch(void* const* d_in, const int* in_sizes, int n_in,
                              void* d_out, int out_size, void* d_ws, size_t ws_size,
                              hipStream_t stream) {
  const u16* x = (const u16*)d_in[0];
  const u16* wr = (const u16*)d_in[1];
  const u16* wi = (const u16*)d_in[2];
  const u16* w1 = (const u16*)d_in[4];
  const u16* b1 = (const u16*)d_in[5];
  const u16* w2 = (const u16*)d_in[6];
  u16* out = (u16*)d_out;
  char* ws = (char*)d_ws;
  c2* T = (c2*)ws;
  uint32_t* WT = (uint32_t*)(ws + (9u << 20));
  u16* h = (u16*)(ws + (16u << 20));
  u16* a = (u16*)(ws + (20u << 20));
  u16* act = (u16*)(ws + (24u << 20));
  float* part = (float*)(ws + (40u << 20));
  unsigned* flags = (unsigned*)(ws + (48u << 20));
  k1_ln_rowfft<<<256, 512, 0, stream>>>(x, wr, wi, T, WT, flags);
  c2* U = (c2*)(ws + (50u << 20));
  k2_colfft<<<512, 256, 0, stream>>>(T, U, WT, (const char*)w1);
  k3g_fused<<<256, 512, 0, stream>>>(U, x, a, w1, b1, act, w2, h, out, part, flags);
}
```

```cpp
#include <hip/hip_runtime.h>
#include <hip/hip_bf16.h>
#include <stdint.h>

typedef unsigned short u16;
typedef __attribute__((ext_vector_type(8))) short bf16x8;
typedef __attribute__((ext_vector_type(4))) short bf16x4;
typedef __attribute__((ext_vector_type(4))) float f32x4;
typedef __attribute__((address_space(3))) bf16x4* lds_b4_ptr;

constexpr int TBS = 4096 + 16;
constexpr int S_ = 2048, D_ = 1024, F_ = 3584, DF_ = 513;

__device__ __forceinline__ float bf2f(u16 v) { return __uint_as_float(((uint32_t)v) << 16); }
__device__ __forceinline__ u16 f2bf(float f) { __bf16 b = (__bf16)f; return __builtin_bit_cast(u16, b); }
__device__ __forceinline__ float rbf(float f) { return bf2f(f2bf(f)); }

typedef __attribute__((ext_vector_type(4))) unsigned int u32v4;
typedef __attribute__((ext_vector_type(4))) float f32v4;
template <typename T> __device__ __forceinline__ void st_nt(T* p, T v) { __builtin_nontemporal_store(v, p); }
typedef __attribute__((ext_vector_type(2))) float c2;
typedef c2 f32x2;
__device__ __forceinline__ uint32_t cvt_pk(float lo, float hi) { uint32_t r; asm("v_cvt_pk_bf16_f32 %0, %1, %2" : "=v"(r) : "v"(lo), "v"(hi)); return r; }
__device__ __forceinline__ c2 unpk(uint32_t u) { return (c2){__uint_as_float(u << 16), __uint_as_float(u & 0xffff0000u)}; }
__device__ __forceinline__ c2 rbf2(c2 v) { return unpk(cvt_pk(v.x, v.y)); }
__device__ __forceinline__ c2 cmul(c2 a, c2 b) {
  c2 t, r;
  asm("v_pk_mul_f32 %0, %1, %2 op_sel:[0,0] op_sel_hi:[0,1]" : "=v"(t) : "v"(a), "v"(b));
  asm("v_pk_fma_f32 %0, %1, %2, %3 op_sel:[1,1,0] op_sel_hi:[1,0,1] neg_lo:[0,1,0]" : "=v"(r) : "v"(a), "v"(b), "v"(t));
  return r;
}
__device__ __forceinline__ c2 sub_i(c2 t, c2 d) { c2 r; asm("v_pk_add_f32 %0, %1, %2 op_sel:[0,1] op_sel_hi:[1,0] neg_hi:[0,1]" : "=v"(r) : "v"(t), "v"(d)); return r; }
__device__ __forceinline__ c2 add_i(c2 t, c2 d) { c2 r; asm("v_pk_add_f32 %0, %1, %2 op_sel:[0,1] op_sel_hi:[1,0] neg_lo:[0,1]" : "=v"(r) : "v"(t), "v"(d)); return r; }
__device__ __forceinline__ c2 add_c(c2 a, c2 b) { c2 r; asm("v_pk_add_f32 %0, %1, %2 neg_hi:[0,1]" : "=v"(r) : "v"(a), "v"(b)); return r; }
__device__ __forceinline__ c2 sub_c(c2 a, c2 b) { c2 r; asm("v_pk_add_f32 %0, %1, %2 neg_lo:[0,1]" : "=v"(r) : "v"(a), "v"(b)); return r; }
template <int DIR> __device__ __forceinline__ c2 addD(c2 t, c2 d) { return DIR > 0 ? add_i(t, d) : sub_i(t, d); }
template <int DIR> __device__ __forceinline__ c2 subD(c2 t, c2 d) { return DIR > 0 ? sub_i(t, d) : add_i(t, d); }
template <int DIR>
__device__ __forceinline__ c2 cexp2pi(float rev) {
  const float r = DIR > 0 ? rev : -rev;
  c2 w = {__builtin_amdgcn_cosf(r), __builtin_amdgcn_sinf(r)};
  asm("s_nop 0" : "+v"(w));
  return w;
}
template <int DIR>
__device__ __forceinline__ void dft4(c2& x0, c2& x1, c2& x2, c2& x3) {
  const c2 t0 = x0 + x2, t1 = x0 - x2, t2 = x1 + x3, d = x1 - x3;
  x0 = t0 + t2; x2 = t0 - t2; x1 = addD<DIR>(t1, d); x3 = subD<DIR>(t1, d);
}
template <int DIR>
__device__ __forceinline__ void dft8(c2 (&v)[8]) {
  c2 e0 = v[0], e1 = v[2], e2 = v[4], e3 = v[6];
  c2 o0 = v[1], o1 = v[3], o2 = v[5], o3 = v[7];
  dft4<DIR>(e0, e1, e2, e3);
  dft4<DIR>(o0, o1, o2, o3);
  const c2 r = {0.70710678118654752440f, 0.70710678118654752440f};
  const c2 u1 = addD<DIR>(o1, o1), u3 = subD<DIR>(o3, o3);
  v[0] = e0 + o0; v[4] = e0 - o0;
  v[1] = __builtin_elementwise_fma(u1, r, e1); v[5] = __builtin_elementwise_fma(u1, -r, e1);
  v[2] = addD<DIR>(e2, o2); v[6] = subD<DIR>(e2, o2);
  v[3] = __builtin_elementwise_fma(u3, -r, e3); v[7] = __builtin_elementwise_fma(u3, r, e3);
}
__device__ __forceinline__ void twiddle8(c2 (&v)[8], c2 w) {
  const c2 w2 = cmul(w, w), w3 = cmul(w2, w), w4 = cmul(w2, w2), w5 = cmul(w4, w), w6 = cmul(w3, w3), w7 = cmul(w4, w3);
  v[1] = cmul(v[1], w); v[2] = cmul(v[2], w2); v[3] = cmul(v[3], w3); v[4] = cmul(v[4], w4);
  v[5] = cmul(v[5], w5); v[6] = cmul(v[6], w6); v[7] = cmul(v[7], w7);
}
__device__ __forceinline__ void twiddle8b(c2 (&v)[8], c2 b, c2 w) {
  const c2 w2 = cmul(w, w), w4 = cmul(w2, w2);
  const c2 b1 = cmul(b, w), b2 = cmul(b, w2), b3 = cmul(b1, w2), b4 = cmul(b, w4), b5 = cmul(b1, w4), b6 = cmul(b2, w4), b7 = cmul(b3, w4);
  v[0] = cmul(v[0], b); v[1] = cmul(v[1], b1); v[2] = cmul(v[2], b2); v[3] = cmul(v[3], b3);
  v[4] = cmul(v[4], b4); v[5] = cmul(v[5], b5); v[6] = cmul(v[6], b6); v[7] = cmul(v[7], b7);
}

__device__ __forceinline__ float wave_sum(float v) {
#define DPP_ADD(ctrl, rmask) v += __uint_as_float(__builtin_amdgcn_update_dpp(0u, __float_as_uint(v), ctrl, rmask, 0xf, true))
  DPP_ADD(0x111, 0xf); DPP_ADD(0x112, 0xf); DPP_ADD(0x114, 0xf); DPP_ADD(0x118, 0xf);
  DPP_ADD(0x142, 0xa); DPP_ADD(0x143, 0xc);
#undef DPP_ADD
  return __uint_as_float(__builtin_amdgcn_readlane(__float_as_uint(v), 63));
}

#define WAVE_SYNC() do { __builtin_amdgcn_fence(__ATOMIC_RELEASE, "wavefront"); __builtin_amdgcn_wave_barrier(); __builtin_amdgcn_fence(__ATOMIC_ACQUIRE, "wavefront"); } while (0)

template <int DIR>
__device__ __forceinline__ void fft512_wave(c2 (&v)[8], c2* ex, int lane) {
  const int hi = lane >> 3, lo = lane & 7;
  dft8<DIR>(v);
  twiddle8(v, cexp2pi<DIR>((float)hi * (1.0f / 64.0f)));
#pragma unroll
  for (int k0 = 0; k0 < 8; ++k0) ex[72 * k0 + lane] = v[k0];
  WAVE_SYNC();
#pragma unroll
  for (int b = 0; b < 8; ++b) v[b] = ex[72 * hi + 8 * b + lo];
  WAVE_SYNC();
  dft8<DIR>(v);
  twiddle8b(v, cexp2pi<DIR>((float)(lo * hi) * (1.0f / 512.0f)), cexp2pi<DIR>((float)lo * (1.0f / 64.0f)));
#pragma unroll
  for (int k1 = 0; k1 < 8; ++k1) ex[66 * lo + 8 * k1 + hi] = v[k1];
  WAVE_SYNC();
#pragma unroll
  for (int c = 0; c < 8; ++c) v[c] = ex[66 * c + lane];
  WAVE_SYNC();
  dft8<DIR>(v);
}

__device__ __forceinline__ void ln_bf16(c2 (&e)[8], uint32_t (&o)[8]) {
  c2 s2 = ((e[0] + e[1]) + (e[2] + e[3])) + ((e[4] + e[5]) + (e[6] + e[7]));
  const float mean = wave_sum(s2.x + s2.y) * (1.0f / 1024.0f);
  c2 q2 = {0.f, 0.f};
#pragma unroll
  for (int i = 0; i < 8; ++i) { const c2 d = e[i] - mean; q2 = __builtin_elementwise_fma(d, d, q2); }
  const float var = wave_sum(q2.x + q2.y) * (1.0f / 1024.0f);
  const float mb = rbf(mean);
  const float sd = rbf(sqrtf(rbf(rbf(var) + 1e-5f)));
  const float ri = 1.0f / sd;
#pragma unroll
  for (int i = 0; i < 8; ++i) {
    const c2 d = rbf2(e[i] - mb);
    const c2 q = d * ri;
    const c2 rem = __builtin_elementwise_fma(-q, (c2){sd, sd}, d);
    const c2 qq = __builtin_elementwise_fma(rem, (c2){ri, ri}, q);
    o[i] = cvt_pk(qq.x, qq.y);
  }
}

__global__ __launch_bounds__(512) void k1_ln_rowfft(const u16* __restrict__ x, const u16* __restrict__ wr, const u16* __restrict__ wi,
                                                     c2* __restrict__ T, uint32_t* __restrict__ WT, unsigned* __restrict__ flags) {
  __shared__ c2 ex_all[8 * 576];
  __shared__ c2 st[512 * 9];
  const int tid = threadIdx.x, lane = tid & 63, w = tid >> 6;
  const int s0 = blockIdx.x * 8;
  const int row = s0 + w;
  const uint32_t* xr = reinterpret_cast<const uint32_t*>(x + (size_t)row * D_);
  uint32_t xu[8];
#pragma unroll
  for (int a = 0; a < 8; ++a) xu[a] = xr[lane + 64 * a];
  if (tid == 0) {
    flags[blockIdx.x * 16] = 0;
    if (blockIdx.x < 32) { flags[4096 + blockIdx.x * 16] = 0; flags[12288 + blockIdx.x * 16] = 0; }
    if (blockIdx.x < 16) flags[8192 + blockIdx.x * 16] = 0;
  }
  {
    uint32_t p[8];
#pragma unroll
    for (int r = 0; r < 8; ++r) {
      const size_t o = (size_t)(s0 + r) * DF_ + tid;
      p[r] = (uint32_t)wr[o] | ((uint32_t)wi[o] << 16);
    }
    u32v4* dst = reinterpret_cast<u32v4*>(WT + (size_t)tid * S_ + s0);
    st_nt(dst, (u32v4){p[0], p[1], p[2], p[3]});
    st_nt(dst + 1, (u32v4){p[4], p[5], p[6], p[7]});
    if (tid < 8) {
      const size_t o = (size_t)(s0 + tid) * DF_ + 512;
      WT[(size_t)512 * S_ + s0 + tid] = (uint32_t)wr[o] | ((uint32_t)wi[o] << 16);
    }
  }
  c2 v[8];
#pragma unroll
  for (int a = 0; a < 8; ++a) v[a] = unpk(xu[a]);
  ln_bf16(v, xu);
#pragma unroll
  for (int a = 0; a < 8; ++a) v[a] = unpk(xu[a]);
  c2* ex = ex_all + w * 576;
  fft512_wave<-1>(v, ex, lane);
#pragma unroll
  for (int j = 4; j < 8; ++j) ex[lane + 64 * j] = v[j];
  ex[512 + lane] = v[0];
  WAVE_SYNC();
  const c2 tw0 = cexp2pi<-1>((float)lane * (1.0f / 1024.0f));
  const c2 half = {0.5f, 0.5f};
#pragma unroll
  for (int j = 0; j < 4; ++j) {
    const int k = lane + 64 * j;
    const c2 z = v[j], zp = ex[512 - k];
    const c2 cj = j == 0 ? (c2){1.f, 0.f} : j == 1 ? (c2){0.92387953251128674f, -0.38268343236508977f}
                : j == 2 ? (c2){0.70710678118654752f, -0.70710678118654752f} : (c2){0.38268343236508977f, -0.92387953251128674f};
    const c2 tw = j == 0 ? tw0 : cmul(tw0, cj);
    const c2 E = add_c(z, zp) * half, Dz = sub_c(z, zp) * half;
    const c2 P = cmul(tw, Dz);
    c2 Xa = sub_i(E, P), Xb = add_i(E, P);
    Xb.y = -Xb.y;
    if (k == 0) { Xa = (c2){z.x + z.y, z.x - z.y}; Xb = (c2){v[4].x, -v[4].y}; }
    st[k * 9 + w] = Xa;
    st[(k == 0 ? 256 : 512 - k) * 9 + w] = Xb;
  }
  __syncthreads();
#pragma unroll
  for (int it = 0; it < 4; ++it) {
    const int idx = it * 512 + tid;
    const int k = idx >> 2, part = idx & 3;
    const c2 a = st[k * 9 + 2 * part], b = st[k * 9 + 2 * part + 1];
    st_nt(reinterpret_cast<f32v4*>(T + (size_t)k * S_ + s0) + part, (f32v4){a.x, a.y, b.x, b.y});
  }
}

#define KEEP(v) asm volatile("" :: "v"(v))
__device__ __forceinline__ uint32_t wt_at(const uint32_t* WT, int k1, int c) { return WT[(size_t)c * S_ + k1]; }
__global__ __launch_bounds__(256) void k2_colfft(const c2* __restrict__ T, c2* __restrict__ U, const uint32_t* __restrict__ WT, const char* __restrict__ pf) {
  __shared__ c2 ex[2304];
  const int t = threadIdx.x, col = blockIdx.x;
  c2 v[8];
#pragma unroll
  for (int a = 0; a < 8; ++a) v[a] = T[(size_t)col * S_ + t + 256 * a];
  uint32_t wv[8];
#pragma unroll
  for (int r = 0; r < 8; ++r) wv[r] = wt_at(WT, t + 256 * r, col);
  uint32_t pfv = 0;
  if (t < 112) pfv = *reinterpret_cast<const uint32_t*>(pf + ((size_t)col * 112 + t) * 128);
  c2* Tg = U + (size_t)(t >> 3) * TBS + col * 8 + (t & 7);
  const int h5 = t >> 5, l5 = t & 31;
  const int m3 = (t >> 2) & 7, d2 = t & 3;
  dft8<-1>(v);
  twiddle8(v, cexp2pi<-1>((float)t * (1.0f / 2048.0f)));
#pragma unroll
  for (int k0 = 0; k0 < 8; ++k0) ex[256 * k0 + t] = v[k0];
  __syncthreads();
#pragma unroll
  for (int b = 0; b < 8; ++b) v[b] = ex[256 * h5 + 32 * b + l5];
  __syncthreads();
  dft8<-1>(v);
  twiddle8(v, cexp2pi<-1>((float)l5 * (1.0f / 256.0f)));
#pragma unroll
  for (int k1 = 0; k1 < 8; ++k1) ex[288 * h5 + 36 * k1 + l5] = v[k1];
  WAVE_SYNC();
#pragma unroll
  for (int c = 0; c < 8; ++c) v[c] = ex[288 * h5 + 36 * m3 + 4 * c + d2];
  __syncthreads();
  dft8<-1>(v);
  twiddle8(v, cexp2pi<-1>((float)d2 * (1.0f / 32.0f)));
#pragma unroll
  for (int k2 = 0; k2 < 8; ++k2) ex[(4 * k2 + d2) * 66 + 8 * m3 + h5] = v[k2];
  __syncthreads();
  const int dd = t >> 6, l6 = t & 63;
#pragma unroll
  for (int d = 0; d < 4; ++d) { v[d] = ex[(4 * dd + d) * 66 + l6]; v[4 + d] = ex[(4 * (dd + 4) + d) * 66 + l6]; }
  __syncthreads();
  dft4<-1>(v[0], v[1], v[2], v[3]);
  dft4<-1>(v[4], v[5], v[6], v[7]);
  if (col == 0) {
#pragma unroll
    for (int k3 = 0; k3 < 4; ++k3) { ex[t + 512 * k3] = v[k3]; ex[t + 256 + 512 * k3] = v[4 + k3]; }
    __syncthreads();
  }
  if (col != 0) {
#pragma unroll
    for (int k3 = 0; k3 < 4; ++k3) {
      v[k3] = cmul(v[k3], unpk(wv[2 * k3]));
      v[4 + k3] = cmul(v[4 + k3], unpk(wv[2 * k3 + 1]));
    }
  } else {
#pragma unroll
    for (int r = 0; r < 8; ++r) {
      const int vi = (r & 1) * 4 + (r >> 1);
      const int k = t + 256 * r, kn = (2048 - k) & 2047;
      const c2 p = v[vi], pn = ex[kn];
      const c2 F0 = {0.5f * (p.x + pn.x), 0.5f * (p.y - pn.y)};
      const c2 F5 = {0.5f * (p.y + pn.y), 0.5f * (pn.x - p.x)};
      const c2 wa = unpk(wv[r]), wan = unpk(wt_at(WT, kn, 0));
      const c2 wb = unpk(wt_at(WT, k, 512)), wbn = unpk(wt_at(WT, kn, 512));
      const c2 W0 = {0.5f * (wa.x + wan.x), 0.5f * (wa.y - wan.y)};
      const c2 W5 = {0.5f * (wb.x + wbn.x), 0.5f * (wb.y - wbn.y)};
      const c2 H0 = cmul(F0, W0), H5 = cmul(F5, W5);
      v[vi] = add_i(H0, H5);
    }
  }
  if (col == 0) __syncthreads();
  dft4<1>(v[0], v[1], v[2], v[3]);
  dft4<1>(v[4], v[5], v[6], v[7]);
  {
    const c2 wa = cexp2pi<1>((float)dd * (1.0f / 32.0f)), wb = cexp2pi<1>((float)(dd + 4) * (1.0f / 32.0f));
    const c2 wa2 = cmul(wa, wa), wa3 = cmul(wa2, wa), wb2 = cmul(wb, wb), wb3 = cmul(wb2, wb);
    v[1] = cmul(v[1], wa); v[2] = cmul(v[2], wa2); v[3] = cmul(v[3], wa3);
    v[5] = cmul(v[5], wb); v[6] = cmul(v[6], wb2); v[7] = cmul(v[7], wb3);
  }
#pragma unroll
  for (int d = 0; d < 4; ++d) { ex[(4 * dd + d) * 66 + l6] = v[d]; ex[(4 * (dd + 4) + d) * 66 + l6] = v[4 + d]; }
  __syncthreads();
#pragma unroll
  for (int k2 = 0; k2 < 8; ++k2) v[k2] = ex[(4 * k2 + d2) * 66 + 8 * m3 + h5];
  __syncthreads();
  dft8<1>(v);
  twiddle8b(v, cexp2pi<1>((float)(m3 * d2) * (1.0f / 256.0f)), cexp2pi<1>((float)m3 * (1.0f / 64.0f)));
#pragma unroll
  for (int c = 0; c < 8; ++c) ex[288 * h5 + 36 * m3 + 4 * c + d2] = v[c];
  WAVE_SYNC();
#pragma unroll
  for (int k1 = 0; k1 < 8; ++k1) v[k1] = ex[288 * h5 + 36 * k1 + l5];
  __syncthreads();
  dft8<1>(v);
  twiddle8b(v, cexp2pi<1>((float)(h5 * l5) * (1.0f / 2048.0f)), cexp2pi<1>((float)h5 * (1.0f / 64.0f)));
#pragma unroll
  for (int b = 0; b < 8; ++b) ex[256 * h5 + 32 * b + l5] = v[b];
  __syncthreads();
#pragma unroll
  for (int k0 = 0; k0 < 8; ++k0) v[k0] = ex[256 * k0 + t];
  dft8<1>(v);
#pragma unroll
  for (int a = 0; a < 8; ++a) st_nt(Tg + (size_t)a * (32 * TBS), v[a]);
  KEEP(pfv);
}

__device__ __forceinline__ void k3_body(char* smem, const c2* __restrict__ T, const u16* __restrict__ x,
                                        u16* __restrict__ h, u16* __restrict__ a_out, const char* __restrict__ pf) {
  c2* ex_all = reinterpret_cast<c2*>(smem);
  c2* st = reinterpret_cast<c2*>(smem) + 8 * 576;
  const int tid = threadIdx.x, lane = tid & 63, w = tid >> 6;
  const int s0 = blockIdx.x * 8;
  const int row = s0 + w;
  float4 q4[4];
#pragma unroll
  for (int it = 0; it < 4; ++it) {
    const int idx = it * 512 + tid;
    q4[it] = reinterpret_cast<const float4*>(T + (size_t)blockIdx.x * TBS)[idx];
  }
  const uint32_t* xr = reinterpret_cast<const uint32_t*>(x + (size_t)row * D_);
  uint32_t xv[8];
#pragma unroll
  for (int a = 0; a < 8; ++a) xv[a] = xr[lane + 64 * a];
  uint32_t pfv = 0;
  if (tid < 224) pfv = *reinterpret_cast<const uint32_t*>(pf + ((size_t)blockIdx.x * 224 + tid) * 128);
#pragma unroll
  for (int it = 0; it < 4; ++it) {
    const int idx = it * 512 + tid;
    const int k = idx >> 2, part = idx & 3;
    st[k * 9 + 2 * part] = (c2){q4[it].x, q4[it].y};
    st[k * 9 + 2 * part + 1] = (c2){q4[it].z, q4[it].w};
  }
  __syncthreads();
  c2 v[8];
  const c2 tw0 = cexp2pi<1>((float)lane * (1.0f / 1024.0f));
#pragma unroll
  for (int j = 0; j < 8; ++j) {
    const int k = lane + 64 * j;
    const c2 g = st[k * 9 + w], gp = st[((512 - k) & 511) * 9 + w];
    const float cs[8] = {1.f, 0.92387953251128674f, 0.70710678118654752f, 0.38268343236508977f, 0.f, -0.38268343236508977f, -0.70710678118654752f, -0.92387953251128674f};
    const float sn[8] = {0.f, 0.38268343236508977f, 0.70710678118654752f, 0.92387953251128674f, 1.f, 0.92387953251128674f, 0.70710678118654752f, 0.38268343236508977f};
    const c2 tw = j == 0 ? tw0 : cmul(tw0, (c2){cs[j], sn[j]});
    c2 Z = add_i(add_c(g, gp), cmul(tw, sub_c(g, gp)));
    if (k == 0) Z = (c2){g.x + g.y, g.x - g.y};
    v[j] = Z;
  }
  c2* ex = ex_all + w * 576;
  fft512_wave<1>(v, ex, lane);
  const float scale = 1.0f / (2048.0f * 1024.0f);
  uint32_t* tr = reinterpret_cast<uint32_t*>(ex);
  const auto hrs = __builtin_amdgcn_make_buffer_rsrc(h, 0, S_ * D_ * 2, 0x00020000);
  const auto ars = __builtin_amdgcn_make_buffer_rsrc(a_out, 0, S_ * D_ * 2, 0x00020000);
  const uint32_t roff = (uint32_t)row * (D_ * 2) + lane * 16;
#pragma unroll
  for (int j = 0; j < 8; ++j) {
    const c2 hh = unpk(xv[j]) + rbf2(v[j] * scale);
    const uint32_t hu = cvt_pk(hh.x, hh.y);
    tr[lane + 64 * j] = hu;
    v[j] = unpk(hu);
  }
  WAVE_SYNC();
  {
    const u32v4 h0 = reinterpret_cast<const u32v4*>(tr)[lane], h1 = reinterpret_cast<const u32v4*>(tr)[64 + lane];
    __builtin_amdgcn_raw_buffer_store_b128(h0, hrs, roff, 0, 16);
    __builtin_amdgcn_raw_buffer_store_b128(h1, hrs, roff + 1024, 0, 16);
  }
  ln_bf16(v, xv);
#pragma unroll
  for (int j = 0; j < 8; ++j) tr[512 + lane + 64 * j] = xv[j];
  WAVE_SYNC();
  {
    const u32v4 a0 = reinterpret_cast<const u32v4*>(tr)[128 + lane], a1 = reinterpret_cast<const u32v4*>(tr)[192 + lane];
    __builtin_amdgcn_raw_buffer_store_b128(a0, ars, roff, 0, 16);
    __builtin_amdgcn_raw_buffer_store_b128(a1, ars, roff + 1024, 0, 16);
  }
  KEEP(pfv);
}

#define WAIT_V(n) asm volatile("s_waitcnt vmcnt(" #n ")" ::: "memory")
#define WAIT_L0() asm volatile("s_waitcnt lgkmcnt(0)" ::: "memory")
#define RAW_BAR() __builtin_amdgcn_s_barrier()

__device__ __forceinline__ uint32_t mish_pair(float c0, float c1, f32x2 b) {
  f32x2 x = unpk(cvt_pk(c0, c1)) + b;
  const f32x2 hh = unpk(cvt_pk(x[0], x[1]));
  f32x2 z = hh * 1.44269504088896340736f;
  f32x2 t = {__builtin_amdgcn_exp2f(__builtin_amdgcn_fmed3f(z[0], -200.0f, 43.0f)), __builtin_amdgcn_exp2f(__builtin_amdgcn_fmed3f(z[1], -200.0f, 43.0f))};
  const f32x2 n = t * (t + 2.0f);
  const f32x2 d = n + 2.0f;
  const f32x2 r = {__builtin_amdgcn_rcpf(d[0]), __builtin_amdgcn_rcpf(d[1])};
  const f32x2 o = (hh * n) * r;
  return cvt_pk(o[0], o[1]);
}

typedef __attribute__((ext_vector_type(4))) unsigned int u32x4;
typedef __attribute__((ext_vector_type(2))) unsigned int u32x2;
template <int OFF> __device__ __forceinline__ void lds_rd128(u32x4& d, uint32_t a) { asm volatile("ds_read_b128 %0, %1 offset:%2" : "=v"(d) : "v"(a), "i"(OFF)); }
template <int OFF> __device__ __forceinline__ void lds_rdtr(u32x2& d, uint32_t a) { asm volatile("ds_read_b64_tr_b16 %0, %1 offset:%2" : "=v"(d) : "v"(a), "i"(OFF)); }
__device__ __forceinline__ bf16x8 mk8(u32x2 lo, u32x2 hi) { u32x4 v = {lo[0], lo[1], hi[0], hi[1]}; return __builtin_bit_cast(bf16x8, v); }
#define SCHED0() __builtin_amdgcn_sched_barrier(0)
__device__ __forceinline__ void mfma16(f32x4& c, const u32x2& alo, const u32x2& ahi, const u32x4& b) {
  u32x4 a = {alo[0], alo[1], ahi[0], ahi[1]};
  asm volatile("v_mfma_f32_16x16x32_bf16 %0, %1, %2, %0" : "+v"(c) : "v"(a), "v"(b));
}
typedef __attribute__((address_space(1))) unsigned int gu32;
template <int N> struct IC { static constexpr int v = N; };

constexpr int G1_A_BYTES = 128 * 128, G1_B_BYTES = 64 * 448, G1_STAGE = G1_A_BYTES + G1_B_BYTES;
constexpr int G1_OT = 57344, G1_OTS = 464;
struct G1Frag { u32x4 a[4]; u32x2 b[7][2]; };
__device__ __forceinline__ void g1_body(char* smem, const u16* __restrict__ A, const u16* __restrict__ W, const u16* __restrict__ b1,
                                        u16* __restrict__ act, unsigned* __restrict__ flags) {
  const int tid = threadIdx.x, lane = tid & 63, w8 = __builtin_amdgcn_readfirstlane(tid >> 6), w = w8 & 3;
  const int wm = w >> 1, wn = w & 1;
  const int bid = blockIdx.x, xcd = bid & 7, idx = bid >> 3;
  const int tm = (xcd & 1) * 8 + (idx & 7), tn = (xcd >> 1) * 4 + (idx >> 3);
  const int m0 = tm * 128, n0 = tn * 224;
  uint32_t aoff[4], boff[7];
#pragma unroll
  for (int i = 0; i < 4; ++i) {
    const int r = w * 32 + i * 8 + (lane >> 3);
    aoff[i] = (uint32_t)(m0 + r) * 2048u + (uint32_t)(((lane & 7) ^ (r & 7)) << 4);
  }
#pragma unroll
  for (int i = 0; i < 7; ++i) {
    const int d = i * 64 + lane, r = d / 28, q = d - r * 28, k = w * 16 + r;
    const int c = (q >> 1) ^ ((k >> 3) & 1);
    boff[i] = (uint32_t)k * (uint32_t)(F_ * 2) + (uint32_t)(n0 * 2) + (uint32_t)(((c << 1) | (q & 1)) << 4);
  }
  const char* Ab = reinterpret_cast<const char*>(A);
  const char* Wb = reinterpret_cast<const char*>(W);
  auto issue1 = [&](int i, int kt, int buf) {
    if (i < 4) {
      __builtin_amdgcn_global_load_lds((const unsigned*)(Ab + kt * 128 + aoff[i]),
                                       (unsigned*)(smem + buf * G1_STAGE + w * 4096 + lane * 16 + i * 1024), 16, 0, 16);
    } else {
      __builtin_amdgcn_global_load_lds((const unsigned*)(Wb + (size_t)kt * (64 * F_ * 2) + boff[i - 4]),
                                       (unsigned*)(smem + buf * G1_STAGE + G1_A_BYTES + w * 7168 + lane * 16 + (i - 4) * 1024), 16, 0, 0);
    }
  };
  const auto ars = __builtin_amdgcn_make_buffer_rsrc(act, 0, S_ * F_ * 2, 0x00020000);
  auto g1_store = [&](auto RND) {
    constexpr int rnd = decltype(RND)::v, CPH = rnd == 0 ? 8 : 6, NIT = rnd == 0 ? 4 : 3;
    if constexpr (rnd == 1) { WAIT_V(0); }
    WAIT_L0();
    RAW_BAR();
    if constexpr (rnd == 1) {
      if (tid == 0) __hip_atomic_fetch_add((gu32*)(flags + 4096 + (tm * 2 + (tn >> 3)) * 16), 1u, __ATOMIC_RELAXED, __HIP_MEMORY_SCOPE_AGENT);
    }
    if (rnd == 1 && w8 >= 4) return;
#pragma unroll
    for (int i = 0; i < (rnd == 0 ? NIT : 2 * NIT); ++i) {
      const int c = rnd == 0 ? tid + 512 * i : tid + 256 * i, r = c / (2 * CPH), q0 = c - r * (2 * CPH);
      const int q = (q0 >= CPH ? 14 - CPH : 0) + q0 + rnd * 8;
      const u32v4 v = *reinterpret_cast<const u32v4*>(smem + G1_OT + r * G1_OTS + q * 16);
      const int dst = (tn >> 3) * 3584 + (rnd == 0 ? (tn & 7) * 256 : 2048 + (tn & 7) * 192) + q0 * 16;
      __builtin_amdgcn_raw_buffer_store_b128(v, ars, (uint32_t)(m0 + r) * (uint32_t)(F_ * 2) + (uint32_t)dst, 0, 16);
    }
  };
  constexpr int NK = 16;
  if (w8 >= 4) {
#pragma unroll
    for (int i = 4; i < 11; ++i) issue1(i, 0, 0);
#pragma unroll
    for (int i = 4; i < 11; ++i) issue1(i, 1, 1);
    if (lane == 0) {
      for (int spin = 0; spin < (1 << 22); ++spin) {
        if (__hip_atomic_load((gu32*)(flags + 8192 + tm * 16), __ATOMIC_RELAXED, __HIP_MEMORY_SCOPE_AGENT) == 16u) break;
        __builtin_amdgcn_s_sleep(1);
      }
    }
    asm volatile("" ::: "memory");
#pragma unroll
    for (int i = 0; i < 4; ++i) issue1(i, 0, 0);
#pragma unroll
    for (int i = 0; i < 4; ++i) issue1(i, 1, 1);
    WAIT_V(4); RAW_BAR();
#pragma unroll
    for (int i = 0; i < 11; ++i) issue1(i, 2, 2);
    int buf = 0;
    for (int kt = 0; kt < NK - 3; ++kt) {
      WAIT_V(11); RAW_BAR();
#pragma unroll
      for (int i = 0; i < 11; ++i) issue1(i, kt + 3, buf);
      buf = buf == 2 ? 0 : buf + 1;
    }
    WAIT_V(11); RAW_BAR();
    WAIT_V(0); RAW_BAR();
    uint2 bb[7];
#pragma unroll
    for (int ni = 0; ni < 7; ++ni) bb[ni] = *reinterpret_cast<const uint2*>(b1 + n0 + wn * 112 + ni * 16 + 4 * (lane >> 4));
    RAW_BAR(); RAW_BAR();
    const f32x4* xfer = reinterpret_cast<const f32x4*>(smem) + w * (14 * 64) + lane;
    char* ot = smem + G1_OT + (wm * 64 + (lane & 15)) * G1_OTS + (wn * 112 + 4 * (lane >> 4)) * 2;
#pragma unroll
    for (int ni = 0; ni < 7; ++ni) {
      const f32x2 b01 = unpk(bb[ni].x), b23 = unpk(bb[ni].y);
#pragma unroll
      for (int mi = 2; mi < 4; ++mi) {
        const f32x4 c = xfer[((mi - 2) * 7 + ni) * 64];
        *reinterpret_cast<uint2*>(ot + mi * 16 * G1_OTS + ni * 32) = make_uint2(mish_pair(c[0], c[1], b01), mish_pair(c[2], c[3], b23));
      }
      if (ni == 3) g1_store(IC<0>{});
    }
    g1_store(IC<1>{});
    return;
  }
  uint2 bb[7];
#pragma unroll
  for (int ni = 0; ni < 7; ++ni) bb[ni] = *reinterpret_cast<const uint2*>(b1 + n0 + wn * 112 + ni * 16 + 4 * (lane >> 4));
  f32x4 acc[4][7];
#pragma unroll
  for (int i = 0; i < 4; ++i)
#pragma unroll
    for (int j = 0; j < 7; ++j) acc[i][j] = (f32x4){0.f, 0.f, 0.f, 0.f};
  const int fr = lane & 15, fg = lane >> 4;
  const uint32_t sbase = (uint32_t)(size_t)(smem);
  const uint32_t ard0 = sbase + (wm * 64 + fr) * 128 + ((fg ^ (fr & 7)) << 4);
  const int q4 = (lane & 15) >> 2, p4 = lane & 3;
  const uint32_t brd = sbase + G1_A_BYTES + (8 * fg + q4) * 448 + 8 * p4 + wn * 224;
  const int f32_ = (fg & 1) * 32;
  const uint32_t brdE = brd + ((wn & 1) ? -f32_ : f32_), brdO = brd + ((wn & 1) ? f32_ : -f32_);
  G1Frag X, Y;
  auto rd = [&](G1Frag& Fr, auto I, auto KS, uint32_t so) {
    constexpr int i = decltype(I)::v, ks = decltype(KS)::v;
    if constexpr (i < 4) {
      lds_rd128<i * 2048>(Fr.a[i], (ard0 ^ (ks * 64)) + so);
    } else {
      constexpr int ni = (i - 4) >> 1, t = (i - 4) & 1;
      lds_rdtr<ks * 32 * 448 + ni * 32 + t * 4 * 448>(Fr.b[ni][t], ((ni & 1) ? brdO : brdE) + so);
    }
  };
  auto mma1 = [&](G1Frag& Fr, auto J) {
    constexpr int j = decltype(J)::v, mi = j / 7, ni = j % 7;
    mfma16(acc[mi][ni], Fr.b[ni][0], Fr.b[ni][1], Fr.a[mi]);
  };
#define G1_TIE(Fr) asm volatile("s_waitcnt lgkmcnt(0)" : "+v"(Fr.a[0]), "+v"(Fr.a[1]), "+v"(Fr.a[2]), "+v"(Fr.a[3]), \
    "+v"(Fr.b[0][0]), "+v"(Fr.b[0][1]), "+v"(Fr.b[1][0]), "+v"(Fr.b[1][1]), "+v"(Fr.b[2][0]), "+v"(Fr.b[2][1]), "+v"(Fr.b[3][0]), "+v"(Fr.b[3][1]), \
    "+v"(Fr.b[4][0]), "+v"(Fr.b[4][1]), "+v"(Fr.b[5][0]), "+v"(Fr.b[5][1]), "+v"(Fr.b[6][0]), "+v"(Fr.b[6][1]) :: "memory")
  auto block = [&](G1Frag& C, G1Frag& R, auto KS, uint32_t so, auto DO_RD, auto DO_IS, int kt_is, int buf_is) {
    auto one = [&](auto J) {
      constexpr int j = decltype(J)::v;
      mma1(C, J);
      if constexpr (decltype(DO_RD)::v && j < 18) rd(R, J, KS, so);
      if constexpr (decltype(DO_IS)::v && j >= 6 && j < 28 && ((j - 6) & 1) == 0) issue1((j - 6) >> 1, kt_is, buf_is);
      SCHED0();
    };
    one(IC<0>{}); one(IC<1>{}); one(IC<2>{}); one(IC<3>{}); one(IC<4>{}); one(IC<5>{}); one(IC<6>{});
    one(IC<7>{}); one(IC<8>{}); one(IC<9>{}); one(IC<10>{}); one(IC<11>{}); one(IC<12>{}); one(IC<13>{});
    one(IC<14>{}); one(IC<15>{}); one(IC<16>{}); one(IC<17>{}); one(IC<18>{}); one(IC<19>{}); one(IC<20>{});
    one(IC<21>{}); one(IC<22>{}); one(IC<23>{}); one(IC<24>{}); one(IC<25>{}); one(IC<26>{}); one(IC<27>{});
  };
  auto rdall = [&](G1Frag& R, uint32_t so) {
    rd(R, IC<0>{}, IC<0>{}, so); rd(R, IC<1>{}, IC<0>{}, so); rd(R, IC<2>{}, IC<0>{}, so); rd(R, IC<3>{}, IC<0>{}, so);
    rd(R, IC<4>{}, IC<0>{}, so); rd(R, IC<5>{}, IC<0>{}, so); rd(R, IC<6>{}, IC<0>{}, so); rd(R, IC<7>{}, IC<0>{}, so);
    rd(R, IC<8>{}, IC<0>{}, so); rd(R, IC<9>{}, IC<0>{}, so); rd(R, IC<10>{}, IC<0>{}, so); rd(R, IC<11>{}, IC<0>{}, so);
    rd(R, IC<12>{}, IC<0>{}, so); rd(R, IC<13>{}, IC<0>{}, so); rd(R, IC<14>{}, IC<0>{}, so); rd(R, IC<15>{}, IC<0>{}, so);
    rd(R, IC<16>{}, IC<0>{}, so); rd(R, IC<17>{}, IC<0>{}, so);
  };
  __builtin_amdgcn_s_setprio(2);
  RAW_BAR();
  rdall(X, 0);
  int buf = 0;
  for (int kt = 0; kt < NK - 3; ++kt) {
    const int nbuf = buf == 2 ? 0 : buf + 1;
    G1_TIE(X); SCHED0();
    block(X, Y, IC<1>{}, buf * G1_STAGE, IC<1>{}, IC<0>{}, 0, 0);
    G1_TIE(Y); RAW_BAR(); SCHED0();
    block(Y, X, IC<0>{}, nbuf * G1_STAGE, IC<1>{}, IC<0>{}, 0, 0);
    buf = nbuf;
  }
  G1_TIE(X); SCHED0();
  block(X, Y, IC<1>{}, 1 * G1_STAGE, IC<1>{}, IC<0>{}, 0, 0);
  G1_TIE(Y); RAW_BAR(); SCHED0();
  block(Y, X, IC<0>{}, 2 * G1_STAGE, IC<1>{}, IC<0>{}, 0, 0);
  G1_TIE(X); SCHED0();
  block(X, Y, IC<1>{}, 2 * G1_STAGE, IC<1>{}, IC<0>{}, 0, 0);
  G1_TIE(Y); RAW_BAR(); SCHED0();
  block(Y, X, IC<0>{}, 0 * G1_STAGE, IC<1>{}, IC<0>{}, 0, 0);
  G1_TIE(X); SCHED0();
  block(X, Y, IC<1>{}, 0 * G1_STAGE, IC<1>{}, IC<0>{}, 0, 0);
  G1_TIE(Y); SCHED0();
  block(Y, X, IC<0>{}, 0, IC<0>{}, IC<0>{}, 0, 0);
  asm volatile("s_nop 15\n\ts_nop 15" : "+v"(acc[0][0]), "+v"(acc[0][1]), "+v"(acc[0][2]), "+v"(acc[0][3]), "+v"(acc[0][4]), "+v"(acc[0][5]), "+v"(acc[0][6]), "+v"(acc[1][0]), "+v"(acc[1][1]), "+v"(acc[1][2]), "+v"(acc[1][3]), "+v"(acc[1][4]), "+v"(acc[1][5]), "+v"(acc[1][6]), "+v"(acc[2][0]), "+v"(acc[2][1]), "+v"(acc[2][2]), "+v"(acc[2][3]), "+v"(acc[2][4]), "+v"(acc[2][5]), "+v"(acc[2][6]), "+v"(acc[3][0]), "+v"(acc[3][1]), "+v"(acc[3][2]), "+v"(acc[3][3]), "+v"(acc[3][4]), "+v"(acc[3][5]), "+v"(acc[3][6]) :: "memory");
  SCHED0();
  __builtin_amdgcn_s_setprio(0);
  RAW_BAR();
  {
    f32x4* xfer = reinterpret_cast<f32x4*>(smem) + w * (14 * 64) + lane;
#pragma unroll
    for (int mi = 2; mi < 4; ++mi)
#pragma unroll
      for (int ni = 0; ni < 7; ++ni) xfer[((mi - 2) * 7 + ni) * 64] = acc[mi][ni];
  }
  WAIT_L0();
  RAW_BAR();
  char* ot = smem + G1_OT + (wm * 64 + fr) * G1_OTS + (wn * 112 + 4 * fg) * 2;
#pragma unroll
  for (int ni = 0; ni < 7; ++ni) {
    const f32x2 b01 = unpk(bb[ni].x), b23 = unpk(bb[ni].y);
#pragma unroll
    for (int mi = 0; mi < 2; ++mi) {
      const f32x4 c = acc[mi][ni];
      *reinterpret_cast<uint2*>(ot + mi * 16 * G1_OTS + ni * 32) = make_uint2(mish_pair(c[0], c[1], b01), mish_pair(c[2], c[3], b23));
    }
    if (ni == 3) g1_store(IC<0>{});
  }
  g1_store(IC<1>{});
}

constexpr int G2_A_BYTES = 128 * 128, G2_B_BYTES = 64 * 256, G2_STAGE = G2_A_BYTES + G2_B_BYTES;
struct G2Frag { u32x4 a[4]; u32x2 b[4][2]; };
__device__ __forceinline__ void g2_body(char* smem, const u16* __restrict__ A, const u16* __restrict__ W, const u16* __restrict__ h,
                                        u16* __restrict__ out, float* __restrict__ part, unsigned* __restrict__ flags,
                                        unsigned* __restrict__ sigB) {
  const int tid = threadIdx.x, lane = tid & 63, w8 = __builtin_amdgcn_readfirstlane(tid >> 6), w = w8 & 3;
  const int wm = w >> 1, wn = w & 1;
  const int bid = blockIdx.x, xcd = bid & 7, idx = bid >> 3;
  const int kh = xcd & 1, tm = ((xcd >> 1) & 1) * 8 + (idx & 7), tn = (xcd >> 2) * 4 + (idx >> 3);
  const int m0 = tm * 128, n0 = tn * 128, tile = tm * 8 + tn;
  auto g2_store = [&]() {
    WAIT_L0();
    RAW_BAR();
#pragma unroll
    for (int i = 0; i < 2; ++i) {
      const int c = tid + 512 * i, r = c >> 4, q = c & 15;
      const int row = (r >> 5) * 64 + kh * 32 + (r & 31);
      const uint4 v = *reinterpret_cast<const uint4*>(smem + r * 272 + q * 16);
      *reinterpret_cast<uint4*>(reinterpret_cast<char*>(out) + (size_t)(m0 + row) * (D_ * 2) + n0 * 2 + q * 16) = v;
    }
  };
  constexpr int NK = 28;
  if (w8 >= 4) {
    auto col8 = [](int st, int c) -> int {
      if (st < 16) return 28 * (st >> 1) + 14 * (st & 1) + c;
      const int v8 = 8 * (st - 16) + c, j = (v8 * 171) >> 11, r8 = v8 - 12 * j;
      return 28 * j + 8 + r8 + (r8 >= 6 ? 8 : 0);
    };
    const int ca = (lane & 7) ^ ((lane >> 3) & 7);
    uint32_t arow[4], bcol[4];
#pragma unroll
    for (int i = 0; i < 4; ++i) arow[i] = (uint32_t)(m0 + w * 32 + i * 8 + (lane >> 3)) * (uint32_t)(F_ * 2) + (uint32_t)(ca * 16);
#pragma unroll
    for (int i = 0; i < 4; ++i) {
      const int k = w * 16 + i * 4 + (lane >> 4), q = lane & 15;
      const int c = (q >> 1) ^ ((k & 3) | (((k >> 3) & 1) << 2));
      bcol[i] = (uint32_t)(k & 7) * (uint32_t)(D_ * 2) + (uint32_t)(n0 * 2) + (uint32_t)(((c << 1) | (q & 1)) << 4);
    }
    const char* Ab = reinterpret_cast<const char*>(A) + kh * (1792 * 2);
    const char* Wb = reinterpret_cast<const char*>(W) + (size_t)kh * (1792 * D_ * 2);
    auto issueA = [&](int kt, int buf) {
#pragma unroll
      for (int i = 0; i < 4; ++i)
        __builtin_amdgcn_global_load_lds((const unsigned*)(Ab + kt * 128 + arow[i]),
                                         (unsigned*)(smem + buf * G2_STAGE + w * 4096 + lane * 16 + i * 1024), 16, 0, 16);
    };
    auto issueB = [&](int kt, int buf) {
#pragma unroll
      for (int i = 0; i < 4; ++i) {
        const uint32_t ro = (uint32_t)col8(kt, 2 * w + (i >> 1)) * (uint32_t)(8 * D_ * 2);
        __builtin_amdgcn_global_load_lds((const unsigned*)(Wb + (ro + bcol[i])),
                                         (unsigned*)(smem + buf * G2_STAGE + G2_A_BYTES + w * 4096 + lane * 16 + i * 1024), 16, 0, 0);
      }
    };
    auto issue = [&](int kt, int buf) { issueA(kt, buf); issueB(kt, buf); };
    auto wait_cnt = [&](unsigned* cnt, unsigned target) {
      if (lane == 0) {
        for (int spin = 0; spin < (1 << 22); ++spin) {
          if (__hip_atomic_load((gu32*)cnt, __ATOMIC_RELAXED, __HIP_MEMORY_SCOPE_AGENT) == target) break;
          __builtin_amdgcn_s_sleep(1);
        }
      }
      asm volatile("" ::: "memory");
    };
    issueB(0, 0); issueB(1, 1);
    wait_cnt(flags + 4096 + (tm * 2 + kh) * 16, 8u);
    issueA(0, 0); issueA(1, 1);
    WAIT_V(4);
    RAW_BAR();
    issue(2, 2); issue(3, 3);
    for (int kt = 0; kt < NK - 4; ++kt) {
      WAIT_V(16); RAW_BAR();
      if (kt == 12) wait_cnt(flags + 12288 + (tm * 2 + kh) * 16, 32u);
      issue(kt + 4, kt & 3);
    }
    WAIT_V(16); RAW_BAR();
    WAIT_V(8); RAW_BAR();
    WAIT_V(0); RAW_BAR();
    RAW_BAR();
    RAW_BAR();
    g2_store();
    return;
  }
  const int fr = lane & 15, fg = lane >> 4;
  u32x2 hv[2][4];
  f32x4 acc[4][4];
#pragma unroll
  for (int i = 0; i < 4; ++i)
#pragma unroll
    for (int j = 0; j < 4; ++j) acc[i][j] = (f32x4){0.f, 0.f, 0.f, 0.f};
  const uint32_t sbase = (uint32_t)(size_t)(smem);
  const uint32_t ard0 = sbase + (wm * 64 + fr) * 128 + ((fg ^ (fr & 7)) << 4);
  const int q4 = (lane & 15) >> 2, p4 = lane & 3;
  const int bsw = q4 | ((fg & 1) << 2);
  const uint32_t brd = sbase + G2_A_BYTES + (8 * fg + q4) * 256 + 8 * p4;
  uint32_t brdn[4];
#pragma unroll
  for (int ni = 0; ni < 4; ++ni) brdn[ni] = brd + (((wn * 4 + ni) ^ bsw) * 32);
  G2Frag X, Y;
  auto rd = [&](G2Frag& Fr, auto I, auto KS, uint32_t so) {
    constexpr int i = decltype(I)::v, ks = decltype(KS)::v;
    if constexpr (i < 4) {
      lds_rd128<i * 2048>(Fr.a[i], (ard0 ^ (ks * 64)) + so);
    } else {
      constexpr int ni = (i - 4) >> 1, t = (i - 4) & 1;
      lds_rdtr<ks * 32 * 256 + t * 4 * 256>(Fr.b[ni][t], brdn[ni] + so);
    }
  };
  auto mma1 = [&](G2Frag& Fr, auto J) {
    constexpr int j = decltype(J)::v, mi = j / 4, ni = j % 4;
    mfma16(acc[mi][ni], Fr.b[ni][0], Fr.b[ni][1], Fr.a[mi]);
  };
#define G2_TIE(Fr) asm volatile("s_waitcnt lgkmcnt(0)" : "+v"(Fr.a[0]), "+v"(Fr.a[1]), "+v"(Fr.a[2]), "+v"(Fr.a[3]), \
    "+v"(Fr.b[0][0]), "+v"(Fr.b[0][1]), "+v"(Fr.b[1][0]), "+v"(Fr.b[1][1]), "+v"(Fr.b[2][0]), "+v"(Fr.b[2][1]), "+v"(Fr.b[3][0]), "+v"(Fr.b[3][1]) :: "memory")
  auto block = [&](G2Frag& C, G2Frag& R, auto KS, uint32_t so, auto DO_RD) {
    auto one = [&](auto J) {
      constexpr int j = decltype(J)::v;
      mma1(C, J);
      if constexpr (decltype(DO_RD)::v && j < 12) rd(R, J, KS, so);
      SCHED0();
    };
    one(IC<0>{}); one(IC<1>{}); one(IC<2>{}); one(IC<3>{}); one(IC<4>{}); one(IC<5>{}); one(IC<6>{}); one(IC<7>{});
    one(IC<8>{}); one(IC<9>{}); one(IC<10>{}); one(IC<11>{}); one(IC<12>{}); one(IC<13>{}); one(IC<14>{}); one(IC<15>{});
  };
  __builtin_amdgcn_s_setprio(2);
  RAW_BAR();
  {
    const auto hrs = __builtin_amdgcn_make_buffer_rsrc(const_cast<u16*>(h), 0, S_ * D_ * 2, 0x00020000);
#pragma unroll
    for (int i = 0; i < 2; ++i)
#pragma unroll
      for (int ni = 0; ni < 4; ++ni)
        hv[i][ni] = __builtin_amdgcn_raw_buffer_load_b64(hrs, (uint32_t)((m0 + wm * 64 + (2 * kh + i) * 16 + fr) * D_ + n0 + wn * 64 + ni * 16 + 4 * fg) * 2u, 0, 16);
  }
  rd(X, IC<0>{}, IC<0>{}, 0); rd(X, IC<1>{}, IC<0>{}, 0); rd(X, IC<2>{}, IC<0>{}, 0); rd(X, IC<3>{}, IC<0>{}, 0);
  rd(X, IC<4>{}, IC<0>{}, 0); rd(X, IC<5>{}, IC<0>{}, 0); rd(X, IC<6>{}, IC<0>{}, 0); rd(X, IC<7>{}, IC<0>{}, 0);
  rd(X, IC<8>{}, IC<0>{}, 0); rd(X, IC<9>{}, IC<0>{}, 0); rd(X, IC<10>{}, IC<0>{}, 0); rd(X, IC<11>{}, IC<0>{}, 0);
  for (int kt = 0; kt < NK - 1; ++kt) {
    if (kt == 7) {
      WAIT_V(0);
      if (lane == 0) __hip_atomic_fetch_add((gu32*)sigB, 1u, __ATOMIC_RELAXED, __HIP_MEMORY_SCOPE_AGENT);
    }
    G2_TIE(X); SCHED0();
    block(X, Y, IC<1>{}, (kt & 3) * G2_STAGE, IC<1>{});
    G2_TIE(Y); RAW_BAR(); SCHED0();
    block(Y, X, IC<0>{}, ((kt + 1) & 3) * G2_STAGE, IC<1>{});
  }
  G2_TIE(X); SCHED0();
  block(X, Y, IC<1>{}, ((NK - 1) & 3) * G2_STAGE, IC<1>{});
  G2_TIE(Y); SCHED0();
  block(Y, X, IC<0>{}, 0, IC<0>{});
  asm volatile("s_nop 15\n\ts_nop 15" : "+v"(acc[0][0]), "+v"(acc[0][1]), "+v"(acc[0][2]), "+v"(acc[0][3]), "+v"(acc[1][0]), "+v"(acc[1][1]), "+v"(acc[1][2]), "+v"(acc[1][3]), "+v"(acc[2][0]), "+v"(acc[2][1]), "+v"(acc[2][2]), "+v"(acc[2][3]), "+v"(acc[3][0]), "+v"(acc[3][1]), "+v"(acc[3][2]), "+v"(acc[3][3]) :: "memory");
  SCHED0();
  const auto rs = __builtin_amdgcn_make_buffer_rsrc(part, 0, 256 * 32768, 0x00020000);
  const uint32_t myoff = (uint32_t)(tile * 2 + kh) * 32768u + (uint32_t)w * 8192u + (uint32_t)lane * 16u;
  const uint32_t proff = (uint32_t)(tile * 2 + (kh ^ 1)) * 32768u + (uint32_t)w * 8192u + (uint32_t)lane * 16u;
  auto send = [&](const f32x4 (&r0)[4], const f32x4 (&r1)[4]) {
#pragma unroll
    for (int ni = 0; ni < 4; ++ni) {
      __builtin_amdgcn_raw_buffer_store_b128(__builtin_bit_cast(u32x4, r0[ni]), rs, myoff + ni * 1024, 0, 16);
      __builtin_amdgcn_raw_buffer_store_b128(__builtin_bit_cast(u32x4, r1[ni]), rs, myoff + 4096 + ni * 1024, 0, 16);
    }
  };
  if (kh == 0) send(acc[2], acc[3]); else send(acc[0], acc[1]);
  WAIT_V(0);
  RAW_BAR();
  if (w == 0) {
    if (lane == 0) {
      __hip_atomic_store((gu32*)(flags + (tile * 2 + kh) * 16), 1u, __ATOMIC_RELAXED, __HIP_MEMORY_SCOPE_AGENT);
      for (int spin = 0; spin < (1 << 22); ++spin) {
        if (__hip_atomic_load((gu32*)(flags + (tile * 2 + (kh ^ 1)) * 16), __ATOMIC_RELAXED, __HIP_MEMORY_SCOPE_AGENT) == 1u) break;
        __builtin_amdgcn_s_sleep(1);
      }
    }
  }
  asm volatile("" ::: "memory");
  RAW_BAR();
  asm volatile("" ::: "memory");
  auto fin = [&](auto I, const f32x4 (&own)[4]) {
    constexpr int i = decltype(I)::v;
    char* ot = smem + (wm * 32 + i * 16 + fr) * 272 + wn * 128 + 8 * fg;
    f32x4 pr[4];
#pragma unroll
    for (int ni = 0; ni < 4; ++ni) pr[ni] = __builtin_bit_cast(f32x4, __builtin_amdgcn_raw_buffer_load_b128(rs, proff + i * 4096 + ni * 1024, 0, 16));
#pragma unroll
    for (int ni = 0; ni < 4; ++ni) {
      const f32x4 c = kh == 0 ? own[ni] + pr[ni] : pr[ni] + own[ni];
      const f32x2 r01 = unpk(hv[i][ni][0]) + unpk(cvt_pk(c[0], c[1])), r23 = unpk(hv[i][ni][1]) + unpk(cvt_pk(c[2], c[3]));
      *reinterpret_cast<uint2*>(ot + ni * 32) = make_uint2(cvt_pk(r01[0], r01[1]), cvt_pk(r23[0], r23[1]));
    }
  };
  if (kh == 0) { fin(IC<0>{}, acc[0]); fin(IC<1>{}, acc[1]); }
  else { fin(IC<0>{}, acc[2]); fin(IC<1>{}, acc[3]); }
  g2_store();
}

__global__ __launch_bounds__(512, 2) void k3g_fused(const c2* __restrict__ U, const u16* __restrict__ x, u16* __restrict__ A,
                                                     const u16* __restrict__ W1, const u16* __restrict__ b1,
                                                     u16* __restrict__ act, const u16* __restrict__ W2, u16* __restrict__ h,
                                                     u16* __restrict__ out, float* __restrict__ part, unsigned* __restrict__ flags) {
  __shared__ __attribute__((aligned(16))) char smem[3 * G1_STAGE];
  uint32_t cpf = 0;
  if ((blockIdx.x >> 3) < 2 && threadIdx.x >= 320) {
    const char* pc = reinterpret_cast<const char*>(__builtin_amdgcn_s_getpc());
    cpf = *reinterpret_cast<const uint32_t*>(pc + 4096 + (threadIdx.x - 320) * 128);
  }
  k3_body(smem, U, x, h, A, reinterpret_cast<const char*>(W2));
  KEEP(cpf);
  WAIT_V(0);
  WAIT_L0();
  RAW_BAR();
  if (threadIdx.x == 0)
    __hip_atomic_fetch_add((gu32*)(flags + 8192 + (blockIdx.x >> 4) * 16), 1u, __ATOMIC_RELAXED, __HIP_MEMORY_SCOPE_AGENT);
  g1_body(smem, A, W1, b1, act, flags);
  WAIT_L0();
  RAW_BAR();
  {
    const int xcd = blockIdx.x & 7, idx = blockIdx.x >> 3;
    const int tm1 = (xcd & 1) * 8 + (idx & 7), tn1 = (xcd >> 1) * 4 + (idx >> 3);
    g2_body(smem, act, W2, h, out, part, flags, flags + 12288 + (tm1 * 2 + (tn1 >> 3)) * 16);
  }
}

extern "C" void kernel_launch(void* const* d_in, const int* in_sizes, int n_in,
                              void* d_out, int out_size, void* d_ws, size_t ws_size,
                              hipStream_t stream) {
  const u16* x = (const u16*)d_in[0];
  const u16* wr = (const u16*)d_in[1];
  const u16* wi = (const u16*)d_in[2];
  const u16* w1 = (const u16*)d_in[4];
  const u16* b1 = (const u16*)d_in[5];
  const u16* w2 = (const u16*)d_in[6];
  u16* out = (u16*)d_out;
  char* ws = (char*)d_ws;
  c2* T = (c2*)ws;
  uint32_t* WT = (uint32_t*)(ws + (9u << 20));
  u16* h = (u16*)(ws + (16u << 20));
  u16* a = (u16*)(ws + (20u << 20));
  u16* act = (u16*)(ws + (24u << 20));
  float* part = (float*)(ws + (40u << 20));
  unsigned* flags = (unsigned*)(ws + (48u << 20));
  k1_ln_rowfft<<<256, 512, 0, stream>>>(x, wr, wi, T, WT, flags);
  c2* U = (c2*)(ws + (50u << 20));
  k2_colfft<<<512, 256, 0, stream>>>(T, U, WT, (const char*)w1);
  k3g_fused<<<256, 512, 0, stream>>>(U, x, a, w1, b1, act, w2, h, out, part, flags);
}
```

```cpp
#include <hip/hip_runtime.h>
#include <hip/hip_bf16.h>
#include <stdint.h>

typedef unsigned short u16;
typedef __attribute__((ext_vector_type(4))) float f32x4;

constexpr int TBS = 4096 + 16;
constexpr int S_ = 2048, D_ = 1024, F_ = 3584, DF_ = 513;

__device__ __forceinline__ float bf2f(u16 v) { return __uint_as_float(((uint32_t)v) << 16); }
__device__ __forceinline__ u16 f2bf(float f) { __bf16 b = (__bf16)f; return __builtin_bit_cast(u16, b); }
__device__ __forceinline__ float rbf(float f) { return bf2f(f2bf(f)); }

typedef __attribute__((ext_vector_type(4))) unsigned int u32v4;
typedef __attribute__((ext_vector_type(4))) float f32v4;
template <typename T> __device__ __forceinline__ void st_nt(T* p, T v) { __builtin_nontemporal_store(v, p); }
typedef __attribute__((ext_vector_type(2))) float c2;
typedef c2 f32x2;
__device__ __forceinline__ uint32_t cvt_pk(float lo, float hi) { uint32_t r; asm("v_cvt_pk_bf16_f32 %0, %1, %2" : "=v"(r) : "v"(lo), "v"(hi)); return r; }
__device__ __forceinline__ c2 unpk(uint32_t u) { return (c2){__uint_as_float(u << 16), __uint_as_float(u & 0xffff0000u)}; }
__device__ __forceinline__ c2 rbf2(c2 v) { return unpk(cvt_pk(v.x, v.y)); }
__device__ __forceinline__ c2 cmul(c2 a, c2 b) {
  c2 t, r;
  asm("v_pk_mul_f32 %0, %1, %2 op_sel:[0,0] op_sel_hi:[0,1]" : "=v"(t) : "v"(a), "v"(b));
  asm("v_pk_fma_f32 %0, %1, %2, %3 op_sel:[1,1,0] op_sel_hi:[1,0,1] neg_lo:[0,1,0]" : "=v"(r) : "v"(a), "v"(b), "v"(t));
  return r;
}
__device__ __forceinline__ c2 sub_i(c2 t, c2 d) { c2 r; asm("v_pk_add_f32 %0, %1, %2 op_sel:[0,1] op_sel_hi:[1,0] neg_hi:[0,1]" : "=v"(r) : "v"(t), "v"(d)); return r; }
__device__ __forceinline__ c2 add_i(c2 t, c2 d) { c2 r; asm("v_pk_add_f32 %0, %1, %2 op_sel:[0,1] op_sel_hi:[1,0] neg_lo:[0,1]" : "=v"(r) : "v"(t), "v"(d)); return r; }
__device__ __forceinline__ c2 add_c(c2 a, c2 b) { c2 r; asm("v_pk_add_f32 %0, %1, %2 neg_hi:[0,1]" : "=v"(r) : "v"(a), "v"(b)); return r; }
__device__ __forceinline__ c2 sub_c(c2 a, c2 b) { c2 r; asm("v_pk_add_f32 %0, %1, %2 neg_lo:[0,1]" : "=v"(r) : "v"(a), "v"(b)); return r; }
template <int DIR> __device__ __forceinline__ c2 addD(c2 t, c2 d) { return DIR > 0 ? add_i(t, d) : sub_i(t, d); }
template <int DIR> __device__ __forceinline__ c2 subD(c2 t, c2 d) { return DIR > 0 ? sub_i(t, d) : add_i(t, d); }
template <int DIR>
__device__ __forceinline__ c2 cexp2pi(float rev) {
  const float r = DIR > 0 ? rev : -rev;
  c2 w = {__builtin_amdgcn_cosf(r), __builtin_amdgcn_sinf(r)};
  asm("s_nop 0" : "+v"(w));
  return w;
}
template <int DIR>
__device__ __forceinline__ void dft4(c2& x0, c2& x1, c2& x2, c2& x3) {
  const c2 t0 = x0 + x2, t1 = x0 - x2, t2 = x1 + x3, d = x1 - x3;
  x0 = t0 + t2; x2 = t0 - t2; x1 = addD<DIR>(t1, d); x3 = subD<DIR>(t1, d);
}
template <int DIR>
__device__ __forceinline__ void dft8(c2 (&v)[8]) {
  c2 e0 = v[0], e1 = v[2], e2 = v[4], e3 = v[6];
  c2 o0 = v[1], o1 = v[3], o2 = v[5], o3 = v[7];
  dft4<DIR>(e0, e1, e2, e3);
  dft4<DIR>(o0, o1, o2, o3);
  const c2 r = {0.70710678118654752440f, 0.70710678118654752440f};
  const c2 u1 = addD<DIR>(o1, o1), u3 = subD<DIR>(o3, o3);
  v[0] = e0 + o0; v[4] = e0 - o0;
  v[1] = __builtin_elementwise_fma(u1, r, e1); v[5] = __builtin_elementwise_fma(u1, -r, e1);
  v[2] = addD<DIR>(e2, o2); v[6] = subD<DIR>(e2, o2);
  v[3] = __builtin_elementwise_fma(u3, -r, e3); v[7] = __builtin_elementwise_fma(u3, r, e3);
}
__device__ __forceinline__ void twiddle8(c2 (&v)[8], c2 w) {
  const c2 w2 = cmul(w, w), w3 = cmul(w2, w), w4 = cmul(w2, w2), w5 = cmul(w4, w), w6 = cmul(w3, w3), w7 = cmul(w4, w3);
  v[1] = cmul(v[1], w); v[2] = cmul(v[2], w2); v[3] = cmul(v[3], w3); v[4] = cmul(v[4], w4);
  v[5] = cmul(v[5], w5); v[6] = cmul(v[6], w6); v[7] = cmul(v[7], w7);
}
__device__ __forceinline__ void twiddle8b(c2 (&v)[8], c2 b, c2 w) {
  const c2 w2 = cmul(w, w), w4 = cmul(w2, w2);
  const c2 b1 = cmul(b, w), b2 = cmul(b, w2), b3 = cmul(b1, w2), b4 = cmul(b, w4), b5 = cmul(b1, w4), b6 = cmul(b2, w4), b7 = cmul(b3, w4);
  v[0] = cmul(v[0], b); v[1] = cmul(v[1], b1); v[2] = cmul(v[2], b2); v[3] = cmul(v[3], b3);
  v[4] = cmul(v[4], b4); v[5] = cmul(v[5], b5); v[6] = cmul(v[6], b6); v[7] = cmul(v[7], b7);
}

__device__ __forceinline__ float wave_sum(float v) {
#define DPP_ADD(ctrl, rmask) v += __uint_as_float(__builtin_amdgcn_update_dpp(0u, __float_as_uint(v), ctrl, rmask, 0xf, true))
  DPP_ADD(0x111, 0xf); DPP_ADD(0x112, 0xf); DPP_ADD(0x114, 0xf); DPP_ADD(0x118, 0xf);
  DPP_ADD(0x142, 0xa); DPP_ADD(0x143, 0xc);
#undef DPP_ADD
  return __uint_as_float(__builtin_amdgcn_readlane(__float_as_uint(v), 63));
}

#define WAVE_SYNC() do { __builtin_amdgcn_fence(__ATOMIC_RELEASE, "wavefront"); __builtin_amdgcn_wave_barrier(); __builtin_amdgcn_fence(__ATOMIC_ACQUIRE, "wavefront"); } while (0)

template <int DIR>
__device__ __forceinline__ void fft512_wave(c2 (&v)[8], c2* ex, int lane) {
  const int hi = lane >> 3, lo = lane & 7;
  dft8<DIR>(v);
  twiddle8(v, cexp2pi<DIR>((float)hi * (1.0f / 64.0f)));
#pragma unroll
  for (int k0 = 0; k0 < 8; ++k0) ex[72 * k0 + lane] = v[k0];
  WAVE_SYNC();
#pragma unroll
  for (int b = 0; b < 8; ++b) v[b] = ex[72 * hi + 8 * b + lo];
  WAVE_SYNC();
  dft8<DIR>(v);
  twiddle8b(v, cexp2pi<DIR>((float)(lo * hi) * (1.0f / 512.0f)), cexp2pi<DIR>((float)lo * (1.0f / 64.0f)));
#pragma unroll
  for (int k1 = 0; k1 < 8; ++k1) ex[66 * lo + 8 * k1 + hi] = v[k1];
  WAVE_SYNC();
#pragma unroll
  for (int c = 0; c < 8; ++c) v[c] = ex[66 * c + lane];
  WAVE_SYNC();
  dft8<DIR>(v);
}

__device__ __forceinline__ void ln_bf16(c2 (&e)[8], uint32_t (&o)[8]) {
  c2 s2 = ((e[0] + e[1]) + (e[2] + e[3])) + ((e[4] + e[5]) + (e[6] + e[7]));
  const float mean = wave_sum(s2.x + s2.y) * (1.0f / 1024.0f);
  c2 q2 = {0.f, 0.f};
#pragma unroll
  for (int i = 0; i < 8; ++i) { const c2 d = e[i] - mean; q2 = __builtin_elementwise_fma(d, d, q2); }
  const float var = wave_sum(q2.x + q2.y) * (1.0f / 1024.0f);
  const float mb = rbf(mean);
  const float sd = rbf(sqrtf(rbf(rbf(var) + 1e-5f)));
  const float ri = 1.0f / sd;
#pragma unroll
  for (int i = 0; i < 8; ++i) {
    const c2 d = rbf2(e[i] - mb);
    const c2 q = d * ri;
    const c2 rem = __builtin_elementwise_fma(-q, (c2){sd, sd}, d);
    const c2 qq = __builtin_elementwise_fma(rem, (c2){ri, ri}, q);
    o[i] = cvt_pk(qq.x, qq.y);
  }
}

__global__ __launch_bounds__(512) void k1_ln_rowfft(const u16* __restrict__ x, const u16* __restrict__ wr, const u16* __restrict__ wi,
                                                     c2* __restrict__ T, uint32_t* __restrict__ WT, unsigned* __restrict__ flags) {
  __shared__ c2 ex_all[8 * 576];
  __shared__ c2 st[512 * 9];
  const int tid = threadIdx.x, lane = tid & 63, w = tid >> 6;
  const int s0 = blockIdx.x * 8;
  const int row = s0 + w;
  const uint32_t* xr = reinterpret_cast<const uint32_t*>(x + (size_t)row * D_);
  uint32_t xu[8];
#pragma unroll
  for (int a = 0; a < 8; ++a) xu[a] = xr[lane + 64 * a];
  if (tid == 0) {
    flags[blockIdx.x * 16] = 0;
    if (blockIdx.x < 32) flags[4096 + blockIdx.x * 16] = 0;
    if (blockIdx.x < 16) flags[8192 + blockIdx.x * 16] = 0;
  }
  {
    uint32_t p[8];
#pragma unroll
    for (int r = 0; r < 8; ++r) {
      const size_t o = (size_t)(s0 + r) * DF_ + tid;
      p[r] = (uint32_t)wr[o] | ((uint32_t)wi[o] << 16);
    }
    u32v4* dst = reinterpret_cast<u32v4*>(WT + (size_t)tid * S_ + s0);
    st_nt(dst, (u32v4){p[0], p[1], p[2], p[3]});
    st_nt(dst + 1, (u32v4){p[4], p[5], p[6], p[7]});
    if (tid < 8) {
      const size_t o = (size_t)(s0 + tid) * DF_ + 512;
      WT[(size_t)512 * S_ + s0 + tid] = (uint32_t)wr[o] | ((uint32_t)wi[o] << 16);
    }
  }
  c2 v[8];
#pragma unroll
  for (int a = 0; a < 8; ++a) v[a] = unpk(xu[a]);
  ln_bf16(v, xu);
#pragma unroll
  for (int a = 0; a < 8; ++a) v[a] = unpk(xu[a]);
  c2* ex = ex_all + w * 576;
  fft512_wave<-1>(v, ex, lane);
#pragma unroll
  for (int j = 4; j < 8; ++j) ex[lane + 64 * j] = v[j];
  ex[512 + lane] = v[0];
  WAVE_SYNC();
  const c2 tw0 = cexp2pi<-1>((float)lane * (1.0f / 1024.0f));
  const c2 half = {0.5f, 0.5f};
#pragma unroll
  for (int j = 0; j < 4; ++j) {
    const int k = lane + 64 * j;
    const c2 z = v[j], zp = ex[512 - k];
    const c2 cj = j == 0 ? (c2){1.f, 0.f} : j == 1 ? (c2){0.92387953251128674f, -0.38268343236508977f}
                : j == 2 ? (c2){0.70710678118654752f, -0.70710678118654752f} : (c2){0.38268343236508977f, -0.92387953251128674f};
    const c2 tw = j == 0 ? tw0 : cmul(tw0, cj);
    const c2 E = add_c(z, zp) * half, Dz = sub_c(z, zp) * half;
    const c2 P = cmul(tw, Dz);
    c2 Xa = sub_i(E, P), Xb = add_i(E, P);
    Xb.y = -Xb.y;
    if (k == 0) { Xa = (c2){z.x + z.y, z.x - z.y}; Xb = (c2){v[4].x, -v[4].y}; }
    st[k * 9 + w] = Xa;
    st[(k == 0 ? 256 : 512 - k) * 9 + w] = Xb;
  }
  __syncthreads();
#pragma unroll
  for (int it = 0; it < 4; ++it) {
    const int idx = it * 512 + tid;
    const int k = idx >> 2, part = idx & 3;
    const c2 a = st[k * 9 + 2 * part], b = st[k * 9 + 2 * part + 1];
    st_nt(reinterpret_cast<f32v4*>(T + (size_t)k * S_ + s0) + part, (f32v4){a.x, a.y, b.x, b.y});
  }
}

#define KEEP(v) asm volatile("" :: "v"(v))
__device__ __forceinline__ uint32_t wt_at(const uint32_t* WT, int k1, int c) { return WT[(size_t)c * S_ + k1]; }
__global__ __launch_bounds__(256) void k2_colfft(const c2* __restrict__ T, c2* __restrict__ U, const uint32_t* __restrict__ WT, const char* __restrict__ pf) {
  __shared__ c2 ex[2304];
  const int t = threadIdx.x, col = blockIdx.x;
  c2 v[8];
#pragma unroll
  for (int a = 0; a < 8; ++a) v[a] = T[(size_t)col * S_ + t + 256 * a];
  uint32_t wv[8];
#pragma unroll
  for (int r = 0; r < 8; ++r) wv[r] = wt_at(WT, t + 256 * r, col);
  uint32_t pfv = 0;
  if (t < 112) pfv = *reinterpret_cast<const uint32_t*>(pf + ((size_t)col * 112 + t) * 128);
  c2* Tg = U + (size_t)(t >> 3) * TBS + col * 8 + (t & 7);
  const int h5 = t >> 5, l5 = t & 31;
  const int m3 = (t >> 2) & 7, d2 = t & 3;
  dft8<-1>(v);
  twiddle8(v, cexp2pi<-1>((float)t * (1.0f / 2048.0f)));
#pragma unroll
  for (int k0 = 0; k0 < 8; ++k0) ex[256 * k0 + t] = v[k0];
  __syncthreads();
#pragma unroll
  for (int b = 0; b < 8; ++b) v[b] = ex[256 * h5 + 32 * b + l5];
  __syncthreads();
  dft8<-1>(v);
  twiddle8(v, cexp2pi<-1>((float)l5 * (1.0f / 256.0f)));
#pragma unroll
  for (int k1 = 0; k1 < 8; ++k1) ex[288 * h5 + 36 * k1 + l5] = v[k1];
  WAVE_SYNC();
#pragma unroll
  for (int c = 0; c < 8; ++c) v[c] = ex[288 * h5 + 36 * m3 + 4 * c + d2];
  __syncthreads();
  dft8<-1>(v);
  twiddle8(v, cexp2pi<-1>((float)d2 * (1.0f / 32.0f)));
#pragma unroll
  for (int k2 = 0; k2 < 8; ++k2) ex[(4 * k2 + d2) * 66 + 8 * m3 + h5] = v[k2];
  __syncthreads();
  const int dd = t >> 6, l6 = t & 63;
#pragma unroll
  for (int d = 0; d < 4; ++d) { v[d] = ex[(4 * dd + d) * 66 + l6]; v[4 + d] = ex[(4 * (dd + 4) + d) * 66 + l6]; }
  __syncthreads();
  dft4<-1>(v[0], v[1], v[2], v[3]);
  dft4<-1>(v[4], v[5], v[6], v[7]);
  if (col == 0) {
#pragma unroll
    for (int k3 = 0; k3 < 4; ++k3) { ex[t + 512 * k3] = v[k3]; ex[t + 256 + 512 * k3] = v[4 + k3]; }
    __syncthreads();
  }
  if (col != 0) {
#pragma unroll
    for (int k3 = 0; k3 < 4; ++k3) {
      v[k3] = cmul(v[k3], unpk(wv[2 * k3]));
      v[4 + k3] = cmul(v[4 + k3], unpk(wv[2 * k3 + 1]));
    }
  } else {
#pragma unroll
    for (int r = 0; r < 8; ++r) {
      const int vi = (r & 1) * 4 + (r >> 1);
      const int k = t + 256 * r, kn = (2048 - k) & 2047;
      const c2 p = v[vi], pn = ex[kn];
      const c2 F0 = {0.5f * (p.x + pn.x), 0.5f * (p.y - pn.y)};
      const c2 F5 = {0.5f * (p.y + pn.y), 0.5f * (pn.x - p.x)};
      const c2 wa = unpk(wv[r]), wan = unpk(wt_at(WT, kn, 0));
      const c2 wb = unpk(wt_at(WT, k, 512)), wbn = unpk(wt_at(WT, kn, 512));
      const c2 W0 = {0.5f * (wa.x + wan.x), 0.5f * (wa.y - wan.y)};
      const c2 W5 = {0.5f * (wb.x + wbn.x), 0.5f * (wb.y - wbn.y)};
      const c2 H0 = cmul(F0, W0), H5 = cmul(F5, W5);
      v[vi] = add_i(H0, H5);
    }
  }
  if (col == 0) __syncthreads();
  dft4<1>(v[0], v[1], v[2], v[3]);
  dft4<1>(v[4], v[5], v[6], v[7]);
  {
    const c2 wa = cexp2pi<1>((float)dd * (1.0f / 32.0f)), wb = cexp2pi<1>((float)(dd + 4) * (1.0f / 32.0f));
    const c2 wa2 = cmul(wa, wa), wa3 = cmul(wa2, wa), wb2 = cmul(wb, wb), wb3 = cmul(wb2, wb);
    v[1] = cmul(v[1], wa); v[2] = cmul(v[2], wa2); v[3] = cmul(v[3], wa3);
    v[5] = cmul(v[5], wb); v[6] = cmul(v[6], wb2); v[7] = cmul(v[7], wb3);
  }
#pragma unroll
  for (int d = 0; d < 4; ++d) { ex[(4 * dd + d) * 66 + l6] = v[d]; ex[(4 * (dd + 4) + d) * 66 + l6] = v[4 + d]; }
  __syncthreads();
#pragma unroll
  for (int k2 = 0; k2 < 8; ++k2) v[k2] = ex[(4 * k2 + d2) * 66 + 8 * m3 + h5];
  __syncthreads();
  dft8<1>(v);
  twiddle8b(v, cexp2pi<1>((float)(m3 * d2) * (1.0f / 256.0f)), cexp2pi<1>((float)m3 * (1.0f / 64.0f)));
#pragma unroll
  for (int c = 0; c < 8; ++c) ex[288 * h5 + 36 * m3 + 4 * c + d2] = v[c];
  WAVE_SYNC();
#pragma unroll
  for (int k1 = 0; k1 < 8; ++k1) v[k1] = ex[288 * h5 + 36 * k1 + l5];
  __syncthreads();
  dft8<1>(v);
  twiddle8b(v, cexp2pi<1>((float)(h5 * l5) * (1.0f / 2048.0f)), cexp2pi<1>((float)h5 * (1.0f / 64.0f)));
#pragma unroll
  for (int b = 0; b < 8; ++b) ex[256 * h5 + 32 * b + l5] = v[b];
  __syncthreads();
#pragma unroll
  for (int k0 = 0; k0 < 8; ++k0) v[k0] = ex[256 * k0 + t];
  dft8<1>(v);
#pragma unroll
  for (int a = 0; a < 8; ++a) st_nt(Tg + (size_t)a * (32 * TBS), v[a]);
  KEEP(pfv);
}

__device__ __forceinline__ void k3_body(char* smem, const c2* __restrict__ T, const u16* __restrict__ x,
                                        u16* __restrict__ h, u16* __restrict__ a_out, const char* __restrict__ pf) {
  c2* ex_all = reinterpret_cast<c2*>(smem);
  c2* st = reinterpret_cast<c2*>(smem) + 8 * 576;
  const int tid = threadIdx.x, lane = tid & 63, w = tid >> 6;
  const int s0 = blockIdx.x * 8;
  const int row = s0 + w;
  float4 q4[4];
#pragma unroll
  for (int it = 0; it < 4; ++it) {
    const int idx = it * 512 + tid;
    q4[it] = reinterpret_cast<const float4*>(T + (size_t)blockIdx.x * TBS)[idx];
  }
  const uint32_t* xr = reinterpret_cast<const uint32_t*>(x + (size_t)row * D_);
  uint32_t xv[8];
#pragma unroll
  for (int a = 0; a < 8; ++a) xv[a] = xr[lane + 64 * a];
  uint32_t pfv = 0;
  if (tid < 224) pfv = *reinterpret_cast<const uint32_t*>(pf + ((size_t)blockIdx.x * 224 + tid) * 128);
#pragma unroll
  for (int it = 0; it < 4; ++it) {
    const int idx = it * 512 + tid;
    const int k = idx >> 2, part = idx & 3;
    st[k * 9 + 2 * part] = (c2){q4[it].x, q4[it].y};
    st[k * 9 + 2 * part + 1] = (c2){q4[it].z, q4[it].w};
  }
  __syncthreads();
  c2 v[8];
  const c2 tw0 = cexp2pi<1>((float)lane * (1.0f / 1024.0f));
#pragma unroll
  for (int j = 0; j < 8; ++j) {
    const int k = lane + 64 * j;
    const c2 g = st[k * 9 + w], gp = st[((512 - k) & 511) * 9 + w];
    const float cs[8] = {1.f, 0.92387953251128674f, 0.70710678118654752f, 0.38268343236508977f, 0.f, -0.38268343236508977f, -0.70710678118654752f, -0.92387953251128674f};
    const float sn[8] = {0.f, 0.38268343236508977f, 0.70710678118654752f, 0.92387953251128674f, 1.f, 0.92387953251128674f, 0.70710678118654752f, 0.38268343236508977f};
    const c2 tw = j == 0 ? tw0 : cmul(tw0, (c2){cs[j], sn[j]});
    c2 Z = add_i(add_c(g, gp), cmul(tw, sub_c(g, gp)));
    if (k == 0) Z = (c2){g.x + g.y, g.x - g.y};
    v[j] = Z;
  }
  c2* ex = ex_all + w * 576;
  fft512_wave<1>(v, ex, lane);
  const float scale = 1.0f / (2048.0f * 1024.0f);
  uint32_t* tr = reinterpret_cast<uint32_t*>(ex);
  const auto hrs = __builtin_amdgcn_make_buffer_rsrc(h, 0, S_ * D_ * 2, 0x00020000);
  const auto ars = __builtin_amdgcn_make_buffer_rsrc(a_out, 0, S_ * D_ * 2, 0x00020000);
  const uint32_t roff = (uint32_t)row * (D_ * 2) + lane * 16;
#pragma unroll
  for (int j = 0; j < 8; ++j) {
    const c2 hh = unpk(xv[j]) + rbf2(v[j] * scale);
    const uint32_t hu = cvt_pk(hh.x, hh.y);
    tr[lane + 64 * j] = hu;
    v[j] = unpk(hu);
  }
  WAVE_SYNC();
  {
    const u32v4 h0 = reinterpret_cast<const u32v4*>(tr)[lane], h1 = reinterpret_cast<const u32v4*>(tr)[64 + lane];
    __builtin_amdgcn_raw_buffer_store_b128(h0, hrs, roff, 0, 16);
    __builtin_amdgcn_raw_buffer_store_b128(h1, hrs, roff + 1024, 0, 16);
  }
  ln_bf16(v, xv);
#pragma unroll
  for (int j = 0; j < 8; ++j) tr[512 + lane + 64 * j] = xv[j];
  WAVE_SYNC();
  {
    const u32v4 a0 = reinterpret_cast<const u32v4*>(tr)[128 + lane], a1 = reinterpret_cast<const u32v4*>(tr)[192 + lane];
    __builtin_amdgcn_raw_buffer_store_b128(a0, ars, roff, 0, 16);
    __builtin_amdgcn_raw_buffer_store_b128(a1, ars, roff + 1024, 0, 16);
  }
  KEEP(pfv);
}

#define WAIT_V(n) asm volatile("s_waitcnt vmcnt(" #n ")" ::: "memory")
#define WAIT_L0() asm volatile("s_waitcnt lgkmcnt(0)" ::: "memory")
#define RAW_BAR() __builtin_amdgcn_s_barrier()

__device__ __forceinline__ uint32_t mish_pair(float c0, float c1, f32x2 b) {
  f32x2 x = unpk(cvt_pk(c0, c1)) + b;
  const f32x2 hh = unpk(cvt_pk(x[0], x[1]));
  f32x2 z = hh * 1.44269504088896340736f;
  f32x2 t = {__builtin_amdgcn_exp2f(__builtin_amdgcn_fmed3f(z[0], -200.0f, 43.0f)), __builtin_amdgcn_exp2f(__builtin_amdgcn_fmed3f(z[1], -200.0f, 43.0f))};
  const f32x2 n = t * (t + 2.0f);
  const f32x2 d = n + 2.0f;
  const f32x2 r = {__builtin_amdgcn_rcpf(d[0]), __builtin_amdgcn_rcpf(d[1])};
  const f32x2 o = (hh * n) * r;
  return cvt_pk(o[0], o[1]);
}

typedef __attribute__((ext_vector_type(4))) unsigned int u32x4;
typedef __attribute__((ext_vector_type(2))) unsigned int u32x2;
template <int OFF> __device__ __forceinline__ void lds_rd128(u32x4& d, uint32_t a) { asm volatile("ds_read_b128 %0, %1 offset:%2" : "=v"(d) : "v"(a), "i"(OFF)); }
template <int OFF> __device__ __forceinline__ void lds_rdtr(u32x2& d, uint32_t a) { asm volatile("ds_read_b64_tr_b16 %0, %1 offset:%2" : "=v"(d) : "v"(a), "i"(OFF)); }
#define SCHED0() __builtin_amdgcn_sched_barrier(0)
__device__ __forceinline__ void mfma16(f32x4& c, const u32x2& alo, const u32x2& ahi, const u32x4& b) {
  u32x4 a = {alo[0], alo[1], ahi[0], ahi[1]};
  asm volatile("v_mfma_f32_16x16x32_bf16 %0, %1, %2, %0" : "+v"(c) : "v"(a), "v"(b));
}
typedef __attribute__((address_space(1))) unsigned int gu32;
template <int N> struct IC { static constexpr int v = N; };

constexpr int G1_A_BYTES = 128 * 128, G1_B_BYTES = 64 * 448, G1_STAGE = G1_A_BYTES + G1_B_BYTES;
constexpr int G1_OT = 57344, G1_OTS = 464;
struct G1Frag { u32x4 a[4]; u32x2 b[7][2]; };
__device__ __forceinline__ void g1_body(char* smem, const u16* __restrict__ A, const u16* __restrict__ W, const u16* __restrict__ b1,
                                        u16* __restrict__ act, unsigned* __restrict__ flags) {
  const int tid = threadIdx.x, lane = tid & 63, w8 = __builtin_amdgcn_readfirstlane(tid >> 6), w = w8 & 3;
  const int wm = w >> 1, wn = w & 1;
  const int bid = blockIdx.x, xcd = bid & 7, idx = bid >> 3;
  const int tm = (xcd & 1) * 8 + (idx & 7), tn = (xcd >> 1) * 4 + (idx >> 3);
  const int m0 = tm * 128, n0 = tn * 224;
  uint32_t aoff[4], boff[7];
#pragma unroll
  for (int i = 0; i < 4; ++i) {
    const int r = w * 32 + i * 8 + (lane >> 3);
    aoff[i] = (uint32_t)(m0 + r) * 2048u + (uint32_t)(((lane & 7) ^ (r & 7)) << 4);
  }
#pragma unroll
  for (int i = 0; i < 7; ++i) {
    const int d = i * 64 + lane, r = d / 28, q = d - r * 28, k = w * 16 + r;
    const int c = (q >> 1) ^ ((k >> 3) & 1);
    boff[i] = (uint32_t)k * (uint32_t)(F_ * 2) + (uint32_t)(n0 * 2) + (uint32_t)(((c << 1) | (q & 1)) << 4);
  }
  const char* Ab = reinterpret_cast<const char*>(A);
  const char* Wb = reinterpret_cast<const char*>(W);
  auto issue1 = [&](int i, int kt, int buf) {
    if (i < 4) {
      __builtin_amdgcn_global_load_lds((const unsigned*)(Ab + kt * 128 + aoff[i]),
                                       (unsigned*)(smem + buf * G1_STAGE + w * 4096 + lane * 16 + i * 1024), 16, 0, 16);
    } else {
      __builtin_amdgcn_global_load_lds((const unsigned*)(Wb + (size_t)kt * (64 * F_ * 2) + boff[i - 4]),
                                       (unsigned*)(smem + buf * G1_STAGE + G1_A_BYTES + w * 7168 + lane * 16 + (i - 4) * 1024), 16, 0, 0);
    }
  };
  const auto ars = __builtin_amdgcn_make_buffer_rsrc(act, 0, S_ * F_ * 2, 0x00020000);
  auto g1_store = [&](auto RND) {
    constexpr int rnd = decltype(RND)::v, CPH = rnd == 0 ? 8 : 6, NIT = rnd == 0 ? 4 : 3;
    WAIT_L0();
    RAW_BAR();
#pragma unroll
    for (int i = 0; i < NIT; ++i) {
      const int c = tid + 512 * i, r = c / (2 * CPH), q0 = c - r * (2 * CPH);
      const int q = (q0 >= CPH ? 14 - CPH : 0) + q0 + rnd * 8;
      const u32v4 v = *reinterpret_cast<const u32v4*>(smem + G1_OT + r * G1_OTS + q * 16);
      __builtin_amdgcn_raw_buffer_store_b128(v, ars, (uint32_t)(m0 + r) * (uint32_t)(F_ * 2) + (uint32_t)(n0 * 2 + q * 16), 0, 16);
    }
  };
  constexpr int NK = 16;
  if (w8 >= 4) {
#pragma unroll
    for (int i = 4; i < 11; ++i) issue1(i, 0, 0);
#pragma unroll
    for (int i = 4; i < 11; ++i) issue1(i, 1, 1);
    if (lane == 0) {
      for (int spin = 0; spin < (1 << 22); ++spin) {
        if (__hip_atomic_load((gu32*)(flags + 8192 + tm * 16), __ATOMIC_RELAXED, __HIP_MEMORY_SCOPE_AGENT) == 16u) break;
        __builtin_amdgcn_s_sleep(1);
      }
    }
    asm volatile("" ::: "memory");
#pragma unroll
    for (int i = 0; i < 4; ++i) issue1(i, 0, 0);
#pragma unroll
    for (int i = 0; i < 4; ++i) issue1(i, 1, 1);
    WAIT_V(4); RAW_BAR();
#pragma unroll
    for (int i = 0; i < 11; ++i) issue1(i, 2, 2);
    int buf = 0;
    for (int kt = 0; kt < NK - 3; ++kt) {
      WAIT_V(11); RAW_BAR();
#pragma unroll
      for (int i = 0; i < 11; ++i) issue1(i, kt + 3, buf);
      buf = buf == 2 ? 0 : buf + 1;
    }
    WAIT_V(11); RAW_BAR();
    WAIT_V(0); RAW_BAR();
    uint2 bb[7];
#pragma unroll
    for (int ni = 0; ni < 7; ++ni) bb[ni] = *reinterpret_cast<const uint2*>(b1 + n0 + wn * 112 + ni * 16 + 4 * (lane >> 4));
    RAW_BAR(); RAW_BAR();
    const f32x4* xfer = reinterpret_cast<const f32x4*>(smem) + w * (14 * 64) + lane;
    char* ot = smem + G1_OT + (wm * 64 + (lane & 15)) * G1_OTS + (wn * 112 + 4 * (lane >> 4)) * 2;
#pragma unroll
    for (int ni = 0; ni < 7; ++ni) {
      const f32x2 b01 = unpk(bb[ni].x), b23 = unpk(bb[ni].y);
#pragma unroll
      for (int mi = 2; mi < 4; ++mi) {
        const f32x4 c = xfer[((mi - 2) * 7 + ni) * 64];
        *reinterpret_cast<uint2*>(ot + mi * 16 * G1_OTS + ni * 32) = make_uint2(mish_pair(c[0], c[1], b01), mish_pair(c[2], c[3], b23));
      }
      if (ni == 3) g1_store(IC<0>{});
    }
    g1_store(IC<1>{});
    return;
  }
  uint2 bb[7];
#pragma unroll
  for (int ni = 0; ni < 7; ++ni) bb[ni] = *reinterpret_cast<const uint2*>(b1 + n0 + wn * 112 + ni * 16 + 4 * (lane >> 4));
  f32x4 acc[4][7];
#pragma unroll
  for (int i = 0; i < 4; ++i)
#pragma unroll
    for (int j = 0; j < 7; ++j) acc[i][j] = (f32x4){0.f, 0.f, 0.f, 0.f};
  const int fr = lane & 15, fg = lane >> 4;
  const uint32_t sbase = (uint32_t)(size_t)(smem);
  const uint32_t ard0 = sbase + (wm * 64 + fr) * 128 + ((fg ^ (fr & 7)) << 4);
  const int q4 = (lane & 15) >> 2, p4 = lane & 3;
  const uint32_t brd = sbase + G1_A_BYTES + (8 * fg + q4) * 448 + 8 * p4 + wn * 224;
  const int f32_ = (fg & 1) * 32;
  const uint32_t brdE = brd + ((wn & 1) ? -f32_ : f32_), brdO = brd + ((wn & 1) ? f32_ : -f32_);
  G1Frag X, Y;
  auto rd = [&](G1Frag& Fr, auto I, auto KS, uint32_t so) {
    constexpr int i = decltype(I)::v, ks = decltype(KS)::v;
    if constexpr (i < 4) {
      lds_rd128<i * 2048>(Fr.a[i], (ard0 ^ (ks * 64)) + so);
    } else {
      constexpr int ni = (i - 4) >> 1, t = (i - 4) & 1;
      lds_rdtr<ks * 32 * 448 + ni * 32 + t * 4 * 448>(Fr.b[ni][t], ((ni & 1) ? brdO : brdE) + so);
    }
  };
  auto mma1 = [&](G1Frag& Fr, auto J) {
    constexpr int j = decltype(J)::v, mi = j / 7, ni = j % 7;
    mfma16(acc[mi][ni], Fr.b[ni][0], Fr.b[ni][1], Fr.a[mi]);
  };
#define G1_TIE(Fr) asm volatile("s_waitcnt lgkmcnt(0)" : "+v"(Fr.a[0]), "+v"(Fr.a[1]), "+v"(Fr.a[2]), "+v"(Fr.a[3]), \
    "+v"(Fr.b[0][0]), "+v"(Fr.b[0][1]), "+v"(Fr.b[1][0]), "+v"(Fr.b[1][1]), "+v"(Fr.b[2][0]), "+v"(Fr.b[2][1]), "+v"(Fr.b[3][0]), "+v"(Fr.b[3][1]), \
    "+v"(Fr.b[4][0]), "+v"(Fr.b[4][1]), "+v"(Fr.b[5][0]), "+v"(Fr.b[5][1]), "+v"(Fr.b[6][0]), "+v"(Fr.b[6][1]) :: "memory")
  auto block = [&](G1Frag& C, G1Frag& R, auto KS, uint32_t so, auto DO_RD, auto DO_IS, int kt_is, int buf_is) {
    auto one = [&](auto J) {
      constexpr int j = decltype(J)::v;
      mma1(C, J);
      if constexpr (decltype(DO_RD)::v && j < 18) rd(R, J, KS, so);
      if constexpr (decltype(DO_IS)::v && j >= 6 && j < 28 && ((j - 6) & 1) == 0) issue1((j - 6) >> 1, kt_is, buf_is);
      SCHED0();
    };
    one(IC<0>{}); one(IC<1>{}); one(IC<2>{}); one(IC<3>{}); one(IC<4>{}); one(IC<5>{}); one(IC<6>{});
    one(IC<7>{}); one(IC<8>{}); one(IC<9>{}); one(IC<10>{}); one(IC<11>{}); one(IC<12>{}); one(IC<13>{});
    one(IC<14>{}); one(IC<15>{}); one(IC<16>{}); one(IC<17>{}); one(IC<18>{}); one(IC<19>{}); one(IC<20>{});
    one(IC<21>{}); one(IC<22>{}); one(IC<23>{}); one(IC<24>{}); one(IC<25>{}); one(IC<26>{}); one(IC<27>{});
  };
  auto rdall = [&](G1Frag& R, uint32_t so) {
    rd(R, IC<0>{}, IC<0>{}, so); rd(R, IC<1>{}, IC<0>{}, so); rd(R, IC<2>{}, IC<0>{}, so); rd(R, IC<3>{}, IC<0>{}, so);
    rd(R, IC<4>{}, IC<0>{}, so); rd(R, IC<5>{}, IC<0>{}, so); rd(R, IC<6>{}, IC<0>{}, so); rd(R, IC<7>{}, IC<0>{}, so);
    rd(R, IC<8>{}, IC<0>{}, so); rd(R, IC<9>{}, IC<0>{}, so); rd(R, IC<10>{}, IC<0>{}, so); rd(R, IC<11>{}, IC<0>{}, so);
    rd(R, IC<12>{}, IC<0>{}, so); rd(R, IC<13>{}, IC<0>{}, so); rd(R, IC<14>{}, IC<0>{}, so); rd(R, IC<15>{}, IC<0>{}, so);
    rd(R, IC<16>{}, IC<0>{}, so); rd(R, IC<17>{}, IC<0>{}, so);
  };
  __builtin_amdgcn_s_setprio(2);
  RAW_BAR();
  rdall(X, 0);
  int buf = 0;
  for (int kt = 0; kt < NK - 3; ++kt) {
    const int nbuf = buf == 2 ? 0 : buf + 1;
    G1_TIE(X); SCHED0();
    block(X, Y, IC<1>{}, buf * G1_STAGE, IC<1>{}, IC<0>{}, 0, 0);
    G1_TIE(Y); RAW_BAR(); SCHED0();
    block(Y, X, IC<0>{}, nbuf * G1_STAGE, IC<1>{}, IC<0>{}, 0, 0);
    buf = nbuf;
  }
  G1_TIE(X); SCHED0();
  block(X, Y, IC<1>{}, 1 * G1_STAGE, IC<1>{}, IC<0>{}, 0, 0);
  G1_TIE(Y); RAW_BAR(); SCHED0();
  block(Y, X, IC<0>{}, 2 * G1_STAGE, IC<1>{}, IC<0>{}, 0, 0);
  G1_TIE(X); SCHED0();
  block(X, Y, IC<1>{}, 2 * G1_STAGE, IC<1>{}, IC<0>{}, 0, 0);
  G1_TIE(Y); RAW_BAR(); SCHED0();
  block(Y, X, IC<0>{}, 0 * G1_STAGE, IC<1>{}, IC<0>{}, 0, 0);
  G1_TIE(X); SCHED0();
  block(X, Y, IC<1>{}, 0 * G1_STAGE, IC<1>{}, IC<0>{}, 0, 0);
  G1_TIE(Y); SCHED0();
  block(Y, X, IC<0>{}, 0, IC<0>{}, IC<0>{}, 0, 0);
  asm volatile("s_nop 15\n\ts_nop 15" : "+v"(acc[0][0]), "+v"(acc[0][1]), "+v"(acc[0][2]), "+v"(acc[0][3]), "+v"(acc[0][4]), "+v"(acc[0][5]), "+v"(acc[0][6]), "+v"(acc[1][0]), "+v"(acc[1][1]), "+v"(acc[1][2]), "+v"(acc[1][3]), "+v"(acc[1][4]), "+v"(acc[1][5]), "+v"(acc[1][6]), "+v"(acc[2][0]), "+v"(acc[2][1]), "+v"(acc[2][2]), "+v"(acc[2][3]), "+v"(acc[2][4]), "+v"(acc[2][5]), "+v"(acc[2][6]), "+v"(acc[3][0]), "+v"(acc[3][1]), "+v"(acc[3][2]), "+v"(acc[3][3]), "+v"(acc[3][4]), "+v"(acc[3][5]), "+v"(acc[3][6]) :: "memory");
  SCHED0();
  __builtin_amdgcn_s_setprio(0);
  RAW_BAR();
  {
    f32x4* xfer = reinterpret_cast<f32x4*>(smem) + w * (14 * 64) + lane;
#pragma unroll
    for (int mi = 2; mi < 4; ++mi)
#pragma unroll
      for (int ni = 0; ni < 7; ++ni) xfer[((mi - 2) * 7 + ni) * 64] = acc[mi][ni];
  }
  WAIT_L0();
  RAW_BAR();
  char* ot = smem + G1_OT + (wm * 64 + fr) * G1_OTS + (wn * 112 + 4 * fg) * 2;
#pragma unroll
  for (int ni = 0; ni < 7; ++ni) {
    const f32x2 b01 = unpk(bb[ni].x), b23 = unpk(bb[ni].y);
#pragma unroll
    for (int mi = 0; mi < 2; ++mi) {
      const f32x4 c = acc[mi][ni];
      *reinterpret_cast<uint2*>(ot + mi * 16 * G1_OTS + ni * 32) = make_uint2(mish_pair(c[0], c[1], b01), mish_pair(c[2], c[3], b23));
    }
    if (ni == 3) g1_store(IC<0>{});
  }
  g1_store(IC<1>{});
}

constexpr int G2_A_BYTES = 128 * 128, G2_B_BYTES = 64 * 256, G2_STAGE = G2_A_BYTES + G2_B_BYTES;
struct G2Frag { u32x4 a[4]; u32x2 b[4][2]; };
__device__ __forceinline__ void g2_body(char* smem, const u16* __restrict__ A, const u16* __restrict__ W, const u16* __restrict__ h,
                                        u16* __restrict__ out, float* __restrict__ part, unsigned* __restrict__ flags) {
  const int tid = threadIdx.x, lane = tid & 63, w8 = __builtin_amdgcn_readfirstlane(tid >> 6), w = w8 & 3;
  const int wm = w >> 1, wn = w & 1;
  const int bid = blockIdx.x, xcd = bid & 7, idx = bid >> 3;
  const int kh = xcd & 1, tm = ((xcd >> 1) & 1) * 8 + (idx & 7), tn = (xcd >> 2) * 4 + (idx >> 3);
  const int m0 = tm * 128, n0 = tn * 128, tile = tm * 8 + tn;
  auto g2_store = [&]() {
    WAIT_L0();
    RAW_BAR();
#pragma unroll
    for (int i = 0; i < 2; ++i) {
      const int c = tid + 512 * i, r = c >> 4, q = c & 15;
      const int row = (r >> 5) * 64 + kh * 32 + (r & 31);
      const uint4 v = *reinterpret_cast<const uint4*>(smem + r * 272 + q * 16);
      *reinterpret_cast<uint4*>(reinterpret_cast<char*>(out) + (size_t)(m0 + row) * (D_ * 2) + n0 * 2 + q * 16) = v;
    }
  };
  constexpr int NK = 28;
  if (w8 >= 4) {
    uint32_t aoff[4], boff[4];
#pragma unroll
    for (int i = 0; i < 4; ++i) {
      const int r = w * 32 + i * 8 + (lane >> 3);
      aoff[i] = (uint32_t)(m0 + r) * (uint32_t)(F_ * 2) + (uint32_t)(((lane & 7) ^ (r & 7)) << 4);
    }
#pragma unroll
    for (int i = 0; i < 4; ++i) {
      const int k = w * 16 + i * 4 + (lane >> 4), q = lane & 15;
      const int c = (q >> 1) ^ ((k & 3) | (((k >> 3) & 1) << 2));
      boff[i] = (uint32_t)k * (uint32_t)(D_ * 2) + (uint32_t)(n0 * 2) + (uint32_t)(((c << 1) | (q & 1)) << 4);
    }
    const char* Ab = reinterpret_cast<const char*>(A) + kh * (1792 * 2);
    const char* Wb = reinterpret_cast<const char*>(W) + (size_t)kh * (1792 * D_ * 2);
    auto issueA = [&](int kt, int buf) {
#pragma unroll
      for (int i = 0; i < 4; ++i)
        __builtin_amdgcn_global_load_lds((const unsigned*)(Ab + kt * 128 + aoff[i]),
                                         (unsigned*)(smem + buf * G2_STAGE + w * 4096 + lane * 16 + i * 1024), 16, 0, 16);
    };
    auto issueB = [&](int kt, int buf) {
#pragma unroll
      for (int i = 0; i < 4; ++i)
        __builtin_amdgcn_global_load_lds((const unsigned*)(Wb + (size_t)kt * (64 * D_ * 2) + boff[i]),
                                         (unsigned*)(smem + buf * G2_STAGE + G2_A_BYTES + w * 4096 + lane * 16 + i * 1024), 16, 0, 0);
    };
    auto issue = [&](int kt, int buf) { issueA(kt, buf); issueB(kt, buf); };
    issueB(0, 0); issueB(1, 1);
    if (lane == 0) {
      for (int spin = 0; spin < (1 << 22); ++spin) {
        if (__hip_atomic_load((gu32*)(flags + 4096 + (tm * 2 + kh) * 16), __ATOMIC_RELAXED, __HIP_MEMORY_SCOPE_AGENT) == 8u) break;
        __builtin_amdgcn_s_sleep(1);
      }
    }
    asm volatile("" ::: "memory");
    issueA(0, 0); issueA(1, 1);
    WAIT_V(4); RAW_BAR();
    issue(2, 2); issue(3, 3);
    for (int kt = 0; kt < NK - 4; ++kt) {
      WAIT_V(16); RAW_BAR();
      issue(kt + 4, kt & 3);
    }
    WAIT_V(16); RAW_BAR();
    WAIT_V(8); RAW_BAR();
    WAIT_V(0); RAW_BAR();
    RAW_BAR();
    RAW_BAR();
    g2_store();
    return;
  }
  const int fr = lane & 15, fg = lane >> 4;
  u32x2 hv[2][4];
  f32x4 acc[4][4];
#pragma unroll
  for (int i = 0; i < 4; ++i)
#pragma unroll
    for (int j = 0; j < 4; ++j) acc[i][j] = (f32x4){0.f, 0.f, 0.f, 0.f};
  const uint32_t sbase = (uint32_t)(size_t)(smem);
  const uint32_t ard0 = sbase + (wm * 64 + fr) * 128 + ((fg ^ (fr & 7)) << 4);
  const int q4 = (lane & 15) >> 2, p4 = lane & 3;
  const int bsw = q4 | ((fg & 1) << 2);
  const uint32_t brd = sbase + G2_A_BYTES + (8 * fg + q4) * 256 + 8 * p4;
  uint32_t brdn[4];
#pragma unroll
  for (int ni = 0; ni < 4; ++ni) brdn[ni] = brd + (((wn * 4 + ni) ^ bsw) * 32);
  G2Frag X, Y;
  auto rd = [&](G2Frag& Fr, auto I, auto KS, uint32_t so) {
    constexpr int i = decltype(I)::v, ks = decltype(KS)::v;
    if constexpr (i < 4) {
      lds_rd128<i * 2048>(Fr.a[i], (ard0 ^ (ks * 64)) + so);
    } else {
      constexpr int ni = (i - 4) >> 1, t = (i - 4) & 1;
      lds_rdtr<ks * 32 * 256 + t * 4 * 256>(Fr.b[ni][t], brdn[ni] + so);
    }
  };
  auto mma1 = [&](G2Frag& Fr, auto J) {
    constexpr int j = decltype(J)::v, mi = j / 4, ni = j % 4;
    mfma16(acc[mi][ni], Fr.b[ni][0], Fr.b[ni][1], Fr.a[mi]);
  };
#define G2_TIE(Fr) asm volatile("s_waitcnt lgkmcnt(0)" : "+v"(Fr.a[0]), "+v"(Fr.a[1]), "+v"(Fr.a[2]), "+v"(Fr.a[3]), \
    "+v"(Fr.b[0][0]), "+v"(Fr.b[0][1]), "+v"(Fr.b[1][0]), "+v"(Fr.b[1][1]), "+v"(Fr.b[2][0]), "+v"(Fr.b[2][1]), "+v"(Fr.b[3][0]), "+v"(Fr.b[3][1]) :: "memory")
  auto block = [&](G2Frag& C, G2Frag& R, auto KS, uint32_t so, auto DO_RD) {
    auto one = [&](auto J) {
      constexpr int j = decltype(J)::v;
      mma1(C, J);
      if constexpr (decltype(DO_RD)::v && j < 12) rd(R, J, KS, so);
      SCHED0();
    };
    one(IC<0>{}); one(IC<1>{}); one(IC<2>{}); one(IC<3>{}); one(IC<4>{}); one(IC<5>{}); one(IC<6>{}); one(IC<7>{});
    one(IC<8>{}); one(IC<9>{}); one(IC<10>{}); one(IC<11>{}); one(IC<12>{}); one(IC<13>{}); one(IC<14>{}); one(IC<15>{});
  };
  __builtin_amdgcn_s_setprio(2);
  RAW_BAR();
  {
    const auto hrs = __builtin_amdgcn_make_buffer_rsrc(const_cast<u16*>(h), 0, S_ * D_ * 2, 0x00020000);
#pragma unroll
    for (int i = 0; i < 2; ++i)
#pragma unroll
      for (int ni = 0; ni < 4; ++ni)
        hv[i][ni] = __builtin_amdgcn_raw_buffer_load_b64(hrs, (uint32_t)((m0 + wm * 64 + (2 * kh + i) * 16 + fr) * D_ + n0 + wn * 64 + ni * 16 + 4 * fg) * 2u, 0, 16);
  }
  rd(X, IC<0>{}, IC<0>{}, 0); rd(X, IC<1>{}, IC<0>{}, 0); rd(X, IC<2>{}, IC<0>{}, 0); rd(X, IC<3>{}, IC<0>{}, 0);
  rd(X, IC<4>{}, IC<0>{}, 0); rd(X, IC<5>{}, IC<0>{}, 0); rd(X, IC<6>{}, IC<0>{}, 0); rd(X, IC<7>{}, IC<0>{}, 0);
  rd(X, IC<8>{}, IC<0>{}, 0); rd(X, IC<9>{}, IC<0>{}, 0); rd(X, IC<10>{}, IC<0>{}, 0); rd(X, IC<11>{}, IC<0>{}, 0);
  for (int kt = 0; kt < NK - 1; ++kt) {
    G2_TIE(X); SCHED0();
    block(X, Y, IC<1>{}, (kt & 3) * G2_STAGE, IC<1>{});
    G2_TIE(Y); RAW_BAR(); SCHED0();
    block(Y, X, IC<0>{}, ((kt + 1) & 3) * G2_STAGE, IC<1>{});
  }
  G2_TIE(X); SCHED0();
  block(X, Y, IC<1>{}, ((NK - 1) & 3) * G2_STAGE, IC<1>{});
  G2_TIE(Y); SCHED0();
  block(Y, X, IC<0>{}, 0, IC<0>{});
  asm volatile("s_nop 15\n\ts_nop 15" : "+v"(acc[0][0]), "+v"(acc[0][1]), "+v"(acc[0][2]), "+v"(acc[0][3]), "+v"(acc[1][0]), "+v"(acc[1][1]), "+v"(acc[1][2]), "+v"(acc[1][3]), "+v"(acc[2][0]), "+v"(acc[2][1]), "+v"(acc[2][2]), "+v"(acc[2][3]), "+v"(acc[3][0]), "+v"(acc[3][1]), "+v"(acc[3][2]), "+v"(acc[3][3]) :: "memory");
  SCHED0();
  const auto rs = __builtin_amdgcn_make_buffer_rsrc(part, 0, 256 * 32768, 0x00020000);
  const uint32_t myoff = (uint32_t)(tile * 2 + kh) * 32768u + (uint32_t)w * 8192u + (uint32_t)lane * 16u;
  const uint32_t proff = (uint32_t)(tile * 2 + (kh ^ 1)) * 32768u + (uint32_t)w * 8192u + (uint32_t)lane * 16u;
  auto send = [&](const f32x4 (&r0)[4], const f32x4 (&r1)[4]) {
#pragma unroll
    for (int ni = 0; ni < 4; ++ni) {
      __builtin_amdgcn_raw_buffer_store_b128(__builtin_bit_cast(u32x4, r0[ni]), rs, myoff + ni * 1024, 0, 16);
      __builtin_amdgcn_raw_buffer_store_b128(__builtin_bit_cast(u32x4, r1[ni]), rs, myoff + 4096 + ni * 1024, 0, 16);
    }
  };
  if (kh == 0) send(acc[2], acc[3]); else send(acc[0], acc[1]);
  WAIT_V(0);
  RAW_BAR();
  if (w == 0) {
    if (lane == 0) {
      __hip_atomic_store((gu32*)(flags + (tile * 2 + kh) * 16), 1u, __ATOMIC_RELAXED, __HIP_MEMORY_SCOPE_AGENT);
      for (int spin = 0; spin < (1 << 22); ++spin) {
        if (__hip_atomic_load((gu32*)(flags + (tile * 2 + (kh ^ 1)) * 16), __ATOMIC_RELAXED, __HIP_MEMORY_SCOPE_AGENT) == 1u) break;
        __builtin_amdgcn_s_sleep(1);
      }
    }
  }
  asm volatile("" ::: "memory");
  RAW_BAR();
  asm volatile("" ::: "memory");
  auto fin = [&](auto I, const f32x4 (&own)[4]) {
    constexpr int i = decltype(I)::v;
    char* ot = smem + (wm * 32 + i * 16 + fr) * 272 + wn * 128 + 8 * fg;
    f32x4 pr[4];
#pragma unroll
    for (int ni = 0; ni < 4; ++ni) pr[ni] = __builtin_bit_cast(f32x4, __builtin_amdgcn_raw_buffer_load_b128(rs, proff + i * 4096 + ni * 1024, 0, 16));
#pragma unroll
    for (int ni = 0; ni < 4; ++ni) {
      const f32x4 c = kh == 0 ? own[ni] + pr[ni] : pr[ni] + own[ni];
      const f32x2 r01 = unpk(hv[i][ni][0]) + unpk(cvt_pk(c[0], c[1])), r23 = unpk(hv[i][ni][1]) + unpk(cvt_pk(c[2], c[3]));
      *reinterpret_cast<uint2*>(ot + ni * 32) = make_uint2(cvt_pk(r01[0], r01[1]), cvt_pk(r23[0], r23[1]));
    }
  };
  if (kh == 0) { fin(IC<0>{}, acc[0]); fin(IC<1>{}, acc[1]); }
  else { fin(IC<0>{}, acc[2]); fin(IC<1>{}, acc[3]); }
  g2_store();
}

__global__ __launch_bounds__(512, 2) void k3g_fused(const c2* __restrict__ U, const u16* __restrict__ x, u16* __restrict__ A,
                                                     const u16* __restrict__ W1, const u16* __restrict__ b1,
                                                     u16* __restrict__ act, const u16* __restrict__ W2, u16* __restrict__ h,
                                                     u16* __restrict__ out, float* __restrict__ part, unsigned* __restrict__ flags) {
  __shared__ __attribute__((aligned(16))) char smem[3 * G1_STAGE];
  uint32_t cpf = 0;
  if ((blockIdx.x >> 3) < 2 && threadIdx.x >= 288) {
    const char* pc = reinterpret_cast<const char*>(__builtin_amdgcn_s_getpc());
    cpf = *reinterpret_cast<const uint32_t*>(pc + 4096 + (threadIdx.x - 288) * 128);
  }
  k3_body(smem, U, x, h, A, reinterpret_cast<const char*>(W2));
  KEEP(cpf);
  WAIT_V(0);
  WAIT_L0();
  RAW_BAR();
  if (threadIdx.x == 0)
    __hip_atomic_fetch_add((gu32*)(flags + 8192 + (blockIdx.x >> 4) * 16), 1u, __ATOMIC_RELAXED, __HIP_MEMORY_SCOPE_AGENT);
  g1_body(smem, A, W1, b1, act, flags);
  WAIT_V(0);
  RAW_BAR();
  if (threadIdx.x == 0) {
    const int xcd = blockIdx.x & 7, idx = blockIdx.x >> 3;
    const int tm = (xcd & 1) * 8 + (idx & 7), tn = (xcd >> 1) * 4 + (idx >> 3);
    __hip_atomic_fetch_add((gu32*)(flags + 4096 + (tm * 2 + (tn >> 3)) * 16), 1u, __ATOMIC_RELAXED, __HIP_MEMORY_SCOPE_AGENT);
  }
  g2_body(smem, act, W2, h, out, part, flags);
}

extern "C" void kernel_launch(void* const* d_in, const int* in_sizes, int n_in,
                              void* d_out, int out_size, void* d_ws, size_t ws_size,
                              hipStream_t stream) {
  const u16* x = (const u16*)d_in[0];
  const u16* wr = (const u16*)d_in[1];
  const u16* wi = (const u16*)d_in[2];
  const u16* w1 = (const u16*)d_in[4];
  const u16* b1 = (const u16*)d_in[5];
  const u16* w2 = (const u16*)d_in[6];
  u16* out = (u16*)d_out;
  char* ws = (char*)d_ws;
  c2* T = (c2*)ws;
  uint32_t* WT = (uint32_t*)(ws + (9u << 20));
  u16* h = (u16*)(ws + (16u << 20));
  u16* a = (u16*)(ws + (20u << 20));
  u16* act = (u16*)(ws + (24u << 20));
  float* part = (float*)(ws + (40u << 20));
  unsigned* flags = (unsigned*)(ws + (48u << 20));
  k1_ln_rowfft<<<256, 512, 0, stream>>>(x, wr, wi, T, WT, flags);
  c2* U = (c2*)(ws + (50u << 20));
  k2_colfft<<<512, 256, 0, stream>>>(T, U, WT, (const char*)w1);
  k3g_fused<<<256, 512, 0, stream>>>(U, x, a, w1, b1, act, w2, h, out, part, flags);
}
```
